```python
import torch

def _softmax(x, axis=-1):
    return torch.softmax(x, dim=axis)
def _gelu(x):
    return torch.nn.functional.gelu(x, approximate="tanh")
def _silu(x):
    return torch.nn.functional.silu(x)
def _sigmoid(x):
    return torch.sigmoid(x)
def _layernorm(x, axis=-1, eps=1e-5):
    m = x.mean(dim=axis, keepdim=True)
    v = x.var(dim=axis, keepdim=True, unbiased=False)
    return (x - m) / torch.sqrt(v + eps)
def _scatter_add(tgt, idx, src, axis=0):
    return tgt.clone().index_add_(axis, idx, src)
def _segment_sum(src, seg, n):
    out = torch.zeros((n,) + src.shape[1:], dtype=src.dtype)
    return out.index_add_(0, seg, src)
def _pad(x, pw):
    flat = tuple(v for pair in reversed(pw) for v in pair)
    return torch.nn.functional.pad(x, flat)

kernel_name = 'sequential_preLN__sliding_local_dilated__dense_activation_silu__prompt'

def setup_inputs():

    _w = (lambda a: (lambda t: t.to(torch.bfloat16) if t.is_floating_point() else t)(torch.as_tensor(a)))
    def _A_setup():
        B, S, D = 1, 2048, 576
        return {
            "x": _w((lambda *s: torch.randn(*s, dtype=torch.bfloat16) * 0.05)(B, S, D)),
            "wqkv": _w((lambda *s: torch.randn(*s, dtype=torch.bfloat16) * 0.05)(D, 3 * D)),
            "wo": _w((lambda *s: torch.randn(*s, dtype=torch.bfloat16) * 0.05)(D, D)),
        }
    _A_in = _A_setup()

    def _B_setup():
        B, S, D, F = 1, 2048, 576, 4608
        return {
            "x": _w((lambda *s: torch.randn(*s, dtype=torch.bfloat16) * 0.05)(B, S, D)),
            "w1": _w((lambda *s: torch.randn(*s, dtype=torch.bfloat16) * 0.8)(D, F) * (D ** -0.5)),
            "w2": _w((lambda *s: torch.randn(*s, dtype=torch.bfloat16) * 0.8)(F, D) * (F ** -0.5)),
        }
    _B_in = _B_setup()

    return {
        "A_x": _A_in["x"],
        "A_wqkv": _A_in["wqkv"],
        "A_wo": _A_in["wo"],
        "B_x": _B_in["x"],
        "B_w1": _B_in["w1"],
        "B_w2": _B_in["w2"],
    }

def reference(A_x, A_wqkv, A_wo, B_x, B_w1, B_w2):
    def _A_ref(x, wqkv, wo):
        B, S, D = x.shape
        H = 32; Dh = D // H
        W, dil = 8, 2
        qkv = (lambda x, *d: x.permute(*d))((x @ wqkv).reshape(B, S, 3, H, Dh), 2, 0, 3, 1, 4)
        q, k, v = qkv[0], qkv[1], qkv[2]
        scale = Dh ** -0.5
        pos = torch.arange(S)
        off = torch.arange(W) * dil
        idx = pos[:, None] - off[None, :]
        mask = idx >= 0
        idx_c = torch.where(mask, idx, 0).reshape(-1)
        kg = k[:, :, idx_c, :].reshape(B, H, S, W, Dh)
        vg = v[:, :, idx_c, :].reshape(B, H, S, W, Dh)
        logits = torch.einsum("bhsd,bhswd->bhsw", q, kg) * scale
        logits = torch.where(mask[None, None, :, :], logits, -1e9)
        attn = _softmax(logits, axis=-1)
        o = torch.einsum("bhsw,bhswd->bhsd", attn, vg)
        return (lambda x, *d: x.permute(*d))(o, 0, 2, 1, 3).reshape(B, S, D) @ wo

    def _B_ref(x, w1, w2):
        h = torch.einsum("bsd,df->bsf", x, w1)
        h = _silu(h)
        out = torch.einsum("bsf,fd->bsd", h, w2)
        return out

    x = A_x
    h = x + _A_ref(_layernorm(x, axis=-1), A_wqkv, A_wo)
    h2 = h + _B_ref(_layernorm(h, axis=-1), B_w1, B_w2)
    return h2
```

```cpp
#include <hip/hip_runtime.h>
#include <hip/hip_bf16.h>
#include <stdint.h>
typedef unsigned short u16;
typedef short bf16x8 __attribute__((ext_vector_type(8)));
typedef float f32x4 __attribute__((ext_vector_type(4)));
constexpr int S = 2048, D = 576, F = 4608, H = 32, DH = 18, W = 8, DIL = 2;
__device__ __forceinline__ float bf2f(u16 b) { return __builtin_bit_cast(float, (unsigned)b << 16); }
__device__ __forceinline__ u16 f2bf(float f) { __hip_bfloat16 h = __float2bfloat16(f); return __builtin_bit_cast(u16, h); }

__global__ void ln_kernel(const u16* x, u16* y) {
  int row = blockIdx.x, lane = threadIdx.x;
  float v[9]; float s = 0;
  for (int i = 0; i < 9; ++i) { v[i] = bf2f(x[row * D + lane + 64 * i]); s += v[i]; }
  for (int o = 32; o; o >>= 1) s += __shfl_xor(s, o);
  float m = s / D, q = 0;
  for (int i = 0; i < 9; ++i) { float d = v[i] - m; q += d * d; }
  for (int o = 32; o; o >>= 1) q += __shfl_xor(q, o);
  float r = 1.0f / sqrtf(q / D + 1e-5f);
  for (int i = 0; i < 9; ++i) y[row * D + lane + 64 * i] = f2bf((v[i] - m) * r);
}
template <int EPI>
__global__ __launch_bounds__(256) void gemm_kernel(const u16* A, const u16* B, u16* C, const u16* R, int M, int N, int K) {
  __shared__ u16 As[64][40];
  __shared__ u16 Bs[64][40];
  int tid = threadIdx.x, lane = tid & 63, w = tid >> 6, wr = w >> 1, wc = w & 1;
  int bm = blockIdx.y * 64, bn = blockIdx.x * 64;
  f32x4 acc[2][2] = {};
  for (int k0 = 0; k0 < K; k0 += 32) {
    { int r = tid >> 2, c = (tid & 3) * 8; *(bf16x8*)&As[r][c] = *(const bf16x8*)&A[(size_t)(bm + r) * K + k0 + c]; }
    { int k = tid >> 3, c = (tid & 7) * 8; bf16x8 v = *(const bf16x8*)&B[(size_t)(k0 + k) * N + bn + c]; for (int j = 0; j < 8; ++j) Bs[c + j][k] = v[j]; }
    __syncthreads();
    bf16x8 a[2], b[2];
    for (int m = 0; m < 2; ++m) a[m] = *(bf16x8*)&As[wr * 32 + m * 16 + (lane & 15)][(lane >> 4) * 8];
    for (int n = 0; n < 2; ++n) b[n] = *(bf16x8*)&Bs[wc * 32 + n * 16 + (lane & 15)][(lane >> 4) * 8];
    for (int m = 0; m < 2; ++m) for (int n = 0; n < 2; ++n) acc[m][n] = __builtin_amdgcn_mfma_f32_16x16x32_bf16(a[m], b[n], acc[m][n], 0, 0, 0);
    __syncthreads();
  }
  for (int m = 0; m < 2; ++m) for (int n = 0; n < 2; ++n) for (int j = 0; j < 4; ++j) {
    int r = bm + wr * 32 + m * 16 + (lane >> 4) * 4 + j, c = bn + wc * 32 + n * 16 + (lane & 15);
    float v = acc[m][n][j];
    if (EPI == 1) v = v / (1.0f + __expf(-v));
    if (EPI == 2) v += bf2f(R[(size_t)r * N + c]);
    C[(size_t)r * N + c] = f2bf(v);
  }
}
__global__ void attn_kernel(const u16* qkv, u16* o) {
  int idx = blockIdx.x * blockDim.x + threadIdx.x;
  int s = idx / H, h = idx % H;
  float q[DH];
  for (int d = 0; d < DH; ++d) q[d] = bf2f(qkv[(size_t)s * 3 * D + h * DH + d]);
  float l[W]; float mx = -3e38f;
  const float scale = 0.23570226039551584f;
  for (int w = 0; w < W; ++w) {
    int p = s - w * DIL; float a = -1e9f;
    if (p >= 0) { a = 0; for (int d = 0; d < DH; ++d) a += q[d] * bf2f(qkv[(size_t)p * 3 * D + D + h * DH + d]); a *= scale; }
    l[w] = a; mx = fmaxf(mx, a);
  }
  float sum = 0;
  for (int w = 0; w < W; ++w) { l[w] = __expf(l[w] - mx); sum += l[w]; }
  float acc[DH] = {};
  for (int w = 0; w < W; ++w) { int p = s - w * DIL; if (p < 0) p = 0; float a = l[w] / sum; for (int d = 0; d < DH; ++d) acc[d] += a * bf2f(qkv[(size_t)p * 3 * D + 2 * D + h * DH + d]); }
  for (int d = 0; d < DH; ++d) o[(size_t)s * D + h * DH + d] = f2bf(acc[d]);
}
extern "C" void kernel_launch(void* const* d_in, const int* in_sizes, int n_in, void* d_out, int out_size, void* d_ws, size_t ws_size, hipStream_t stream) {
  const u16* x = (const u16*)d_in[0]; const u16* wqkv = (const u16*)d_in[1]; const u16* wo = (const u16*)d_in[2];
  const u16* w1 = (const u16*)d_in[4]; const u16* w2 = (const u16*)d_in[5];
  u16* ws = (u16*)d_ws;
  u16* xn = ws; u16* qkv = xn + S * D; u16* o = qkv + S * 3 * D; u16* h = o + S * D; u16* hn = h + S * D; u16* hh = hn + S * D;
  ln_kernel<<<S, 64, 0, stream>>>(x, xn);
  gemm_kernel<0><<<dim3(3 * D / 64, S / 64), 256, 0, stream>>>(xn, wqkv, qkv, nullptr, S, 3 * D, D);
  attn_kernel<<<S * H / 256, 256, 0, stream>>>(qkv, o);
  gemm_kernel<2><<<dim3(D / 64, S / 64), 256, 0, stream>>>(o, wo, h, x, S, D, D);
  ln_kernel<<<S, 64, 0, stream>>>(h, hn);
  gemm_kernel<1><<<dim3(F / 64, S / 64), 256, 0, stream>>>(hn, w1, hh, nullptr, S, F, D);
  gemm_kernel<2><<<dim3(D / 64, S / 64), 256, 0, stream>>>(hh, w2, (u16*)d_out, h, S, D, F);
}
```

```cpp
#include <hip/hip_runtime.h>
struct Args { const void *x, *wqkv, *wo, *w1, *w2; void *out, *ws; };

__global__ __launch_bounds__(256, 1) void block_kernel(Args a) {
#if defined(__HIP_DEVICE_COMPILE__)
  int tid = threadIdx.x;
  asm volatile(
"s_load_dwordx2 s[20:21], %1, 8\n"
"s_load_dwordx2 s[16:17], %1, 0\n"
"s_load_dwordx2 s[28:29], %1, 24\n"
"s_load_dwordx2 s[36:37], %1, 48\n"
"s_load_dwordx2 s[24:25], %1, 16\n"
"s_load_dwordx2 s[32:33], %1, 32\n"
"s_load_dwordx2 s[40:41], %1, 40\n"
"v_lshrrev_b32 v1, 6, v0\n"
"v_and_b32 v0, 63, v0\n"
"v_readfirstlane_b32 s6, v1\n"
"s_and_b32 s9, %2, 0\n"
"s_bfe_u32 s8, %2, 0x30003\n"
"s_add_u32 s5, s9, s8\n"
"s_and_b32 s7, s5, 1\n"
"s_and_b32 s8, %2, 7\n"
"s_lshr_b32 s55, %2, 6\n"
"s_lshl_b32 s4, s8, 2\n"
"s_add_u32 s4, s4, s55\n"
"s_lshl_b32 s94, s55, 3\n"
"s_add_u32 s94, s94, s8\n"
"s_mov_b32 s22, 1990656\n"
"s_mov_b32 s23, 0x20000\n"
"s_mov_b32 s18, 2359296\n"
"s_mov_b32 s19, 0x20000\n"
"s_mov_b32 s30, 5308416\n"
"s_mov_b32 s31, 0x20000\n"
"s_mov_b32 s26, 663552\n"
"s_mov_b32 s27, 0x20000\n"
"s_mov_b32 s38, 39665664\n"
"s_mov_b32 s39, 0x20000\n"
"v_lshlrev_b32 v1, 4, v0\n"
"v_lshrrev_b32 v12, 3, v0\n"
"v_and_b32 v13, 7, v0\n"
"v_lshrrev_b32 v14, 4, v0\n"
"v_mul_u32_u24 v32, 57, v0\n"
"v_lshrrev_b32 v32, 9, v32\n"
"v_cmp_lt_u32 vcc, 26, v0\n"
"v_mul_u32_u24 v32, 1008, v32\n"
"v_add_u32 v32, v32, v1\n"
"v_add_u32 v15, 10368, v32\n"
"v_cndmask_b32 v32, v32, v15, vcc\n"
"v_and_b32 v164, 6, v12\n"
"v_xor_b32 v164, v164, v13\n"
"v_mul_u32_u24 v43, 9216, v12\n"
"v_lshl_add_u32 v43, v164, 4, v43\n"
"v_mul_u32_u24 v150, 1152, v12\n"
"v_lshl_add_u32 v3, v164, 4, v150\n"
"v_lshrrev_b32 v164, 1, v0\n"
"v_mul_u32_u24 v35, 1120, v164\n"
"v_add_u32 v35, v35, v1\n"
"s_lshl_b32 s99, s94, 2\n"
"s_add_u32 s99, s99, s6\n"
"s_lshl_b32 s96, s6, 12\n"
"v_lshrrev_b32 v148, 5, v0\n"
"v_mul_u32_u24 v148, 640, v148\n"
"v_add_u32 v148, v148, v1\n"
"v_mov_b32 v149, 2130706432\n"
"s_lshl_b32 s8, s4, 3\n"
"s_add_u32 s8, s8, s5\n"
"s_lshl_b32 s87, s8, 2\n"
"s_add_u32 s87, s87, s6\n"
"s_mul_i32 s87, s87, 2304\n"
"v_mov_b32_dpp v149, v148 quad_perm:[0,1,2,3] row_mask:0x5 bank_mask:0x3\n"
"s_mul_i32 s10, s99, 27648\n"
"s_mul_i32 s11, s5, 144\n"
"s_add_u32 s10, s10, s11\n"
"s_mul_i32 s11, s6, 3456\n"
"s_waitcnt lgkmcnt(0)\n"
"s_and_b32 s21, s21, 0xffff\n"
"s_and_b32 s17, s17, 0xffff\n"
"s_and_b32 s29, s29, 0xffff\n"
"s_and_b32 s25, s25, 0xffff\n"
"s_and_b32 s37, s37, 0xffff\n"
"s_cmp_ge_u32 s99, 72\n"
"s_cbranch_scc1 LA0_block_kernel_a\n"
"s_mov_b32 exec_hi, 0x3fffff\n"
"s_add_u32 m0, s11, 0\n"
"s_add_u32 s13, s10, 0\n"
"buffer_load_dwordx4 v32, s[20:23], s13 offen lds\n"
"s_add_u32 m0, s11, 864\n"
"s_add_u32 s13, s10, 3456\n"
"buffer_load_dwordx4 v32, s[20:23], s13 offen lds\n"
"s_add_u32 m0, s11, 1728\n"
"s_add_u32 s13, s10, 6912\n"
"buffer_load_dwordx4 v32, s[20:23], s13 offen lds\n"
"s_add_u32 m0, s11, 2592\n"
"s_add_u32 s13, s10, 10368\n"
"buffer_load_dwordx4 v32, s[20:23], s13 offen lds\n"
"s_mov_b32 exec_hi, -1\n"
"s_branch LA0_block_kernel_e\n"
"LA0_block_kernel_a:\n"
"s_cmp_ge_u32 s99, 106\n"
"s_cbranch_scc1 LA0_block_kernel_b\n"
"s_branch LA0_block_kernel_e\n"
"LA0_block_kernel_b:\n"
"s_cmp_ge_u32 s99, 124\n"
"s_cbranch_scc1 LA0_block_kernel_e\n"
"LA0_block_kernel_e:\n"
"buffer_load_dwordx2 v[6:7], off, s[36:39], 0 sc1\n"
"s_lshr_b32 s12, %2, 3\n"
"s_lshl_b32 s12, s12, 2\n"
"s_add_u32 s12, s12, s6\n"
"s_getpc_b64 s[10:11]\n"
"LPC_block_kernel:\n"
"s_lshl_b32 s12, s12, 9\n"
"s_cmp_lt_u32 s12, LEND_block_kernel-LPC_block_kernel-512\n"
"s_cselect_b32 s12, s12, 0\n"
"v_lshl_add_u32 v10, v14, 7, s12\n"
"global_load_dword v4, v10, s[10:11]\n"
"buffer_load_dwordx4 v[48:51], v148, s[16:19], s87 offen offset:0\n"
"buffer_load_dwordx4 v[52:55], v148, s[16:19], s87 offen offset:512\n"
"buffer_load_dwordx4 v[56:59], v149, s[16:19], s87 offen offset:1024\n"
"v_and_b32 v15, 15, v0\n"
"v_lshlrev_b32 v2, 3, v0\n"
"v_lshlrev_b32 v8, 2, v0\n"
"v_mov_b32 v172, -1\n"
"v_mov_b32 v173, -1\n"
"v_mov_b32 v174, -1\n"
"v_mov_b32 v175, -1\n"
"s_lshl_b32 s88, s8, 7\n"
"s_lshl_b32 s10, s6, 3\n"
"s_add_u32 s88, s88, s10\n"
"s_add_u32 s88, s88, 98304\n"
"s_lshl_b32 s8, s5, 2\n"
"s_add_u32 s8, s8, s6\n"
"s_lshl_b32 s9, s4, 13\n"
"s_add_u32 s9, s9, 2228224\n"
"s_lshl_b32 s10, s8, 8\n"
"s_add_u32 s57, s9, s10\n"
"s_lshl_b32 s9, s4, 16\n"
"s_add_u32 s9, s9, 131072\n"
"s_lshl_b32 s10, s8, 9\n"
"s_add_u32 s56, s9, s10\n"
"v_lshrrev_b32 v165, 5, v0\n"
"v_mul_u32_u24 v164, 15872, v165\n"
"v_add_u32 v42, v164, v1\n"
"s_lshl_b32 s9, s4, 2\n"
"s_add_u32 s9, s9, s6\n"
"s_mul_i32 s63, s9, 18432\n"
"s_mul_i32 s9, s9, 19456\n"
"s_add_u32 s9, s9, 2490368\n"
"s_lshl_b32 s11, s5, 11\n"
"s_add_u32 s64, s9, s11\n"
"s_add_u32 s69, s9, 16384\n"
"s_lshr_b32 s10, s5, 1\n"
"s_lshl_b32 s10, s10, 9\n"
"s_lshr_b32 s11, s6, 1\n"
"s_cmp_eq_u32 s11, s7\n"
"s_cselect_b32 s10, s10, 2130706432\n"
"v_add_u32 v176, s10, v2\n"
"s_lshl_b32 s10, s5, 7\n"
"v_lshl_add_u32 v164, v165, 4, v15\n"
"v_lshl_add_u32 v177, v164, 2, s10\n"
"s_lshl_b32 s20, s5, 5\n"
"s_add_u32 s20, s20, s94\n"
"s_lshl_b32 s20, s20, 7\n"
"s_mul_i32 s72, s8, 73728\n"
"s_add_u32 s72, s72, 37240832\n"
"s_and_b32 s10, s6, 1\n"
"s_xor_b32 s10, s10, s94\n"
"s_lshl_b32 s10, s10, 12\n"
"s_add_u32 s89, s72, s10\n"
"v_and_b32 v164, 3, v0\n"
"v_lshlrev_b32 v164, 3, v164\n"
"v_lshrrev_b32 v165, 2, v0\n"
"v_lshl_add_u32 v164, v165, 7, v164\n"
"v_and_b32 v165, 3, v12\n"
"v_lshl_add_u32 v164, v165, 5, v164\n"
"v_add_u32 v168, s96, v164\n"
"v_xor_b32 v169, 32, v168\n"
"v_xor_b32 v170, 64, v168\n"
"v_xor_b32 v171, 96, v168\n"
"v_and_b32 v165, 3, v0\n"
"s_mul_i32 s10, s6, 36\n"
"s_and_b32 s11, s6, 1\n"
"s_lshl_b32 s11, s11, 2\n"
"s_add_u32 s10, s10, s11\n"
"s_lshl_b32 s11, s11, 3\n"
"v_xor_b32 v47, s11, v1\n"
"s_lshr_b32 s11, s6, 1\n"
"s_mul_i32 s9, s11, 72\n"
"s_add_u32 s9, s9, 32\n"
"v_lshrrev_b32 v164, 2, v0\n"
"v_mul_u32_u24 v164, 864, v164\n"
"v_lshl_add_u32 v33, v165, 3, v164\n"
"v_add_u32 v33, s10, v33\n"
"v_add_u32 v34, 1, v165\n"
"v_lshrrev_b32 v34, 1, v34\n"
"v_mul_u32_u24 v34, 144, v34\n"
"v_add3_u32 v34, v34, v164, s9\n"
"s_mov_b32 s45, 0xffff0000\n"
"v_mov_b32 v45, 0x3f803f80\n"
"v_and_b32 v164, 1, v0\n"
"v_cmp_eq_u32 s[48:49], 1, v164\n"
"v_lshlrev_b32 v164, 5, v164\n"
"v_lshrrev_b32 v153, 5, v0\n"
"v_lshl_add_u32 v153, v153, 2, v164\n"
"v_xor_b32 v164, v14, v13\n"
"v_lshl_add_u32 v150, v164, 4, v150\n"
"v_xor_b32 v151, 64, v150\n"
"s_lshl_b32 s10, s11, 6\n"
"v_xor_b32 v152, s10, v150\n"
"v_add_u32 v151, 9216, v151\n"
"s_lshl_b32 s83, s6, 11\n"
"s_add_u32 s83, s83, 73728\n"
"s_lshl_b32 s85, s11, 10\n"
"s_cselect_b32 s86, -1, 0xffffff00\n"
"s_add_u32 s85, s85, 71680\n"
"s_lshl_b32 s10, s4, 3\n"
"s_add_u32 s10, s10, s11\n"
"s_sub_u32 s10, s10, 2\n"
"s_cselect_b32 s11, 2130706432, 0\n"
"s_cselect_b32 s10, 0, s10\n"
"s_mul_i32 s84, s10, 9216\n"
"v_add_u32 v152, s11, v152\n"
"s_mul_i32 s9, s55, 0\n"
"s_and_b32 s10, s5, 7\n"
"s_mul_i32 s10, s10, 1\n"
"s_add_u32 s9, s9, s10\n"
"s_mul_i32 s10, s9, 7282\n"
"s_lshr_b32 s10, s10, 16\n"
"s_mul_i32 s10, s10, 9\n"
"s_sub_u32 s9, s9, s10\n"
"s_lshl_b32 s74, s9, 7\n"
"s_add_u32 s75, s74, 128\n"
"s_cmpk_eq_u32 s75, 1152\n"
"s_cselect_b32 s75, 0, s75\n"
"s_add_u32 s76, s75, 128\n"
"s_cmpk_eq_u32 s76, 1152\n"
"s_cselect_b32 s76, 0, s76\n"
"s_add_u32 s77, s76, 128\n"
"s_cmpk_eq_u32 s77, 1152\n"
"s_cselect_b32 s77, 0, s77\n"
"s_add_u32 s78, s77, 128\n"
"s_cmpk_eq_u32 s78, 1152\n"
"s_cselect_b32 s78, 0, s78\n"
"s_add_u32 s79, s78, 128\n"
"s_cmpk_eq_u32 s79, 1152\n"
"s_cselect_b32 s79, 0, s79\n"
"s_add_u32 s80, s79, 128\n"
"s_cmpk_eq_u32 s80, 1152\n"
"s_cselect_b32 s80, 0, s80\n"
"s_add_u32 s81, s80, 128\n"
"s_cmpk_eq_u32 s81, 1152\n"
"s_cselect_b32 s81, 0, s81\n"
"s_add_u32 s82, s81, 128\n"
"s_cmpk_eq_u32 s82, 1152\n"
"s_cselect_b32 s82, 0, s82\n"
"v_lshrrev_b32 v164, 5, v0\n"
"v_mul_u32_u24 v164, 40832, v164\n"
"s_mul_i32 s10, s6, 10240\n"
"s_add_u32 s10, s10, 71680\n"
"v_add3_u32 v164, v164, v8, s10\n"
"v_and_b32 v165, 124, v8\n"
"v_add_u32 v165, 153600, v165\n"
"ds_write_b32 v164, v45\n"
"ds_write_b32 v165, v45\n"
"v_lshrrev_b32 v164, 1, v15\n"
"v_xor_b32 v164, v164, v14\n"
"v_lshlrev_b32 v164, 4, v164\n"
"v_lshl_add_u32 v164, v15, 7, v164\n"
"v_add_u32 v28, 71680, v164\n"
"s_and_b32 s10, s6, 1\n"
"s_lshl_b32 s10, s10, 6\n"
"v_xor_b32 v28, s10, v28\n"
"v_xor_b32 v29, 64, v28\n"
"s_mul_i32 s10, s6, 5\n"
"s_lshl_b32 s11, s5, 12\n"
"s_add_u32 s11, s11, 39600128\n"
"v_min_u32 v164, 4, v12\n"
"v_add_u32 v164, s10, v164\n"
"v_min_u32 v164, 17, v164\n"
"v_lshl_add_u32 v44, v164, 7, s11\n"
"s_lshl_b32 s9, %2, 2\n"
"s_add_u32 s9, s9, s6\n"
"s_mul_i32 s9, s9, 768\n"
"v_min_u32 v46, 5, v12\n"
"v_lshl_add_u32 v46, v46, 7, s9\n"
"s_waitcnt vmcnt(4) lgkmcnt(0)\n"
"s_barrier\n"
"s_cmp_ge_u32 s99, 72\n"
"s_cbranch_scc1 LA1_block_kernel_a\n"
"ds_read_b64_tr_b16 a[0:1], v33 offset:0\n"
"ds_read_b64_tr_b16 a[2:3], v33 offset:432\n"
"ds_read_b64_tr_b16 a[4:5], v33 offset:144\n"
"ds_read_b64_tr_b16 a[6:7], v33 offset:576\n"
"ds_read_b64_tr_b16 a[8:9], v33 offset:288\n"
"ds_read_b64_tr_b16 a[10:11], v33 offset:720\n"
"ds_read_b64_tr_b16 a[12:13], v34 offset:0\n"
"ds_read_b64_tr_b16 a[14:15], v34 offset:432\n"
"s_waitcnt lgkmcnt(6)\n"
"buffer_store_dwordx4 a[0:3], v1, s[36:39], s89 offen offset:0 sc1\n"
"s_waitcnt lgkmcnt(4)\n"
"buffer_store_dwordx4 a[4:7], v1, s[36:39], s89 offen offset:1024 sc1\n"
"s_waitcnt lgkmcnt(2)\n"
"buffer_store_dwordx4 a[8:11], v1, s[36:39], s89 offen offset:2048 sc1\n"
"s_xor_b32 s13, s94, s6\n"
"s_bitcmp1_b32 s13, 0\n"
"s_cbranch_scc1 LQE_block_kernel\n"
"s_waitcnt lgkmcnt(0)\n"
"buffer_store_dwordx4 a[12:15], v47, s[36:39], s89 offen offset:3072 sc1\n"
"LQE_block_kernel:\n"
"s_branch LA1_block_kernel_e\n"
"LA1_block_kernel_a:\n"
"s_cmp_ge_u32 s99, 106\n"
"s_cbranch_scc1 LA1_block_kernel_b\n"
"s_sub_u32 s12, s99, 72\n"
"s_mul_i32 s9, s12, 3641\n"
"s_lshr_b32 s9, s9, 16\n"
"s_mul_i32 s10, s9, 18\n"
"s_sub_u32 s10, s12, s10\n"
"s_mul_i32 s92, s10, 294912\n"
"s_lshl_b32 s11, s9, 7\n"
"s_add_u32 s92, s92, s11\n"
"s_mul_i32 s11, s5, 1152\n"
"s_add_u32 s92, s92, s11\n"
"s_mul_i32 s11, s5, 9\n"
"s_add_u32 s11, s11, s9\n"
"s_mul_i32 s93, s11, 73728\n"
"s_lshl_b32 s11, s10, 10\n"
"s_add_u32 s93, s93, s11\n"
"s_add_u32 s93, s93, 26476544\n"
"s_add_u32 m0, s96, 0\n"
"s_add_u32 s13, s92, 0\n"
"buffer_load_dwordx4 v43, s[28:31], s13 offen lds\n"
"s_add_u32 m0, s96, 1024\n"
"s_add_u32 s13, s92, 73728\n"
"buffer_load_dwordx4 v43, s[28:31], s13 offen lds\n"
"s_add_u32 m0, s96, 2048\n"
"s_add_u32 s13, s92, 147456\n"
"buffer_load_dwordx4 v43, s[28:31], s13 offen lds\n"
"s_add_u32 m0, s96, 3072\n"
"s_add_u32 s13, s92, 221184\n"
"buffer_load_dwordx4 v43, s[28:31], s13 offen lds\n"
"s_branch LA1_block_kernel_e\n"
"LA1_block_kernel_b:\n"
"s_cmp_ge_u32 s99, 124\n"
"s_cbranch_scc1 LA1_block_kernel_e\n"
"s_sub_u32 s9, s99, 106\n"
"s_mul_i32 s92, s9, 36864\n"
"s_lshr_b32 s10, s5, 1\n"
"s_lshl_b32 s10, s10, 5\n"
"s_add_u32 s92, s92, s10\n"
"buffer_load_dwordx4 a[0:3], v35, s[24:27], s92 offen offset:1024\n"
"s_mul_i32 s10, s5, 18\n"
"s_add_u32 s9, s9, s10\n"
"s_lshl_b32 s9, s9, 10\n"
"s_add_u32 s93, s9, 37093376\n"
"LA1_block_kernel_e:\n"
"v_add_u32 v6, 1, v6\n"
"s_bitcmp1_b32 s6, 0\n"
"s_cselect_b32 s13, s75, s74\n"
"s_cselect_b32 m0, 10240, 0\n"
"s_add_u32 m0, m0, s85\n"
"s_add_u32 s13, s13, s84\n"
"s_addk_i32 s85, 20480\n"
"s_mov_b32 exec_lo, s86\n"
"buffer_load_dwordx4 v152, s[16:19], s13 offen lds\n"
"s_mov_b32 exec_lo, -1\n"
"s_add_u32 m0, s83, 0\n"
"s_add_u32 s13, s63, s74\n"
"buffer_load_dwordx4 v150, s[16:19], s13 offen lds\n"
"s_add_u32 m0, s83, 1024\n"
"s_add_u32 s13, s63, s74\n"
"buffer_load_dwordx4 v151, s[16:19], s13 offen lds\n"
"s_add_u32 m0, s83, 10240\n"
"s_add_u32 s13, s63, s75\n"
"buffer_load_dwordx4 v150, s[16:19], s13 offen lds\n"
"s_add_u32 m0, s83, 11264\n"
"s_add_u32 s13, s63, s75\n"
"buffer_load_dwordx4 v151, s[16:19], s13 offen lds\n"
"s_bitcmp1_b32 s6, 0\n"
"s_cselect_b32 s13, s77, s76\n"
"s_cselect_b32 m0, 10240, 0\n"
"s_add_u32 m0, m0, s85\n"
"s_add_u32 s13, s13, s84\n"
"s_addk_i32 s85, 20480\n"
"s_mov_b32 exec_lo, s86\n"
"buffer_load_dwordx4 v152, s[16:19], s13 offen lds\n"
"s_mov_b32 exec_lo, -1\n"
"s_add_u32 m0, s83, 20480\n"
"s_add_u32 s13, s63, s76\n"
"buffer_load_dwordx4 v150, s[16:19], s13 offen lds\n"
"s_add_u32 m0, s83, 21504\n"
"s_add_u32 s13, s63, s76\n"
"buffer_load_dwordx4 v151, s[16:19], s13 offen lds\n"
"s_add_u32 m0, s83, 30720\n"
"s_add_u32 s13, s63, s77\n"
"buffer_load_dwordx4 v150, s[16:19], s13 offen lds\n"
"s_add_u32 m0, s83, 31744\n"
"s_add_u32 s13, s63, s77\n"
"buffer_load_dwordx4 v151, s[16:19], s13 offen lds\n"
"s_waitcnt vmcnt(10)\n"
"s_barrier\n"
"s_cmp_lg_u32 s6, 0\n"
"s_cbranch_scc1 LFA0_block_kernel\n"
"s_add_u32 s13, s20, 39600128\n"
"s_mov_b32 exec_lo, 0xffff\n"
"s_mov_b32 exec_hi, 0\n"
"buffer_store_dwordx2 v[6:7], v2, s[36:39], s13 offen sc1\n"
"s_mov_b64 exec, -1\n"
"LFA0_block_kernel:\n"
"s_waitcnt vmcnt(10)\n"
"s_cmp_ge_u32 s99, 72\n"
"s_cbranch_scc1 LA2_block_kernel_a\n"
"s_branch LA2_block_kernel_e\n"
"LA2_block_kernel_a:\n"
"s_cmp_ge_u32 s99, 106\n"
"s_cbranch_scc1 LA2_block_kernel_b\n"
"ds_read_b64_tr_b16 a[0:1], v168 offset:0\n"
"ds_read_b64_tr_b16 a[2:3], v168 offset:2048\n"
"ds_read_b64_tr_b16 a[4:5], v169 offset:0\n"
"ds_read_b64_tr_b16 a[6:7], v169 offset:2048\n"
"ds_read_b64_tr_b16 a[8:9], v170 offset:0\n"
"ds_read_b64_tr_b16 a[10:11], v170 offset:2048\n"
"ds_read_b64_tr_b16 a[12:13], v171 offset:0\n"
"ds_read_b64_tr_b16 a[14:15], v171 offset:2048\n"
"s_waitcnt lgkmcnt(6)\n"
"s_add_u32 s13, s93, 0\n"
"buffer_store_dwordx4 a[0:3], v1, s[36:39], s13 offen sc1\n"
"s_waitcnt lgkmcnt(4)\n"
"s_add_u32 s13, s93, 18432\n"
"buffer_store_dwordx4 a[4:7], v1, s[36:39], s13 offen sc1\n"
"s_waitcnt lgkmcnt(2)\n"
"s_add_u32 s13, s93, 36864\n"
"buffer_store_dwordx4 a[8:11], v1, s[36:39], s13 offen sc1\n"
"s_waitcnt lgkmcnt(0)\n"
"s_add_u32 s13, s93, 55296\n"
"buffer_store_dwordx4 a[12:15], v1, s[36:39], s13 offen sc1\n"
"s_branch LA2_block_kernel_e\n"
"LA2_block_kernel_b:\n"
"s_cmp_ge_u32 s99, 124\n"
"s_cbranch_scc1 LA2_block_kernel_e\n"
"buffer_store_dwordx4 a[0:3], v1, s[36:39], s93 offen sc1\n"
"LA2_block_kernel_e:\n"
"buffer_load_dword v4, v46, s[24:27], 0 offen\n"
"buffer_store_dword v172, v8, s[36:39], s57 offen sc1\n"
"buffer_store_dword v172, v177, s[36:39], s69 offen offset:2048 sc1\n"
"s_add_u32 s13, s56, 0\n"
"buffer_store_dwordx4 v[172:175], v42, s[36:39], s13 offen sc1\n"
"s_add_u32 s13, s56, 32768\n"
"buffer_store_dwordx4 v[172:175], v42, s[36:39], s13 offen sc1\n"
"s_waitcnt vmcnt(15)\n"
"v_mov_b32 v156, 0\n"
"v_mov_b32 v157, 0\n"
"v_dot2c_f32_bf16 v156, v48, v45\n"
"v_dot2c_f32_bf16 v157, v48, v48\n"
"v_dot2c_f32_bf16 v156, v49, v45\n"
"v_dot2c_f32_bf16 v157, v49, v49\n"
"v_dot2c_f32_bf16 v156, v50, v45\n"
"v_dot2c_f32_bf16 v157, v50, v50\n"
"v_dot2c_f32_bf16 v156, v51, v45\n"
"v_dot2c_f32_bf16 v157, v51, v51\n"
"v_dot2c_f32_bf16 v156, v52, v45\n"
"v_dot2c_f32_bf16 v157, v52, v52\n"
"v_dot2c_f32_bf16 v156, v53, v45\n"
"v_dot2c_f32_bf16 v157, v53, v53\n"
"v_dot2c_f32_bf16 v156, v54, v45\n"
"v_dot2c_f32_bf16 v157, v54, v54\n"
"v_dot2c_f32_bf16 v156, v55, v45\n"
"v_dot2c_f32_bf16 v157, v55, v55\n"
"v_dot2c_f32_bf16 v156, v56, v45\n"
"v_dot2c_f32_bf16 v157, v56, v56\n"
"v_dot2c_f32_bf16 v156, v57, v45\n"
"v_dot2c_f32_bf16 v157, v57, v57\n"
"v_dot2c_f32_bf16 v156, v58, v45\n"
"v_dot2c_f32_bf16 v157, v58, v58\n"
"v_dot2c_f32_bf16 v156, v59, v45\n"
"v_dot2c_f32_bf16 v157, v59, v59\n"
"s_nop 1\n"
"v_add_f32_dpp v156, v156, v156 quad_perm:[1,0,3,2] row_mask:0xf bank_mask:0xf\n"
"v_add_f32_dpp v157, v157, v157 quad_perm:[1,0,3,2] row_mask:0xf bank_mask:0xf\n"
"s_nop 0\n"
"v_add_f32_dpp v156, v156, v156 quad_perm:[2,3,0,1] row_mask:0xf bank_mask:0xf\n"
"v_add_f32_dpp v157, v157, v157 quad_perm:[2,3,0,1] row_mask:0xf bank_mask:0xf\n"
"s_nop 0\n"
"v_add_f32_dpp v156, v156, v156 row_half_mirror row_mask:0xf bank_mask:0xf\n"
"v_add_f32_dpp v157, v157, v157 row_half_mirror row_mask:0xf bank_mask:0xf\n"
"s_nop 0\n"
"v_add_f32_dpp v156, v156, v156 row_mirror row_mask:0xf bank_mask:0xf\n"
"v_add_f32_dpp v157, v157, v157 row_mirror row_mask:0xf bank_mask:0xf\n"
"v_mov_b32 v158, v156\n"
"v_mov_b32 v159, v157\n"
"s_nop 0\n"
"v_permlane16_swap_b32 v156, v158\n"
"v_permlane16_swap_b32 v157, v159\n"
"v_add_f32 v156, v156, v158\n"
"v_add_f32 v157, v157, v159\n"
"v_mul_f32 v160, 0x3ae38e39, v156\n"
"v_mul_f32 v161, 0x3ae38e39, v157\n"
"v_fma_f32 v161, -v160, v160, v161\n"
"v_max_f32 v161, 0, v161\n"
"v_add_f32 v161, 0x3727c5ac, v161\n"
"v_rsq_f32 v162, v161\n"
"s_nop 0\n"
"v_cndmask_b32_e64 v162, -v160, v162, s[48:49]\n"
"s_mov_b32 exec_lo, 3\n"
"s_mov_b32 exec_hi, 3\n"
"buffer_store_dword v162, v153, s[36:39], s88 offen sc1\n"
"s_mov_b64 exec, -1\n"
"s_bitcmp1_b32 s6, 0\n"
"s_cselect_b32 s13, s79, s78\n"
"s_cselect_b32 m0, 10240, 0\n"
"s_add_u32 m0, m0, s85\n"
"s_add_u32 s13, s13, s84\n"
"s_addk_i32 s85, 20480\n"
"s_mov_b32 exec_lo, s86\n"
"buffer_load_dwordx4 v152, s[16:19], s13 offen lds\n"
"s_mov_b32 exec_lo, -1\n"
"s_add_u32 m0, s83, 40960\n"
"s_add_u32 s13, s63, s78\n"
"buffer_load_dwordx4 v150, s[16:19], s13 offen lds\n"
"s_add_u32 m0, s83, 41984\n"
"s_add_u32 s13, s63, s78\n"
"buffer_load_dwordx4 v151, s[16:19], s13 offen lds\n"
"s_add_u32 m0, s83, 51200\n"
"s_add_u32 s13, s63, s79\n"
"buffer_load_dwordx4 v150, s[16:19], s13 offen lds\n"
"s_add_u32 m0, s83, 52224\n"
"s_add_u32 s13, s63, s79\n"
"buffer_load_dwordx4 v151, s[16:19], s13 offen lds\n"
"s_bitcmp1_b32 s6, 0\n"
"s_cselect_b32 s13, s81, s80\n"
"s_cselect_b32 m0, 10240, 0\n"
"s_add_u32 m0, m0, s85\n"
"s_add_u32 s13, s13, s84\n"
"s_addk_i32 s85, 20480\n"
"s_mov_b32 exec_lo, s86\n"
"buffer_load_dwordx4 v152, s[16:19], s13 offen lds\n"
"s_mov_b32 exec_lo, -1\n"
"s_add_u32 m0, s83, 61440\n"
"s_add_u32 s13, s63, s80\n"
"buffer_load_dwordx4 v150, s[16:19], s13 offen lds\n"
"s_add_u32 m0, s83, 62464\n"
"s_add_u32 s13, s63, s80\n"
"buffer_load_dwordx4 v151, s[16:19], s13 offen lds\n"
"s_add_u32 m0, s83, 71680\n"
"s_add_u32 s13, s63, s81\n"
"buffer_load_dwordx4 v150, s[16:19], s13 offen lds\n"
"s_add_u32 m0, s83, 72704\n"
"s_add_u32 s13, s63, s81\n"
"buffer_load_dwordx4 v151, s[16:19], s13 offen lds\n"
"s_bitcmp1_b32 s6, 0\n"
"s_cselect_b32 s13, s82, s82\n"
"s_cselect_b32 m0, 0, 0\n"
"s_add_u32 m0, m0, s85\n"
"s_add_u32 s13, s13, s84\n"
"s_addk_i32 s85, 20480\n"
"s_mov_b32 exec_lo, s86\n"
"buffer_load_dwordx4 v152, s[16:19], s13 offen lds\n"
"s_mov_b32 exec_lo, -1\n"
"s_add_u32 m0, s83, 81920\n"
"s_add_u32 s13, s63, s82\n"
"buffer_load_dwordx4 v150, s[16:19], s13 offen lds\n"
"s_add_u32 m0, s83, 82944\n"
"s_add_u32 s13, s63, s82\n"
"buffer_load_dwordx4 v151, s[16:19], s13 offen lds\n"
"buffer_load_dwordx2 v[10:11], v44, s[36:39], 0 offen sc1\n"
"s_waitcnt vmcnt(0)\n"
"v_cmp_ne_u64 vcc, v[10:11], v[6:7]\n"
"s_nop 0\n"
"s_cbranch_vccnz LFA_block_kernel_slow\n"
"LFA_block_kernel_back:\n"
"s_barrier\n"
"s_lshl_b32 s44, s74, 6\n"
"s_add_u32 s44, s44, s72\n"
"buffer_load_dwordx4 a[80:83], v1, s[36:39], s44 offen offset:0 sc1\n"
"buffer_load_dwordx4 a[84:87], v1, s[36:39], s44 offen offset:1024 sc1\n"
"buffer_load_dwordx4 a[88:91], v1, s[36:39], s44 offen offset:2048 sc1\n"
"buffer_load_dwordx4 a[92:95], v1, s[36:39], s44 offen offset:3072 sc1\n"
"s_addk_i32 s44, 4096\n"
"buffer_load_dwordx4 a[96:99], v1, s[36:39], s44 offen offset:0 sc1\n"
"buffer_load_dwordx4 a[100:103], v1, s[36:39], s44 offen offset:1024 sc1\n"
"buffer_load_dwordx4 a[104:107], v1, s[36:39], s44 offen offset:2048 sc1\n"
"ds_read_b128 a[0:3], v28 offset:0\n"
"ds_read_b128 a[20:23], v29 offset:0\n"
"ds_read_b128 a[4:7], v28 offset:2048\n"
"ds_read_b128 a[24:27], v29 offset:2048\n"
"ds_read_b128 a[8:11], v28 offset:4096\n"
"ds_read_b128 a[28:31], v29 offset:4096\n"
"ds_read_b128 a[12:15], v28 offset:6144\n"
"ds_read_b128 a[32:35], v29 offset:6144\n"
"ds_read_b128 a[16:19], v28 offset:8192\n"
"ds_read_b128 a[36:39], v29 offset:8192\n"
"s_lshl_b32 s44, s75, 6\n"
"s_add_u32 s44, s44, s72\n"
"buffer_load_dwordx4 a[112:115], v1, s[36:39], s44 offen offset:0 sc1\n"
"buffer_load_dwordx4 a[116:119], v1, s[36:39], s44 offen offset:1024 sc1\n"
"buffer_load_dwordx4 a[120:123], v1, s[36:39], s44 offen offset:2048 sc1\n"
"buffer_load_dwordx4 a[124:127], v1, s[36:39], s44 offen offset:3072 sc1\n"
"s_addk_i32 s44, 4096\n"
"buffer_load_dwordx4 a[128:131], v1, s[36:39], s44 offen offset:0 sc1\n"
"buffer_load_dwordx4 a[132:135], v1, s[36:39], s44 offen offset:1024 sc1\n"
"buffer_load_dwordx4 a[136:139], v1, s[36:39], s44 offen offset:2048 sc1\n"
"s_waitcnt vmcnt(7) lgkmcnt(0)\n"
"v_mfma_f32_16x16x32_bf16 v[108:111], a[92:95], a[0:3], 0\n"
"s_lshl_b32 s44, s76, 6\n"
"s_add_u32 s44, s44, s72\n"
"buffer_load_dwordx4 a[144:147], v1, s[36:39], s44 offen offset:0 sc1\n"
"v_mfma_f32_16x16x32_bf16 v[112:115], a[92:95], a[4:7], 0\n"
"s_and_b32 s9, %2, 6\n"
"ds_read_b128 a[40:43], v28 offset:10240\n"
"ds_read_b128 a[60:63], v29 offset:10240\n"
"v_mfma_f32_16x16x32_bf16 v[116:119], a[92:95], a[8:11], 0\n"
"s_bfe_u32 s10, %2, 0x10003\n"
"ds_read_b128 a[44:47], v28 offset:12288\n"
"ds_read_b128 a[64:67], v29 offset:12288\n"
"v_mfma_f32_16x16x32_bf16 v[120:123], a[92:95], a[12:15], 0\n"
"s_add_u32 s91, s9, s10\n"
"ds_read_b128 a[48:51], v28 offset:14336\n"
"ds_read_b128 a[68:71], v29 offset:14336\n"
"v_mfma_f32_16x16x32_bf16 v[124:127], a[92:95], a[16:19], 0\n"
"s_and_b32 s9, %2, 1\n"
"buffer_load_dwordx4 a[148:151], v1, s[36:39], s44 offen offset:1024 sc1\n"
"v_mfma_f32_16x16x32_bf16 v[68:71], a[84:87], a[0:3], 0\n"
"s_lshl_b32 s9, s9, 4\n"
"ds_read_b128 a[52:55], v28 offset:16384\n"
"ds_read_b128 a[72:75], v29 offset:16384\n"
"v_mfma_f32_16x16x32_bf16 v[68:71], a[100:103], a[20:23], v[68:71]\n"
"s_lshr_b32 s10, %2, 4\n"
"ds_read_b128 a[56:59], v28 offset:18432\n"
"ds_read_b128 a[76:79], v29 offset:18432\n"
"v_mfma_f32_16x16x32_bf16 v[72:75], a[84:87], a[4:7], 0\n"
"s_add_u32 s90, s9, s10\n"
"s_lshl_b32 s9, s6, 9\n"
"s_add_u32 s9, s9, 39634944\n"
"v_mfma_f32_16x16x32_bf16 v[72:75], a[100:103], a[24:27], v[72:75]\n"
"s_lshl_b32 s10, s5, 12\n"
"s_add_u32 s10, s10, s9\n"
"s_lshl_b32 s11, s91, 12\n"
"v_mfma_f32_16x16x32_bf16 v[76:79], a[84:87], a[8:11], 0\n"
"s_add_u32 s11, s11, s9\n"
"buffer_load_dwordx4 a[152:155], v1, s[36:39], s44 offen offset:2048 sc1\n"
"v_mfma_f32_16x16x32_bf16 v[76:79], a[100:103], a[28:31], v[76:79]\n"
"v_bfe_u32 v157, v0, 2, 2\n"
"v_lshl_add_u32 v9, v157, 7, s10\n"
"v_mfma_f32_16x16x32_bf16 v[80:83], a[84:87], a[12:15], 0\n"
"v_lshl_add_u32 v156, v157, 7, s11\n"
"v_and_b32 v157, 1, v157\n"
"v_mfma_f32_16x16x32_bf16 v[80:83], a[100:103], a[32:35], v[80:83]\n"
"s_lshl_b32 s9, s6, 13\n"
"s_add_u32 s9, s9, 39632896\n"
"s_lshl_b32 s10, s94, 7\n"
"v_mfma_f32_16x16x32_bf16 v[84:87], a[84:87], a[16:19], 0\n"
"s_add_u32 s10, s10, s9\n"
"v_mov_b32_dpp v9, v156 quad_perm:[0,1,2,3] row_mask:0x2 bank_mask:0xf\n"
"v_lshl_add_u32 v156, v157, 12, s10\n"
"v_mfma_f32_16x16x32_bf16 v[84:87], a[100:103], a[36:39], v[84:87]\n"
"s_and_b32 s11, s90, 3\n"
"buffer_load_dwordx4 a[156:159], v1, s[36:39], s44 offen offset:3072 sc1\n"
"v_mfma_f32_16x16x32_bf16 v[48:51], a[80:83], a[0:3], 0\n"
"s_lshl_b32 s11, s11, 3\n"
"s_lshr_b32 s10, s90, 2\n"
"s_add_u32 s10, s10, s11\n"
"v_mfma_f32_16x16x32_bf16 v[48:51], a[96:99], a[20:23], v[48:51]\n"
"s_lshl_b32 s10, s10, 7\n"
"s_add_u32 s10, s10, s9\n"
"v_mov_b32_dpp v9, v156 quad_perm:[0,1,2,3] row_mask:0x4 bank_mask:0x3\n"
"v_mfma_f32_16x16x32_bf16 v[52:55], a[80:83], a[4:7], 0\n"
"v_lshl_add_u32 v156, v157, 12, s10\n"
"v_mfma_f32_16x16x32_bf16 v[52:55], a[96:99], a[24:27], v[52:55]\n"
"s_addk_i32 s44, 4096\n"
"buffer_load_dwordx4 a[160:163], v1, s[36:39], s44 offen offset:0 sc1\n"
"v_mfma_f32_16x16x32_bf16 v[56:59], a[80:83], a[8:11], 0\n"
"s_sub_u32 s9, s4, 1\n"
"s_cselect_b32 s9, 0, s9\n"
"s_and_b32 s10, s9, 3\n"
"v_mfma_f32_16x16x32_bf16 v[56:59], a[96:99], a[28:31], v[56:59]\n"
"s_lshl_b32 s10, s10, 3\n"
"s_lshr_b32 s9, s9, 2\n"
"s_add_u32 s9, s9, s10\n"
"v_mfma_f32_16x16x32_bf16 v[60:63], a[80:83], a[12:15], 0\n"
"s_lshl_b32 s9, s9, 7\n"
"s_add_u32 s9, s9, 39657472\n"
"v_mov_b32_dpp v9, v156 quad_perm:[0,1,2,3] row_mask:0x4 bank_mask:0xc\n"
"v_mfma_f32_16x16x32_bf16 v[60:63], a[96:99], a[32:35], v[60:63]\n"
"v_lshl_add_u32 v156, v157, 12, s9\n"
"v_add_u32 v30, 61440, v28\n"
"v_mfma_f32_16x16x32_bf16 v[64:67], a[80:83], a[16:19], 0\n"
"buffer_load_dwordx4 a[164:167], v1, s[36:39], s44 offen offset:1024 sc1\n"
"v_mfma_f32_16x16x32_bf16 v[64:67], a[96:99], a[36:39], v[64:67]\n"
"v_add_u32 v31, 61440, v29\n"
"s_and_b32 s9, s6, 1\n"
"v_mfma_f32_16x16x32_bf16 v[88:91], a[0:3], a[88:91], 0\n"
"s_add_u32 s10, s9, s4\n"
"s_lshl_b32 s10, s10, 3\n"
"s_lshr_b32 s11, s6, 1\n"
"v_mfma_f32_16x16x32_bf16 v[88:91], a[20:23], a[104:107], v[88:91]\n"
"s_lshl_b32 s11, s11, 5\n"
"v_mov_b32_dpp v9, v156 quad_perm:[0,1,2,3] row_mask:0x8 bank_mask:0xf\n"
"v_add_u32 v156, s10, v12\n"
"v_mfma_f32_16x16x32_bf16 v[92:95], a[4:7], a[88:91], 0\n"
"v_add_u32 v156, -2, v156\n"
"v_min_u32 v156, 0xfe0000, v156\n"
"v_mfma_f32_16x16x32_bf16 v[92:95], a[24:27], a[104:107], v[92:95]\n"
"buffer_load_dwordx4 a[168:171], v1, s[36:39], s44 offen offset:2048 sc1\n"
"v_mfma_f32_16x16x32_bf16 v[96:99], a[8:11], a[88:91], 0\n"
"v_lshl_add_u32 v156, v156, 7, s11\n"
"v_lshl_add_u32 v178, v13, 2, v156\n"
"v_mfma_f32_16x16x32_bf16 v[96:99], a[28:31], a[104:107], v[96:99]\n"
"s_mul_i32 s9, s9, 2130706432\n"
"v_or_b32 v156, s9, v178\n"
"s_mov_b32 s10, 96768\n"
"v_mfma_f32_16x16x32_bf16 v[100:103], a[12:15], a[88:91], 0\n"
"v_lshl_add_u32 v179, v15, 2, s10\n"
"v_mov_b32_dpp v178, v156 quad_perm:[0,1,2,3] row_mask:0xe bank_mask:0xf\n"
"v_mfma_f32_16x16x32_bf16 v[100:103], a[32:35], a[104:107], v[100:103]\n"
"v_lshl_add_u32 v45, v14, 4, s10\n"
"s_and_b32 s54, s5, 3\n"
"v_mfma_f32_16x16x32_bf16 v[104:107], a[16:19], a[88:91], 0\n"
"v_mfma_f32_16x16x32_bf16 v[104:107], a[36:39], a[104:107], v[104:107]\n"
"s_waitcnt vmcnt(7) lgkmcnt(0)\n"
"v_mfma_f32_16x16x32_bf16 v[108:111], a[124:127], a[40:43], v[108:111]\n"
"s_lshl_b32 s44, s77, 6\n"
"s_add_u32 s44, s44, s72\n"
"buffer_load_dwordx4 a[80:83], v1, s[36:39], s44 offen offset:0 sc1\n"
"v_mfma_f32_16x16x32_bf16 v[112:115], a[124:127], a[44:47], v[112:115]\n"
"s_add_u32 s10, s54, s6\n"
"ds_read_b128 a[0:3], v28 offset:20480\n"
"ds_read_b128 a[20:23], v29 offset:20480\n"
"v_mfma_f32_16x16x32_bf16 v[116:119], a[124:127], a[48:51], v[116:119]\n"
"s_and_b32 s10, s10, 3\n"
"ds_read_b128 a[4:7], v28 offset:22528\n"
"ds_read_b128 a[24:27], v29 offset:22528\n"
"v_mfma_f32_16x16x32_bf16 v[120:123], a[124:127], a[52:55], v[120:123]\n"
"s_mul_i32 s70, s10, 165888\n"
"ds_read_b128 a[8:11], v28 offset:24576\n"
"ds_read_b128 a[28:31], v29 offset:24576\n"
"v_mfma_f32_16x16x32_bf16 v[124:127], a[124:127], a[56:59], v[124:127]\n"
"s_lshl_b32 s11, s5, 7\n"
"buffer_load_dwordx4 a[84:87], v1, s[36:39], s44 offen offset:1024 sc1\n"
"v_mfma_f32_16x16x32_bf16 v[68:71], a[116:119], a[40:43], v[68:71]\n"
"s_add_u32 s70, s70, s11\n"
"ds_read_b128 a[12:15], v28 offset:26624\n"
"ds_read_b128 a[32:35], v29 offset:26624\n"
"v_mfma_f32_16x16x32_bf16 v[68:71], a[132:135], a[60:63], v[68:71]\n"
"s_mul_i32 s71, s10, 4608\n"
"ds_read_b128 a[16:19], v28 offset:28672\n"
"ds_read_b128 a[36:39], v29 offset:28672\n"
"v_mfma_f32_16x16x32_bf16 v[72:75], a[116:119], a[44:47], v[72:75]\n"
"s_mul_i32 s11, s5, 18432\n"
"s_add_u32 s71, s71, s11\n"
"s_add_u32 s71, s71, 37093376\n"
"v_mfma_f32_16x16x32_bf16 v[72:75], a[132:135], a[64:67], v[72:75]\n"
"s_lshl_b32 s9, s4, 2\n"
"s_add_u32 s9, s9, s6\n"
"s_lshl_b32 s9, s9, 14\n"
"v_mfma_f32_16x16x32_bf16 v[76:79], a[116:119], a[48:51], v[76:79]\n"
"s_add_u32 s9, s9, 131072\n"
"buffer_load_dwordx4 a[88:91], v1, s[36:39], s44 offen offset:2048 sc1\n"
"v_mfma_f32_16x16x32_bf16 v[76:79], a[132:135], a[68:71], v[76:79]\n"
"s_add_u32 s10, s54, 0\n"
"s_and_b32 s10, s10, 3\n"
"s_lshl_b32 s10, s10, 12\n"
"v_mfma_f32_16x16x32_bf16 v[80:83], a[116:119], a[52:55], v[80:83]\n"
"s_add_u32 s58, s9, s10\n"
"s_add_u32 s10, s54, 1\n"
"s_and_b32 s10, s10, 3\n"
"v_mfma_f32_16x16x32_bf16 v[80:83], a[132:135], a[72:75], v[80:83]\n"
"s_lshl_b32 s10, s10, 12\n"
"s_add_u32 s59, s9, s10\n"
"s_add_u32 s10, s54, 2\n"
"v_mfma_f32_16x16x32_bf16 v[84:87], a[116:119], a[56:59], v[84:87]\n"
"s_and_b32 s10, s10, 3\n"
"s_lshl_b32 s10, s10, 12\n"
"s_add_u32 s60, s9, s10\n"
"v_mfma_f32_16x16x32_bf16 v[84:87], a[132:135], a[76:79], v[84:87]\n"
"s_add_u32 s10, s54, 3\n"
"buffer_load_dwordx4 a[92:95], v1, s[36:39], s44 offen offset:3072 sc1\n"
"v_mfma_f32_16x16x32_bf16 v[48:51], a[112:115], a[40:43], v[48:51]\n"
"s_and_b32 s10, s10, 3\n"
"s_lshl_b32 s10, s10, 12\n"
"s_add_u32 s61, s9, s10\n"
"v_mfma_f32_16x16x32_bf16 v[48:51], a[128:131], a[60:63], v[48:51]\n"
"s_lshl_b32 s9, s4, 13\n"
"s_lshl_b32 s10, s6, 6\n"
"s_add_u32 s9, s9, s10\n"
"v_mfma_f32_16x16x32_bf16 v[52:55], a[112:115], a[44:47], v[52:55]\n"
"s_add_u32 s62, s9, 2228224\n"
"s_lshl_b32 s10, s54, 1\n"
"v_add_u32 v156, s10, v14\n"
"v_mfma_f32_16x16x32_bf16 v[52:55], a[128:131], a[64:67], v[52:55]\n"
"s_addk_i32 s44, 4096\n"
"buffer_load_dwordx4 a[96:99], v1, s[36:39], s44 offen offset:0 sc1\n"
"v_mfma_f32_16x16x32_bf16 v[56:59], a[112:115], a[48:51], v[56:59]\n"
"v_and_b32 v156, 7, v156\n"
"v_lshlrev_b32 v156, 10, v156\n"
"v_mfma_f32_16x16x32_bf16 v[56:59], a[128:131], a[68:71], v[56:59]\n"
"v_lshl_add_u32 v26, v15, 2, v156\n"
"v_xor_b32 v27, 0x1000, v26\n"
"v_mfma_f32_16x16x32_bf16 v[60:63], a[112:115], a[52:55], v[60:63]\n"
"v_and_b32 v156, 3, v0\n"
"v_lshlrev_b32 v156, 3, v156\n"
"v_mfma_f32_16x16x32_bf16 v[60:63], a[128:131], a[72:75], v[60:63]\n"
"v_lshrrev_b32 v157, 5, v0\n"
"v_mul_u32_u24 v157, 12, v157\n"
"v_mfma_f32_16x16x32_bf16 v[64:67], a[112:115], a[56:59], v[64:67]\n"
"buffer_load_dwordx4 a[100:103], v1, s[36:39], s44 offen offset:1024 sc1\n"
"v_mfma_f32_16x16x32_bf16 v[64:67], a[128:131], a[76:79], v[64:67]\n"
"v_lshrrev_b32 v158, 2, v0\n"
"v_add_u32 v157, v157, v158\n"
"v_mfma_f32_16x16x32_bf16 v[88:91], a[40:43], a[120:123], v[88:91]\n"
"v_lshl_add_u32 v24, v157, 5, v156\n"
"v_add_u32 v24, 73728, v24\n"
"v_mfma_f32_16x16x32_bf16 v[88:91], a[60:63], a[136:139], v[88:91]\n"
"v_bfe_u32 v158, v157, 1, 2\n"
"v_lshl_add_u32 v157, v157, 7, v156\n"
"v_mfma_f32_16x16x32_bf16 v[92:95], a[44:47], a[120:123], v[92:95]\n"
"v_lshl_add_u32 v16, v158, 5, v157\n"
"v_bfe_u32 v157, v0, 2, 2\n"
"v_mfma_f32_16x16x32_bf16 v[92:95], a[64:67], a[136:139], v[92:95]\n"
"buffer_load_dwordx4 a[104:107], v1, s[36:39], s44 offen offset:2048 sc1\n"
"v_mfma_f32_16x16x32_bf16 v[96:99], a[48:51], a[120:123], v[96:99]\n"
"v_mul_u32_u24 v158, 72, v14\n"
"v_add_u32 v158, v158, v157\n"
"v_mfma_f32_16x16x32_bf16 v[96:99], a[68:71], a[136:139], v[96:99]\n"
"v_lshl_add_u32 v159, v158, 7, v156\n"
"v_and_b32 v157, 1, v12\n"
"v_mfma_f32_16x16x32_bf16 v[100:103], a[52:55], a[120:123], v[100:103]\n"
"s_mul_i32 s8, s6, 18432\n"
"v_lshl_add_u32 v20, v157, 5, v159\n"
"v_xor_b32 v17, 32, v16\n"
"v_mfma_f32_16x16x32_bf16 v[100:103], a[72:75], a[136:139], v[100:103]\n"
"s_mul_i32 s12, s6, 4608\n"
"v_xor_b32 v21, 32, v20\n"
"v_xor_b32 v18, 64, v16\n"
"v_mfma_f32_16x16x32_bf16 v[104:107], a[56:59], a[120:123], v[104:107]\n"
"s_add_u32 s12, s12, 73728\n"
"v_mfma_f32_16x16x32_bf16 v[104:107], a[76:79], a[136:139], v[104:107]\n"
"s_waitcnt vmcnt(7) lgkmcnt(0)\n"
"s_barrier\n"
"v_mfma_f32_16x16x32_bf16 v[108:111], a[156:159], a[0:3], v[108:111]\n"
"s_cmp_lg_u32 s6, 0\n"
"s_cbranch_scc1 LFD0_block_kernel\n"
"s_add_u32 s13, s20, 39632896\n"
"s_mov_b32 exec_lo, 0xffff\n"
"s_mov_b32 exec_hi, 0\n"
"buffer_store_dwordx2 v[6:7], v2, s[36:39], s13 offen sc1\n"
"s_mov_b64 exec, -1\n"
"LFD0_block_kernel:\n"
"v_mfma_f32_16x16x32_bf16 v[112:115], a[156:159], a[4:7], v[112:115]\n"
"ds_read_b128 a[40:43], v28 offset:30720\n"
"ds_read_b128 a[60:63], v29 offset:30720\n"
"v_mfma_f32_16x16x32_bf16 v[116:119], a[156:159], a[8:11], v[116:119]\n"
"ds_read_b128 a[44:47], v28 offset:32768\n"
"ds_read_b128 a[64:67], v29 offset:32768\n"
"v_mfma_f32_16x16x32_bf16 v[120:123], a[156:159], a[12:15], v[120:123]\n"
"s_lshl_b32 s44, s78, 6\n"
"ds_read_b128 a[48:51], v28 offset:34816\n"
"ds_read_b128 a[68:71], v29 offset:34816\n"
"v_mfma_f32_16x16x32_bf16 v[124:127], a[156:159], a[16:19], v[124:127]\n"
"s_add_u32 s44, s44, s72\n"
"buffer_load_dwordx4 a[112:115], v1, s[36:39], s44 offen offset:0 sc1\n"
"v_mfma_f32_16x16x32_bf16 v[68:71], a[148:151], a[0:3], v[68:71]\n"
"ds_read_b128 a[52:55], v28 offset:36864\n"
"ds_read_b128 a[72:75], v29 offset:36864\n"
"v_mfma_f32_16x16x32_bf16 v[68:71], a[164:167], a[20:23], v[68:71]\n"
"ds_read_b128 a[56:59], v28 offset:38912\n"
"ds_read_b128 a[76:79], v29 offset:38912\n"
"v_mfma_f32_16x16x32_bf16 v[72:75], a[148:151], a[4:7], v[72:75]\n"
"v_xor_b32 v22, 64, v20\n"
"v_xor_b32 v19, 96, v16\n"
"v_mfma_f32_16x16x32_bf16 v[72:75], a[164:167], a[24:27], v[72:75]\n"
"buffer_load_dwordx4 a[116:119], v1, s[36:39], s44 offen offset:1024 sc1\n"
"v_mfma_f32_16x16x32_bf16 v[76:79], a[148:151], a[8:11], v[76:79]\n"
"v_xor_b32 v23, 96, v20\n"
"v_lshl_add_u32 v25, v158, 5, v156\n"
"v_mfma_f32_16x16x32_bf16 v[76:79], a[164:167], a[28:31], v[76:79]\n"
"v_add_u32 v25, 73728, v25\n"
"v_lshlrev_b32 v157, 2, v14\n"
"v_mfma_f32_16x16x32_bf16 v[80:83], a[148:151], a[12:15], v[80:83]\n"
"v_cmp_le_u32 s[50:51], v157, v15\n"
"v_add_u32 v157, 2, v157\n"
"v_mfma_f32_16x16x32_bf16 v[80:83], a[164:167], a[32:35], v[80:83]\n"
"buffer_load_dwordx4 a[120:123], v1, s[36:39], s44 offen offset:2048 sc1\n"
"v_mfma_f32_16x16x32_bf16 v[84:87], a[148:151], a[16:19], v[84:87]\n"
"v_cmp_le_u32 s[52:53], v157, v15\n"
"v_mov_b32 v156, 0x0c0c0100\n"
"v_mfma_f32_16x16x32_bf16 v[84:87], a[164:167], a[36:39], v[84:87]\n"
"v_mov_b32 v157, 0x01000c0c\n"
"v_cndmask_b32 v156, v156, v157, s[48:49]\n"
"v_mfma_f32_16x16x32_bf16 v[48:51], a[144:147], a[0:3], v[48:51]\n"
"v_mov_b32 v157, 0x0c0c0302\n"
"v_mov_b32 v158, 0x03020c0c\n"
"v_mfma_f32_16x16x32_bf16 v[48:51], a[160:163], a[20:23], v[48:51]\n"
"buffer_load_dwordx4 a[124:127], v1, s[36:39], s44 offen offset:3072 sc1\n"
"v_mfma_f32_16x16x32_bf16 v[52:55], a[144:147], a[4:7], v[52:55]\n"
"v_cndmask_b32 v157, v157, v158, s[48:49]\n"
"v_mov_b32 v158, 0x0c0c0c0c\n"
"v_mfma_f32_16x16x32_bf16 v[52:55], a[160:163], a[24:27], v[52:55]\n"
"v_cndmask_b32 v36, v156, v158, s[50:51]\n"
"v_cndmask_b32 v37, v157, v158, s[52:53]\n"
"v_mfma_f32_16x16x32_bf16 v[56:59], a[144:147], a[8:11], v[56:59]\n"
"v_cndmask_b32 v38, v158, v156, s[50:51]\n"
"v_cndmask_b32 v39, v158, v157, s[52:53]\n"
"v_mfma_f32_16x16x32_bf16 v[56:59], a[160:163], a[28:31], v[56:59]\n"
"s_addk_i32 s44, 4096\n"
"buffer_load_dwordx4 a[128:131], v1, s[36:39], s44 offen offset:0 sc1\n"
"v_mfma_f32_16x16x32_bf16 v[60:63], a[144:147], a[12:15], v[60:63]\n"
"s_mul_i32 s10, s6, 640\n"
"s_add_u32 s10, s10, 94208\n"
"v_add_u32 v156, 1, v14\n"
"v_mfma_f32_16x16x32_bf16 v[60:63], a[160:163], a[32:35], v[60:63]\n"
"v_lshrrev_b32 v156, 2, v156\n"
"v_mul_u32_u24 v156, 320, v156\n"
"v_mfma_f32_16x16x32_bf16 v[64:67], a[144:147], a[16:19], v[64:67]\n"
"v_lshl_add_u32 v40, v15, 1, v156\n"
"v_add_u32 v40, s10, v40\n"
"v_mfma_f32_16x16x32_bf16 v[64:67], a[160:163], a[36:39], v[64:67]\n"
"buffer_load_dwordx4 a[132:135], v1, s[36:39], s44 offen offset:1024 sc1\n"
"v_mfma_f32_16x16x32_bf16 v[88:91], a[0:3], a[152:155], v[88:91]\n"
"v_and_b32 v156, 1, v0\n"
"v_mul_u32_u24 v41, 160, v156\n"
"v_mfma_f32_16x16x32_bf16 v[88:91], a[20:23], a[168:171], v[88:91]\n"
"v_lshl_add_u32 v41, v14, 3, v41\n"
"v_add_u32 v41, s10, v41\n"
"v_mfma_f32_16x16x32_bf16 v[92:95], a[4:7], a[152:155], v[92:95]\n"
"v_mov_b32 v182, 0\n"
"v_mov_b32 v183, 0\n"
"v_mfma_f32_16x16x32_bf16 v[92:95], a[24:27], a[168:171], v[92:95]\n"
"buffer_load_dwordx4 a[136:139], v1, s[36:39], s44 offen offset:2048 sc1\n"
"v_mfma_f32_16x16x32_bf16 v[96:99], a[8:11], a[152:155], v[96:99]\n"
"v_mov_b32 v186, 0\n"
"v_mov_b32 v187, 0\n"
"v_mfma_f32_16x16x32_bf16 v[96:99], a[28:31], a[168:171], v[96:99]\n"
"v_mov_b32 v190, 0\n"
"v_mov_b32 v191, 0\n"
"v_mfma_f32_16x16x32_bf16 v[100:103], a[12:15], a[152:155], v[100:103]\n"
"v_mov_b32 v194, 0\n"
"v_mov_b32 v195, 0\n"
"v_mfma_f32_16x16x32_bf16 v[100:103], a[32:35], a[168:171], v[100:103]\n"
"v_mov_b32 v198, 0\n"
"v_mov_b32 v199, 0\n"
"v_mfma_f32_16x16x32_bf16 v[104:107], a[16:19], a[152:155], v[104:107]\n"
"v_mfma_f32_16x16x32_bf16 v[104:107], a[36:39], a[168:171], v[104:107]\n"
"s_waitcnt vmcnt(7) lgkmcnt(0)\n"
"v_mfma_f32_16x16x32_bf16 v[108:111], a[92:95], a[40:43], v[108:111]\n"
"s_lshl_b32 s44, s79, 6\n"
"s_add_u32 s44, s44, s72\n"
"buffer_load_dwordx4 a[144:147], v1, s[36:39], s44 offen offset:0 sc1\n"
"v_mfma_f32_16x16x32_bf16 v[112:115], a[92:95], a[44:47], v[112:115]\n"
"ds_read_b128 a[0:3], v28 offset:40960\n"
"ds_read_b128 a[20:23], v29 offset:40960\n"
"v_mfma_f32_16x16x32_bf16 v[116:119], a[92:95], a[48:51], v[116:119]\n"
"ds_read_b128 a[4:7], v28 offset:43008\n"
"ds_read_b128 a[24:27], v29 offset:43008\n"
"v_mfma_f32_16x16x32_bf16 v[120:123], a[92:95], a[52:55], v[120:123]\n"
"buffer_load_dwordx4 a[148:151], v1, s[36:39], s44 offen offset:1024 sc1\n"
"v_mfma_f32_16x16x32_bf16 v[124:127], a[92:95], a[56:59], v[124:127]\n"
"ds_read_b128 a[8:11], v28 offset:45056\n"
"ds_read_b128 a[28:31], v29 offset:45056\n"
"v_mfma_f32_16x16x32_bf16 v[68:71], a[84:87], a[40:43], v[68:71]\n"
"ds_read_b128 a[12:15], v28 offset:47104\n"
"ds_read_b128 a[32:35], v29 offset:47104\n"
"v_mfma_f32_16x16x32_bf16 v[68:71], a[100:103], a[60:63], v[68:71]\n"
"buffer_load_dwordx4 a[152:155], v1, s[36:39], s44 offen offset:2048 sc1\n"
"v_mfma_f32_16x16x32_bf16 v[72:75], a[84:87], a[44:47], v[72:75]\n"
"ds_read_b128 a[16:19], v28 offset:49152\n"
"ds_read_b128 a[36:39], v29 offset:49152\n"
"v_mfma_f32_16x16x32_bf16 v[72:75], a[100:103], a[64:67], v[72:75]\n"
"v_mov_b32 v202, 0\n"
"v_mov_b32 v203, 0\n"
"v_mfma_f32_16x16x32_bf16 v[76:79], a[84:87], a[48:51], v[76:79]\n"
"buffer_load_dwordx4 a[156:159], v1, s[36:39], s44 offen offset:3072 sc1\n"
"v_mfma_f32_16x16x32_bf16 v[76:79], a[100:103], a[68:71], v[76:79]\n"
"v_mov_b32 v206, 0\n"
"v_mov_b32 v207, 0\n"
"v_mfma_f32_16x16x32_bf16 v[80:83], a[84:87], a[52:55], v[80:83]\n"
"v_mov_b32 v210, 0\n"
"v_mov_b32 v211, 0\n"
"v_mfma_f32_16x16x32_bf16 v[80:83], a[100:103], a[72:75], v[80:83]\n"
"v_mov_b32 v214, 0\n"
"v_mov_b32 v215, 0\n"
"v_mfma_f32_16x16x32_bf16 v[84:87], a[84:87], a[56:59], v[84:87]\n"
"s_addk_i32 s44, 4096\n"
"buffer_load_dwordx4 a[160:163], v1, s[36:39], s44 offen offset:0 sc1\n"
"v_mfma_f32_16x16x32_bf16 v[84:87], a[100:103], a[76:79], v[84:87]\n"
"s_cmp_eq_u32 s4, 0\n"
"s_cselect_b32 s10, 0xf149f2ca, 0\n"
"v_mov_b32 v144, s10\n"
"v_mfma_f32_16x16x32_bf16 v[48:51], a[80:83], a[40:43], v[48:51]\n"
"v_mov_b32 v145, s10\n"
"v_mov_b32 v146, s10\n"
"v_mfma_f32_16x16x32_bf16 v[48:51], a[96:99], a[60:63], v[48:51]\n"
"buffer_load_dwordx4 a[164:167], v1, s[36:39], s44 offen offset:1024 sc1\n"
"v_mfma_f32_16x16x32_bf16 v[52:55], a[80:83], a[44:47], v[52:55]\n"
"v_mov_b32 v147, s10\n"
"s_mov_b32 s47, 0xbf317218\n"
"v_mfma_f32_16x16x32_bf16 v[52:55], a[96:99], a[64:67], v[52:55]\n"
"s_and_b32 s33, s33, 0xffff\n"
"s_mov_b32 s34, 5308416\n"
"s_mov_b32 s35, 0x20000\n"
"v_mfma_f32_16x16x32_bf16 v[56:59], a[80:83], a[48:51], v[56:59]\n"
"s_and_b32 s41, s41, 0xffff\n"
"buffer_load_dwordx4 a[168:171], v1, s[36:39], s44 offen offset:2048 sc1\n"
"v_mfma_f32_16x16x32_bf16 v[56:59], a[96:99], a[68:71], v[56:59]\n"
"s_mov_b32 s42, 2359296\n"
"s_mov_b32 s43, 0x20000\n"
"s_mul_i32 s10, s91, 36\n"
"v_mfma_f32_16x16x32_bf16 v[60:63], a[80:83], a[52:55], v[60:63]\n"
"s_add_u32 s10, s10, s6\n"
"s_mul_i32 s10, s10, 18432\n"
"s_add_u32 s95, s10, 26476544\n"
"v_mfma_f32_16x16x32_bf16 v[60:63], a[96:99], a[72:75], v[60:63]\n"
"s_add_u32 m0, s8, 0\n"
"s_mul_i32 s73, s6, 3072\n"
"s_add_u32 s73, s73, 98304\n"
"v_mfma_f32_16x16x32_bf16 v[64:67], a[80:83], a[56:59], v[64:67]\n"
"s_add_u32 s13, s70, 0\n"
"buffer_load_dwordx4 v3, s[24:27], s13 offen lds\n"
"v_mfma_f32_16x16x32_bf16 v[64:67], a[96:99], a[76:79], v[64:67]\n"
"v_lshl_add_u32 v156, v14, 3, s73\n"
"v_lshl_add_u32 v32, v15, 5, v156\n"
"v_mfma_f32_16x16x32_bf16 v[88:91], a[40:43], a[88:91], v[88:91]\n"
"s_add_u32 m0, s8, 1024\n"
"v_lshl_add_u32 v156, v15, 7, v156\n"
"v_bfe_u32 v157, v0, 1, 2\n"
"v_mfma_f32_16x16x32_bf16 v[88:91], a[60:63], a[104:107], v[88:91]\n"
"s_add_u32 s13, s70, 9216\n"
"buffer_load_dwordx4 v3, s[24:27], s13 offen lds\n"
"v_mfma_f32_16x16x32_bf16 v[92:95], a[44:47], a[88:91], v[92:95]\n"
"v_lshl_add_u32 v47, v157, 5, v156\n"
"s_mul_i32 s9, s99, 3641\n"
"v_mfma_f32_16x16x32_bf16 v[92:95], a[64:67], a[104:107], v[92:95]\n"
"s_add_u32 m0, s8, 2048\n"
"s_lshr_b32 s9, s9, 16\n"
"s_mul_i32 s10, s9, 18\n"
"v_mfma_f32_16x16x32_bf16 v[96:99], a[48:51], a[88:91], v[96:99]\n"
"s_add_u32 s13, s70, 18432\n"
"buffer_load_dwordx4 v3, s[24:27], s13 offen lds\n"
"v_mfma_f32_16x16x32_bf16 v[96:99], a[68:71], a[104:107], v[96:99]\n"
"s_sub_u32 s10, s99, s10\n"
"s_mul_i32 s22, s10, 36864\n"
"s_lshl_b32 s11, s9, 7\n"
"v_mfma_f32_16x16x32_bf16 v[100:103], a[52:55], a[88:91], v[100:103]\n"
"s_add_u32 s22, s22, s11\n"
"s_mul_i32 s11, s5, 663552\n"
"s_add_u32 s22, s22, s11\n"
"v_mfma_f32_16x16x32_bf16 v[100:103], a[72:75], a[104:107], v[100:103]\n"
"s_mul_i32 s11, s5, 9\n"
"s_add_u32 s11, s11, s9\n"
"s_mul_i32 s23, s11, 73728\n"
"v_mfma_f32_16x16x32_bf16 v[104:107], a[56:59], a[88:91], v[104:107]\n"
"v_mfma_f32_16x16x32_bf16 v[104:107], a[76:79], a[104:107], v[104:107]\n"
"s_waitcnt vmcnt(10) lgkmcnt(0)\n"
"v_mfma_f32_16x16x32_bf16 v[108:111], a[124:127], a[0:3], v[108:111]\n"
"s_lshl_b32 s44, s80, 6\n"
"s_add_u32 s44, s44, s72\n"
"buffer_load_dwordx4 a[80:83], v1, s[36:39], s44 offen offset:0 sc1\n"
"v_mfma_f32_16x16x32_bf16 v[112:115], a[124:127], a[4:7], v[112:115]\n"
"s_lshl_b32 s11, s10, 10\n"
"ds_read_b128 a[40:43], v28 offset:51200\n"
"ds_read_b128 a[60:63], v29 offset:51200\n"
"v_mfma_f32_16x16x32_bf16 v[116:119], a[124:127], a[8:11], v[116:119]\n"
"s_add_u32 s23, s23, s11\n"
"ds_read_b128 a[44:47], v28 offset:53248\n"
"ds_read_b128 a[64:67], v29 offset:53248\n"
"v_mfma_f32_16x16x32_bf16 v[120:123], a[124:127], a[12:15], v[120:123]\n"
"s_add_u32 s23, s23, 31784960\n"
"buffer_load_dwordx4 a[84:87], v1, s[36:39], s44 offen offset:1024 sc1\n"
"v_mfma_f32_16x16x32_bf16 v[124:127], a[124:127], a[16:19], v[124:127]\n"
"s_add_u32 s99, s99, 34\n"
"ds_read_b128 a[48:51], v28 offset:55296\n"
"ds_read_b128 a[68:71], v29 offset:55296\n"
"v_mfma_f32_16x16x32_bf16 v[68:71], a[116:119], a[0:3], v[68:71]\n"
"s_mul_i32 s9, s99, 3641\n"
"ds_read_b128 a[52:55], v28 offset:57344\n"
"ds_read_b128 a[72:75], v29 offset:57344\n"
"v_mfma_f32_16x16x32_bf16 v[68:71], a[132:135], a[20:23], v[68:71]\n"
"s_lshr_b32 s9, s9, 16\n"
"buffer_load_dwordx4 a[88:91], v1, s[36:39], s44 offen offset:2048 sc1\n"
"v_mfma_f32_16x16x32_bf16 v[72:75], a[116:119], a[4:7], v[72:75]\n"
"s_mul_i32 s10, s9, 18\n"
"ds_read_b128 a[56:59], v28 offset:59392\n"
"ds_read_b128 a[76:79], v29 offset:59392\n"
"v_mfma_f32_16x16x32_bf16 v[72:75], a[132:135], a[24:27], v[72:75]\n"
"s_sub_u32 s10, s99, s10\n"
"s_mul_i32 s92, s10, 294912\n"
"s_lshl_b32 s11, s9, 7\n"
"v_mfma_f32_16x16x32_bf16 v[76:79], a[116:119], a[8:11], v[76:79]\n"
"s_add_u32 s92, s92, s11\n"
"buffer_load_dwordx4 a[92:95], v1, s[36:39], s44 offen offset:3072 sc1\n"
"v_mfma_f32_16x16x32_bf16 v[76:79], a[132:135], a[28:31], v[76:79]\n"
"s_mul_i32 s11, s5, 1152\n"
"s_add_u32 s92, s92, s11\n"
"s_mul_i32 s11, s5, 9\n"
"v_mfma_f32_16x16x32_bf16 v[80:83], a[116:119], a[12:15], v[80:83]\n"
"s_add_u32 s11, s11, s9\n"
"s_mul_i32 s93, s11, 73728\n"
"s_lshl_b32 s11, s10, 10\n"
"v_mfma_f32_16x16x32_bf16 v[80:83], a[132:135], a[32:35], v[80:83]\n"
"s_addk_i32 s44, 4096\n"
"buffer_load_dwordx4 a[96:99], v1, s[36:39], s44 offen offset:0 sc1\n"
"v_mfma_f32_16x16x32_bf16 v[84:87], a[116:119], a[16:19], v[84:87]\n"
"s_add_u32 s93, s93, s11\n"
"s_add_u32 s93, s93, 26476544\n"
"s_add_u32 s96, s96, 112640\n"
"v_mfma_f32_16x16x32_bf16 v[84:87], a[132:135], a[36:39], v[84:87]\n"
"v_add_u32 v168, 112640, v168\n"
"v_add_u32 v169, 112640, v169\n"
"v_mfma_f32_16x16x32_bf16 v[48:51], a[112:115], a[0:3], v[48:51]\n"
"buffer_load_dwordx4 a[100:103], v1, s[36:39], s44 offen offset:1024 sc1\n"
"v_mfma_f32_16x16x32_bf16 v[48:51], a[128:131], a[20:23], v[48:51]\n"
"v_add_u32 v170, 112640, v170\n"
"v_add_u32 v171, 112640, v171\n"
"v_mfma_f32_16x16x32_bf16 v[52:55], a[112:115], a[4:7], v[52:55]\n"
"s_lshl_b32 s99, s6, 5\n"
"s_add_u32 s99, s99, s94\n"
"s_add_u32 s99, s99, 128\n"
"v_mfma_f32_16x16x32_bf16 v[52:55], a[128:131], a[24:27], v[52:55]\n"
"s_mul_i32 s9, s99, 3641\n"
"buffer_load_dwordx4 a[104:107], v1, s[36:39], s44 offen offset:2048 sc1\n"
"v_mfma_f32_16x16x32_bf16 v[56:59], a[112:115], a[8:11], v[56:59]\n"
"s_lshr_b32 s9, s9, 16\n"
"s_mul_i32 s10, s9, 18\n"
"s_sub_u32 s10, s99, s10\n"
"v_mfma_f32_16x16x32_bf16 v[56:59], a[128:131], a[28:31], v[56:59]\n"
"s_mul_i32 s97, s10, 36864\n"
"s_lshl_b32 s11, s9, 7\n"
"s_add_u32 s97, s97, s11\n"
"v_mfma_f32_16x16x32_bf16 v[60:63], a[112:115], a[12:15], v[60:63]\n"
"s_mul_i32 s11, s5, 663552\n"
"buffer_load_dwordx2 v[10:11], v9, s[36:39], 0 offen sc1\n"
"v_mfma_f32_16x16x32_bf16 v[60:63], a[128:131], a[32:35], v[60:63]\n"
"s_add_u32 s97, s97, s11\n"
"s_mul_i32 s11, s5, 9\n"
"s_add_u32 s11, s11, s9\n"
"v_mfma_f32_16x16x32_bf16 v[64:67], a[112:115], a[16:19], v[64:67]\n"
"s_add_u32 m0, s8, 3072\n"
"s_mul_i32 s98, s11, 73728\n"
"s_lshl_b32 s11, s10, 10\n"
"v_mfma_f32_16x16x32_bf16 v[64:67], a[128:131], a[36:39], v[64:67]\n"
"s_add_u32 s13, s70, 27648\n"
"buffer_load_dwordx4 v3, s[24:27], s13 offen lds\n"
"v_mfma_f32_16x16x32_bf16 v[88:91], a[0:3], a[120:123], v[88:91]\n"
"s_add_u32 s98, s98, s11\n"
"s_add_u32 s98, s98, 31784960\n"
"v_mfma_f32_16x16x32_bf16 v[88:91], a[20:23], a[136:139], v[88:91]\n"
"s_add_u32 m0, s8, 4096\n"
"v_mfma_f32_16x16x32_bf16 v[92:95], a[4:7], a[120:123], v[92:95]\n"
"s_add_u32 s13, s70, 36864\n"
"buffer_load_dwordx4 v3, s[24:27], s13 offen lds\n"
"v_mfma_f32_16x16x32_bf16 v[92:95], a[24:27], a[136:139], v[92:95]\n"
"v_mfma_f32_16x16x32_bf16 v[96:99], a[8:11], a[120:123], v[96:99]\n"
"s_add_u32 m0, s8, 5120\n"
"v_mfma_f32_16x16x32_bf16 v[96:99], a[28:31], a[136:139], v[96:99]\n"
"s_add_u32 s13, s70, 46080\n"
"buffer_load_dwordx4 v3, s[24:27], s13 offen lds\n"
"v_mfma_f32_16x16x32_bf16 v[100:103], a[12:15], a[120:123], v[100:103]\n"
"v_mfma_f32_16x16x32_bf16 v[100:103], a[32:35], a[136:139], v[100:103]\n"
"v_mfma_f32_16x16x32_bf16 v[104:107], a[16:19], a[120:123], v[104:107]\n"
"v_mfma_f32_16x16x32_bf16 v[104:107], a[36:39], a[136:139], v[104:107]\n"
"s_waitcnt vmcnt(14) lgkmcnt(0)\n"
"v_mfma_f32_16x16x32_bf16 v[108:111], a[156:159], a[40:43], v[108:111]\n"
"s_lshl_b32 s44, s81, 6\n"
"s_add_u32 s44, s44, s72\n"
"buffer_load_dwordx4 a[112:115], v1, s[36:39], s44 offen offset:0 sc1\n"
"v_mfma_f32_16x16x32_bf16 v[112:115], a[156:159], a[44:47], v[112:115]\n"
"ds_read_b128 a[0:3], v30 offset:0\n"
"ds_read_b128 a[20:23], v31 offset:0\n"
"v_mfma_f32_16x16x32_bf16 v[116:119], a[156:159], a[48:51], v[116:119]\n"
"ds_read_b128 a[4:7], v30 offset:2048\n"
"ds_read_b128 a[24:27], v31 offset:2048\n"
"v_mfma_f32_16x16x32_bf16 v[120:123], a[156:159], a[52:55], v[120:123]\n"
"buffer_load_dwordx4 a[116:119], v1, s[36:39], s44 offen offset:1024 sc1\n"
"v_mfma_f32_16x16x32_bf16 v[124:127], a[156:159], a[56:59], v[124:127]\n"
"ds_read_b128 a[8:11], v30 offset:4096\n"
"ds_read_b128 a[28:31], v31 offset:4096\n"
"v_mfma_f32_16x16x32_bf16 v[68:71], a[148:151], a[40:43], v[68:71]\n"
"ds_read_b128 a[12:15], v30 offset:6144\n"
"ds_read_b128 a[32:35], v31 offset:6144\n"
"v_mfma_f32_16x16x32_bf16 v[68:71], a[164:167], a[60:63], v[68:71]\n"
"buffer_load_dwordx4 a[120:123], v1, s[36:39], s44 offen offset:2048 sc1\n"
"v_mfma_f32_16x16x32_bf16 v[72:75], a[148:151], a[44:47], v[72:75]\n"
"ds_read_b128 a[16:19], v30 offset:8192\n"
"ds_read_b128 a[36:39], v31 offset:8192\n"
"v_mfma_f32_16x16x32_bf16 v[72:75], a[164:167], a[64:67], v[72:75]\n"
"v_mfma_f32_16x16x32_bf16 v[76:79], a[148:151], a[48:51], v[76:79]\n"
"buffer_load_dwordx4 a[124:127], v1, s[36:39], s44 offen offset:3072 sc1\n"
"v_mfma_f32_16x16x32_bf16 v[76:79], a[164:167], a[68:71], v[76:79]\n"
"v_mfma_f32_16x16x32_bf16 v[80:83], a[148:151], a[52:55], v[80:83]\n"
"v_mfma_f32_16x16x32_bf16 v[80:83], a[164:167], a[72:75], v[80:83]\n"
"v_mfma_f32_16x16x32_bf16 v[84:87], a[148:151], a[56:59], v[84:87]\n"
"s_addk_i32 s44, 4096\n"
"buffer_load_dwordx4 a[128:131], v1, s[36:39], s44 offen offset:0 sc1\n"
"v_mfma_f32_16x16x32_bf16 v[84:87], a[164:167], a[76:79], v[84:87]\n"
"v_mfma_f32_16x16x32_bf16 v[48:51], a[144:147], a[40:43], v[48:51]\n"
"v_mfma_f32_16x16x32_bf16 v[48:51], a[160:163], a[60:63], v[48:51]\n"
"buffer_load_dwordx4 a[132:135], v1, s[36:39], s44 offen offset:1024 sc1\n"
"v_mfma_f32_16x16x32_bf16 v[52:55], a[144:147], a[44:47], v[52:55]\n"
"v_mfma_f32_16x16x32_bf16 v[52:55], a[160:163], a[64:67], v[52:55]\n"
"v_mfma_f32_16x16x32_bf16 v[56:59], a[144:147], a[48:51], v[56:59]\n"
"buffer_load_dwordx4 a[136:139], v1, s[36:39], s44 offen offset:2048 sc1\n"
"v_mfma_f32_16x16x32_bf16 v[56:59], a[160:163], a[68:71], v[56:59]\n"
"v_mfma_f32_16x16x32_bf16 v[60:63], a[144:147], a[52:55], v[60:63]\n"
"v_mfma_f32_16x16x32_bf16 v[60:63], a[160:163], a[72:75], v[60:63]\n"
"s_add_u32 m0, s8, 6144\n"
"v_mfma_f32_16x16x32_bf16 v[64:67], a[144:147], a[56:59], v[64:67]\n"
"s_add_u32 s13, s70, 55296\n"
"buffer_load_dwordx4 v3, s[24:27], s13 offen lds\n"
"v_mfma_f32_16x16x32_bf16 v[64:67], a[160:163], a[76:79], v[64:67]\n"
"v_mfma_f32_16x16x32_bf16 v[88:91], a[40:43], a[152:155], v[88:91]\n"
"s_add_u32 m0, s8, 7168\n"
"v_mfma_f32_16x16x32_bf16 v[88:91], a[60:63], a[168:171], v[88:91]\n"
"s_add_u32 s13, s70, 64512\n"
"buffer_load_dwordx4 v3, s[24:27], s13 offen lds\n"
"v_mfma_f32_16x16x32_bf16 v[92:95], a[44:47], a[152:155], v[92:95]\n"
"v_mfma_f32_16x16x32_bf16 v[92:95], a[64:67], a[168:171], v[92:95]\n"
"s_add_u32 m0, s8, 8192\n"
"v_mfma_f32_16x16x32_bf16 v[96:99], a[48:51], a[152:155], v[96:99]\n"
"s_add_u32 s13, s70, 73728\n"
"buffer_load_dwordx4 v3, s[24:27], s13 offen lds\n"
"v_mfma_f32_16x16x32_bf16 v[96:99], a[68:71], a[168:171], v[96:99]\n"
"v_mfma_f32_16x16x32_bf16 v[100:103], a[52:55], a[152:155], v[100:103]\n"
"v_mfma_f32_16x16x32_bf16 v[100:103], a[72:75], a[168:171], v[100:103]\n"
"v_mfma_f32_16x16x32_bf16 v[104:107], a[56:59], a[152:155], v[104:107]\n"
"s_waitcnt vmcnt(13)\n"
"v_cmp_ne_u64 vcc, v[10:11], v[6:7]\n"
"s_nop 0\n"
"s_cbranch_vccnz LFB_block_kernel_slow\n"
"LFB_block_kernel_back:\n"
"v_mfma_f32_16x16x32_bf16 v[104:107], a[76:79], a[168:171], v[104:107]\n"
"s_waitcnt vmcnt(14) lgkmcnt(0)\n"
"s_barrier\n"
"v_mfma_f32_16x16x32_bf16 v[108:111], a[92:95], a[0:3], v[108:111]\n"
"s_lshl_b32 s13, s6, 8\n"
"s_add_u32 m0, s13, 96768\n"
"s_mov_b32 s13, 98304\n"
"buffer_load_dword v178, s[36:39], s13 offen sc1 lds\n"
"v_mfma_f32_16x16x32_bf16 v[112:115], a[92:95], a[4:7], v[112:115]\n"
"ds_read_b128 a[40:43], v30 offset:10240\n"
"ds_read_b128 a[60:63], v31 offset:10240\n"
"v_mfma_f32_16x16x32_bf16 v[116:119], a[92:95], a[8:11], v[116:119]\n"
"s_lshl_b32 s44, s82, 6\n"
"ds_read_b128 a[44:47], v30 offset:12288\n"
"ds_read_b128 a[64:67], v31 offset:12288\n"
"v_mfma_f32_16x16x32_bf16 v[120:123], a[92:95], a[12:15], v[120:123]\n"
"s_add_u32 s44, s44, s72\n"
"buffer_load_dwordx4 a[144:147], v1, s[36:39], s44 offen offset:0 sc1\n"
"v_mfma_f32_16x16x32_bf16 v[124:127], a[92:95], a[16:19], v[124:127]\n"
"ds_read_b128 a[48:51], v30 offset:14336\n"
"ds_read_b128 a[68:71], v31 offset:14336\n"
"v_mfma_f32_16x16x32_bf16 v[68:71], a[84:87], a[0:3], v[68:71]\n"
"ds_read_b128 a[52:55], v30 offset:16384\n"
"ds_read_b128 a[72:75], v31 offset:16384\n"
"v_mfma_f32_16x16x32_bf16 v[68:71], a[100:103], a[20:23], v[68:71]\n"
"buffer_load_dwordx4 a[148:151], v1, s[36:39], s44 offen offset:1024 sc1\n"
"v_mfma_f32_16x16x32_bf16 v[72:75], a[84:87], a[4:7], v[72:75]\n"
"ds_read_b128 a[56:59], v30 offset:18432\n"
"ds_read_b128 a[76:79], v31 offset:18432\n"
"v_mfma_f32_16x16x32_bf16 v[72:75], a[100:103], a[24:27], v[72:75]\n"
"v_mfma_f32_16x16x32_bf16 v[76:79], a[84:87], a[8:11], v[76:79]\n"
"buffer_load_dwordx4 a[152:155], v1, s[36:39], s44 offen offset:2048 sc1\n"
"v_mfma_f32_16x16x32_bf16 v[76:79], a[100:103], a[28:31], v[76:79]\n"
"v_mfma_f32_16x16x32_bf16 v[80:83], a[84:87], a[12:15], v[80:83]\n"
"v_mfma_f32_16x16x32_bf16 v[80:83], a[100:103], a[32:35], v[80:83]\n"
"buffer_load_dwordx4 a[156:159], v1, s[36:39], s44 offen offset:3072 sc1\n"
"v_mfma_f32_16x16x32_bf16 v[84:87], a[84:87], a[16:19], v[84:87]\n"
"v_mfma_f32_16x16x32_bf16 v[84:87], a[100:103], a[36:39], v[84:87]\n"
"v_mfma_f32_16x16x32_bf16 v[48:51], a[80:83], a[0:3], v[48:51]\n"
"s_addk_i32 s44, 4096\n"
"buffer_load_dwordx4 a[160:163], v1, s[36:39], s44 offen offset:0 sc1\n"
"v_mfma_f32_16x16x32_bf16 v[48:51], a[96:99], a[20:23], v[48:51]\n"
"v_mfma_f32_16x16x32_bf16 v[52:55], a[80:83], a[4:7], v[52:55]\n"
"v_mfma_f32_16x16x32_bf16 v[52:55], a[96:99], a[24:27], v[52:55]\n"
"buffer_load_dwordx4 a[164:167], v1, s[36:39], s44 offen offset:1024 sc1\n"
"v_mfma_f32_16x16x32_bf16 v[56:59], a[80:83], a[8:11], v[56:59]\n"
"v_mfma_f32_16x16x32_bf16 v[56:59], a[96:99], a[28:31], v[56:59]\n"
"v_mfma_f32_16x16x32_bf16 v[60:63], a[80:83], a[12:15], v[60:63]\n"
"buffer_load_dwordx4 a[168:171], v1, s[36:39], s44 offen offset:2048 sc1\n"
"v_mfma_f32_16x16x32_bf16 v[60:63], a[96:99], a[32:35], v[60:63]\n"
"v_mfma_f32_16x16x32_bf16 v[64:67], a[80:83], a[16:19], v[64:67]\n"
"s_add_u32 m0, s8, 9216\n"
"v_mfma_f32_16x16x32_bf16 v[64:67], a[96:99], a[36:39], v[64:67]\n"
"s_add_u32 s13, s70, 82944\n"
"buffer_load_dwordx4 v3, s[24:27], s13 offen lds\n"
"v_mfma_f32_16x16x32_bf16 v[88:91], a[0:3], a[88:91], v[88:91]\n"
"v_mfma_f32_16x16x32_bf16 v[88:91], a[20:23], a[104:107], v[88:91]\n"
"s_add_u32 m0, s8, 10240\n"
"v_mfma_f32_16x16x32_bf16 v[92:95], a[4:7], a[88:91], v[92:95]\n"
"s_add_u32 s13, s70, 92160\n"
"buffer_load_dwordx4 v3, s[24:27], s13 offen lds\n"
"v_mfma_f32_16x16x32_bf16 v[92:95], a[24:27], a[104:107], v[92:95]\n"
"v_mfma_f32_16x16x32_bf16 v[96:99], a[8:11], a[88:91], v[96:99]\n"
"s_add_u32 m0, s8, 11264\n"
"v_mfma_f32_16x16x32_bf16 v[96:99], a[28:31], a[104:107], v[96:99]\n"
"s_add_u32 s13, s70, 101376\n"
"buffer_load_dwordx4 v3, s[24:27], s13 offen lds\n"
"v_mfma_f32_16x16x32_bf16 v[100:103], a[12:15], a[88:91], v[100:103]\n"
"v_mfma_f32_16x16x32_bf16 v[100:103], a[32:35], a[104:107], v[100:103]\n"
"v_mfma_f32_16x16x32_bf16 v[104:107], a[16:19], a[88:91], v[104:107]\n"
"v_mfma_f32_16x16x32_bf16 v[104:107], a[36:39], a[104:107], v[104:107]\n"
"s_waitcnt vmcnt(10) lgkmcnt(0)\n"
"s_barrier\n"
"v_mfma_f32_16x16x32_bf16 v[108:111], a[124:127], a[40:43], v[108:111]\n"
"s_mul_i32 s10, s6, 2560\n"
"ds_read_b128 a[0:3], v30 offset:20480\n"
"ds_read_b128 a[20:23], v31 offset:20480\n"
"v_mfma_f32_16x16x32_bf16 v[112:115], a[124:127], a[44:47], v[112:115]\n"
"s_xor_b32 s11, s6, 1\n"
"ds_read_b128 a[4:7], v30 offset:22528\n"
"ds_read_b128 a[24:27], v31 offset:22528\n"
"v_mfma_f32_16x16x32_bf16 v[116:119], a[124:127], a[48:51], v[116:119]\n"
"s_mul_i32 s11, s11, 2560\n"
"ds_read_b128 a[8:11], v30 offset:24576\n"
"ds_read_b128 a[28:31], v31 offset:24576\n"
"v_mfma_f32_16x16x32_bf16 v[120:123], a[124:127], a[52:55], v[120:123]\n"
"s_add_u32 s10, s10, 129024\n"
"ds_read_b128 a[12:15], v30 offset:26624\n"
"ds_read_b128 a[32:35], v31 offset:26624\n"
"v_mfma_f32_16x16x32_bf16 v[124:127], a[124:127], a[56:59], v[124:127]\n"
"s_add_u32 s11, s11, 129024\n"
"ds_read_b128 a[16:19], v30 offset:28672\n"
"ds_read_b128 a[36:39], v31 offset:28672\n"
"v_mfma_f32_16x16x32_bf16 v[68:71], a[116:119], a[40:43], v[68:71]\n"
"s_add_u32 m0, s8, 12288\n"
"s_add_u32 s13, s70, 110592\n"
"buffer_load_dwordx4 v3, s[24:27], s13 offen lds\n"
"v_mfma_f32_16x16x32_bf16 v[68:71], a[132:135], a[60:63], v[68:71]\n"
"s_add_u32 m0, s8, 13312\n"
"v_add_u32 v33, s10, v2\n"
"v_add_u32 v34, s11, v2\n"
"v_mfma_f32_16x16x32_bf16 v[72:75], a[116:119], a[44:47], v[72:75]\n"
"s_add_u32 s13, s70, 119808\n"
"buffer_load_dwordx4 v3, s[24:27], s13 offen lds\n"
"v_mfma_f32_16x16x32_bf16 v[72:75], a[132:135], a[64:67], v[72:75]\n"
"v_mfma_f32_16x16x32_bf16 v[76:79], a[116:119], a[48:51], v[76:79]\n"
"s_add_u32 m0, s8, 14336\n"
"v_mfma_f32_16x16x32_bf16 v[76:79], a[132:135], a[68:71], v[76:79]\n"
"s_add_u32 s13, s70, 129024\n"
"buffer_load_dwordx4 v3, s[24:27], s13 offen lds\n"
"v_mfma_f32_16x16x32_bf16 v[80:83], a[116:119], a[52:55], v[80:83]\n"
"s_add_u32 m0, s8, 15360\n"
"v_add_u32 v143, 97344, v8\n"
"ds_read2_b32 v[226:227], v179 offset0:0 offset1:128\n"
"v_mfma_f32_16x16x32_bf16 v[80:83], a[132:135], a[72:75], v[80:83]\n"
"s_add_u32 s13, s70, 138240\n"
"buffer_load_dwordx4 v3, s[24:27], s13 offen lds\n"
"v_mfma_f32_16x16x32_bf16 v[84:87], a[116:119], a[56:59], v[84:87]\n"
"ds_read2_b32 v[228:229], v179 offset0:16 offset1:144\n"
"ds_read2_b32 v[230:231], v179 offset0:32 offset1:160\n"
"v_mfma_f32_16x16x32_bf16 v[84:87], a[132:135], a[76:79], v[84:87]\n"
"s_waitcnt vmcnt(7) lgkmcnt(3)\n"
"ds_read2_b32 v[232:233], v179 offset0:48 offset1:176\n"
"ds_read2_b32 v[234:235], v179 offset0:64 offset1:192\n"
"ds_read_b128 v[236:239], v45 offset:0\n"
"ds_read_b128 v[240:243], v45 offset:64\n"
"v_mfma_f32_16x16x32_bf16 v[108:111], a[156:159], a[0:3], v[108:111]\n"
"s_add_u32 m0, s8, 16384\n"
"s_add_u32 s13, s70, 147456\n"
"buffer_load_dwordx4 v3, s[24:27], s13 offen lds\n"
"v_mfma_f32_16x16x32_bf16 v[112:115], a[156:159], a[4:7], v[112:115]\n"
"s_add_u32 m0, s8, 17408\n"
"ds_read_b128 v[244:247], v45 offset:128\n"
"ds_read_b128 v[248:251], v45 offset:192\n"
"v_mfma_f32_16x16x32_bf16 v[116:119], a[156:159], a[8:11], v[116:119]\n"
"s_add_u32 s13, s70, 156672\n"
"buffer_load_dwordx4 v3, s[24:27], s13 offen lds\n"
"v_mfma_f32_16x16x32_bf16 v[120:123], a[156:159], a[12:15], v[120:123]\n"
"ds_read_b128 v[252:255], v45 offset:256\n"
"ds_read_b128 v[148:151], v45 offset:512\n"
"v_mfma_f32_16x16x32_bf16 v[124:127], a[156:159], a[16:19], v[124:127]\n"
"s_add_u32 m0, s12, 0\n"
"ds_read_b128 v[152:155], v45 offset:576\n"
"ds_read_b128 v[156:159], v45 offset:640\n"
"v_mfma_f32_16x16x32_bf16 v[68:71], a[148:151], a[0:3], v[68:71]\n"
"s_add_u32 s13, s71, 0\n"
"buffer_load_dwordx4 v1, s[36:39], s13 offen sc1 lds\n"
"ds_read_b128 v[160:163], v45 offset:704\n"
"ds_read_b128 v[164:167], v45 offset:768\n"
"v_mfma_f32_16x16x32_bf16 v[68:71], a[164:167], a[20:23], v[68:71]\n"
"s_add_u32 m0, s12, 1024\n"
"ds_read_b32 v143, v143\n"
"v_mfma_f32_16x16x32_bf16 v[72:75], a[148:151], a[4:7], v[72:75]\n"
"s_add_u32 s13, s71, 1024\n"
"buffer_load_dwordx4 v1, s[36:39], s13 offen sc1 lds\n"
"v_mfma_f32_16x16x32_bf16 v[72:75], a[164:167], a[24:27], v[72:75]\n"
"ds_write_b64 v33, v[110:111] offset:0\n"
"v_mfma_f32_16x16x32_bf16 v[76:79], a[148:151], a[8:11], v[76:79]\n"
"s_add_u32 m0, s12, 2048\n"
"ds_write_b64 v33, v[114:115] offset:512\n"
"v_mfma_f32_16x16x32_bf16 v[76:79], a[164:167], a[28:31], v[76:79]\n"
"s_add_u32 s13, s71, 2048\n"
"buffer_load_dwordx4 v1, s[36:39], s13 offen sc1 lds\n"
"v_mfma_f32_16x16x32_bf16 v[80:83], a[148:151], a[12:15], v[80:83]\n"
"ds_write_b64 v33, v[118:119] offset:1024\n"
"ds_write_b64 v33, v[122:123] offset:1536\n"
"v_mfma_f32_16x16x32_bf16 v[80:83], a[164:167], a[32:35], v[80:83]\n"
"s_add_u32 m0, s12, 3072\n"
"ds_write_b64 v33, v[126:127] offset:2048\n"
"v_mfma_f32_16x16x32_bf16 v[84:87], a[148:151], a[16:19], v[84:87]\n"
"s_add_u32 s13, s71, 3072\n"
"buffer_load_dwordx4 v1, s[36:39], s13 offen sc1 lds\n"
"v_mfma_f32_16x16x32_bf16 v[84:87], a[164:167], a[36:39], v[84:87]\n"
"s_add_u32 m0, s12, 4096\n"
"v_mfma_f32_16x16x32_bf16 v[48:51], a[112:115], a[40:43], v[48:51]\n"
"s_add_u32 s13, s71, 4096\n"
"s_mov_b32 exec_hi, 0\n"
"buffer_load_dwordx4 v1, s[36:39], s13 offen sc1 lds\n"
"s_mov_b32 exec_hi, -1\n"
"v_mfma_f32_16x16x32_bf16 v[48:51], a[128:131], a[60:63], v[48:51]\n"
"s_waitcnt lgkmcnt(0)\n"
"s_barrier\n"
"ds_read_b64 v[110:111], v34 offset:0\n"
"ds_read_b64 v[114:115], v34 offset:512\n"
"ds_read_b64 v[118:119], v34 offset:1024\n"
"ds_read_b64 v[122:123], v34 offset:1536\n"
"ds_read_b64 v[126:127], v34 offset:2048\n"
"v_mfma_f32_16x16x32_bf16 v[52:55], a[112:115], a[44:47], v[52:55]\n"
"s_add_u32 m0, s73, 0\n"
"v_mfma_f32_16x16x32_bf16 v[52:55], a[128:131], a[64:67], v[52:55]\n"
"s_lshl_b32 s13, s5, 7\n"
"s_add_u32 s13, s13, s63\n"
"buffer_load_dwordx4 v3, s[16:19], s13 offen lds\n"
"s_waitcnt lgkmcnt(10)\n"
"v_mul_f32 v143, 0x3eae1a93, v143\n"
"v_mfma_f32_16x16x32_bf16 v[56:59], a[112:115], a[48:51], v[56:59]\n"
"s_add_u32 m0, s73, 1024\n"
"v_fmac_f32_dpp v72, v68, v228 row_newbcast:0 row_mask:0xf bank_mask:0xf\n"
"v_fmac_f32_dpp v73, v69, v228 row_newbcast:0 row_mask:0xf bank_mask:0xf\n"
"v_fmac_f32_dpp v74, v70, v228 row_newbcast:0 row_mask:0xf bank_mask:0xf\n"
"v_fmac_f32_dpp v75, v71, v228 row_newbcast:0 row_mask:0xf bank_mask:0xf\n"
"v_mfma_f32_16x16x32_bf16 v[56:59], a[128:131], a[68:71], v[56:59]\n"
"s_lshl_b32 s13, s5, 7\n"
"s_add_u32 s13, s13, s63\n"
"s_addk_i32 s13, 9216\n"
"buffer_load_dwordx4 v3, s[16:19], s13 offen lds\n"
"v_mul_f32 v72, v72, v229\n"
"v_mul_f32 v73, v73, v229\n"
"v_mfma_f32_16x16x32_bf16 v[60:63], a[112:115], a[52:55], v[60:63]\n"
"v_mul_f32 v74, v74, v229\n"
"v_mul_f32 v75, v75, v229\n"
"v_cvt_pk_bf16_f32 v200, v72, v73\n"
"v_cvt_pk_bf16_f32 v201, v74, v75\n"
"v_mfma_f32_16x16x32_bf16 v[60:63], a[128:131], a[72:75], v[60:63]\n"
"s_add_u32 m0, s73, 2048\n"
"v_fmac_f32_dpp v76, v68, v230 row_newbcast:0 row_mask:0xf bank_mask:0xf\n"
"v_fmac_f32_dpp v77, v69, v230 row_newbcast:0 row_mask:0xf bank_mask:0xf\n"
"v_fmac_f32_dpp v78, v70, v230 row_newbcast:0 row_mask:0xf bank_mask:0xf\n"
"v_fmac_f32_dpp v79, v71, v230 row_newbcast:0 row_mask:0xf bank_mask:0xf\n"
"v_mfma_f32_16x16x32_bf16 v[64:67], a[112:115], a[56:59], v[64:67]\n"
"s_lshr_b32 s13, s5, 1\n"
"s_lshl_b32 s13, s13, 5\n"
"s_add_u32 s13, s13, s63\n"
"s_addk_i32 s13, 1024\n"
"s_mov_b32 exec_hi, 0\n"
"buffer_load_dwordx4 v35, s[16:19], s13 offen lds\n"
"s_mov_b32 exec_hi, -1\n"
"v_mul_f32 v76, v76, v231\n"
"v_mul_f32 v77, v77, v231\n"
"v_mfma_f32_16x16x32_bf16 v[64:67], a[128:131], a[76:79], v[64:67]\n"
"s_add_u32 m0, s96, 0\n"
"v_mul_f32 v78, v78, v231\n"
"v_mul_f32 v79, v79, v231\n"
"v_cvt_pk_bf16_f32 v204, v76, v77\n"
"v_cvt_pk_bf16_f32 v205, v78, v79\n"
"v_mfma_f32_16x16x32_bf16 v[48:51], a[144:147], a[0:3], v[48:51]\n"
"s_add_u32 s13, s92, 0\n"
"buffer_load_dwordx4 v43, s[28:31], s13 offen lds\n"
"v_fmac_f32_dpp v80, v68, v232 row_newbcast:0 row_mask:0xf bank_mask:0xf\n"
"v_fmac_f32_dpp v81, v69, v232 row_newbcast:0 row_mask:0xf bank_mask:0xf\n"
"v_mfma_f32_16x16x32_bf16 v[48:51], a[160:163], a[20:23], v[48:51]\n"
"v_fmac_f32_dpp v82, v70, v232 row_newbcast:0 row_mask:0xf bank_mask:0xf\n"
"v_fmac_f32_dpp v83, v71, v232 row_newbcast:0 row_mask:0xf bank_mask:0xf\n"
"v_mul_f32 v80, v80, v233\n"
"v_mul_f32 v81, v81, v233\n"
"v_mfma_f32_16x16x32_bf16 v[52:55], a[144:147], a[4:7], v[52:55]\n"
"s_add_u32 m0, s96, 1024\n"
"v_mul_f32 v82, v82, v233\n"
"v_mul_f32 v83, v83, v233\n"
"v_cvt_pk_bf16_f32 v208, v80, v81\n"
"v_cvt_pk_bf16_f32 v209, v82, v83\n"
"v_mfma_f32_16x16x32_bf16 v[52:55], a[160:163], a[24:27], v[52:55]\n"
"s_add_u32 s13, s92, 73728\n"
"buffer_load_dwordx4 v43, s[28:31], s13 offen lds\n"
"v_fmac_f32_dpp v84, v68, v234 row_newbcast:0 row_mask:0xf bank_mask:0xf\n"
"v_fmac_f32_dpp v85, v69, v234 row_newbcast:0 row_mask:0xf bank_mask:0xf\n"
"v_mfma_f32_16x16x32_bf16 v[56:59], a[144:147], a[8:11], v[56:59]\n"
"s_add_u32 m0, s96, 2048\n"
"v_fmac_f32_dpp v86, v70, v234 row_newbcast:0 row_mask:0xf bank_mask:0xf\n"
"v_fmac_f32_dpp v87, v71, v234 row_newbcast:0 row_mask:0xf bank_mask:0xf\n"
"v_mul_f32 v84, v84, v235\n"
"v_mul_f32 v85, v85, v235\n"
"v_mfma_f32_16x16x32_bf16 v[56:59], a[160:163], a[28:31], v[56:59]\n"
"s_add_u32 s13, s92, 147456\n"
"buffer_load_dwordx4 v43, s[28:31], s13 offen lds\n"
"v_mul_f32 v86, v86, v235\n"
"v_mul_f32 v87, v87, v235\n"
"v_mfma_f32_16x16x32_bf16 v[60:63], a[144:147], a[12:15], v[60:63]\n"
"v_cvt_pk_bf16_f32 v212, v84, v85\n"
"v_cvt_pk_bf16_f32 v213, v86, v87\n"
"v_fmac_f32_dpp v68, v68, v226 row_newbcast:0 row_mask:0xf bank_mask:0xf\n"
"v_fmac_f32_dpp v69, v69, v226 row_newbcast:0 row_mask:0xf bank_mask:0xf\n"
"v_mfma_f32_16x16x32_bf16 v[60:63], a[160:163], a[32:35], v[60:63]\n"
"s_add_u32 m0, s96, 3072\n"
"v_fmac_f32_dpp v70, v70, v226 row_newbcast:0 row_mask:0xf bank_mask:0xf\n"
"v_fmac_f32_dpp v71, v71, v226 row_newbcast:0 row_mask:0xf bank_mask:0xf\n"
"v_mul_f32 v68, v68, v227\n"
"v_mul_f32 v69, v69, v227\n"
"v_mfma_f32_16x16x32_bf16 v[64:67], a[144:147], a[16:19], v[64:67]\n"
"s_add_u32 s13, s92, 221184\n"
"buffer_load_dwordx4 v43, s[28:31], s13 offen lds\n"
"v_mul_f32 v70, v70, v227\n"
"v_mul_f32 v71, v71, v227\n"
"v_mfma_f32_16x16x32_bf16 v[64:67], a[160:163], a[36:39], v[64:67]\n"
"v_cvt_pk_bf16_f32 v196, v68, v69\n"
"v_cvt_pk_bf16_f32 v197, v70, v71\n"
"s_waitcnt lgkmcnt(0)\n"
"v_add_f32 v108, v108, v110\n"
"v_add_f32 v109, v109, v111\n"
"v_mfma_f32_16x16x32_bf16 v[88:91], a[40:43], a[120:123], v[88:91]\n"
"v_add_f32 v112, v112, v114\n"
"v_add_f32 v113, v113, v115\n"
"v_add_f32 v116, v116, v118\n"
"v_add_f32 v117, v117, v119\n"
"v_mfma_f32_16x16x32_bf16 v[88:91], a[60:63], a[136:139], v[88:91]\n"
"v_add_f32 v120, v120, v122\n"
"v_add_f32 v121, v121, v123\n"
"v_add_f32 v124, v124, v126\n"
"v_add_f32 v125, v125, v127\n"
"v_fmac_f32_dpp v112, v108, v228 row_newbcast:0 row_mask:0xf bank_mask:0xf\n"
"v_fmac_f32_dpp v113, v109, v228 row_newbcast:0 row_mask:0xf bank_mask:0xf\n"
"v_mfma_f32_16x16x32_bf16 v[92:95], a[44:47], a[120:123], v[92:95]\n"
"v_fmac_f32_dpp v116, v108, v230 row_newbcast:0 row_mask:0xf bank_mask:0xf\n"
"v_fmac_f32_dpp v117, v109, v230 row_newbcast:0 row_mask:0xf bank_mask:0xf\n"
"v_fmac_f32_dpp v120, v108, v232 row_newbcast:0 row_mask:0xf bank_mask:0xf\n"
"v_fmac_f32_dpp v121, v109, v232 row_newbcast:0 row_mask:0xf bank_mask:0xf\n"
"v_mfma_f32_16x16x32_bf16 v[92:95], a[64:67], a[136:139], v[92:95]\n"
"v_fmac_f32_dpp v124, v108, v234 row_newbcast:0 row_mask:0xf bank_mask:0xf\n"
"v_fmac_f32_dpp v125, v109, v234 row_newbcast:0 row_mask:0xf bank_mask:0xf\n"
"v_fmac_f32_dpp v108, v108, v226 row_newbcast:0 row_mask:0xf bank_mask:0xf\n"
"v_fmac_f32_dpp v109, v109, v226 row_newbcast:0 row_mask:0xf bank_mask:0xf\n"
"v_mfma_f32_16x16x32_bf16 v[96:99], a[48:51], a[120:123], v[96:99]\n"
"v_mul_f32_dpp v112, v229, v112 quad_perm:[0,1,2,3] row_mask:0xe bank_mask:0xf\n"
"v_mul_f32_dpp v113, v229, v113 quad_perm:[0,1,2,3] row_mask:0xe bank_mask:0xf\n"
"v_mul_f32_dpp v116, v231, v116 quad_perm:[0,1,2,3] row_mask:0xe bank_mask:0xf\n"
"v_mul_f32_dpp v117, v231, v117 quad_perm:[0,1,2,3] row_mask:0xe bank_mask:0xf\n"
"v_mfma_f32_16x16x32_bf16 v[96:99], a[68:71], a[136:139], v[96:99]\n"
"v_mul_f32_dpp v120, v233, v120 quad_perm:[0,1,2,3] row_mask:0xe bank_mask:0xf\n"
"v_mul_f32_dpp v121, v233, v121 quad_perm:[0,1,2,3] row_mask:0xe bank_mask:0xf\n"
"v_mul_f32_dpp v124, v235, v124 quad_perm:[0,1,2,3] row_mask:0xe bank_mask:0xf\n"
"v_mul_f32_dpp v125, v235, v125 quad_perm:[0,1,2,3] row_mask:0xe bank_mask:0xf\n"
"v_mfma_f32_16x16x32_bf16 v[100:103], a[52:55], a[120:123], v[100:103]\n"
"v_mul_f32_dpp v108, v227, v108 quad_perm:[0,1,2,3] row_mask:0xe bank_mask:0xf\n"
"v_mul_f32_dpp v109, v227, v109 quad_perm:[0,1,2,3] row_mask:0xe bank_mask:0xf\n"
"v_cvt_pk_bf16_f32 v139, v112, v113\n"
"v_cvt_pk_bf16_f32 v140, v116, v117\n"
"v_mfma_f32_16x16x32_bf16 v[100:103], a[72:75], a[136:139], v[100:103]\n"
"v_cvt_pk_bf16_f32 v141, v120, v121\n"
"v_cvt_pk_bf16_f32 v142, v124, v125\n"
"v_cvt_pk_bf16_f32 v138, v108, v109\n"
"ds_write_b16 v40, v138 offset:0\n"
"v_mfma_f32_16x16x32_bf16 v[104:107], a[56:59], a[120:123], v[104:107]\n"
"ds_write_b16_d16_hi v40, v138 offset:160\n"
"ds_write_b16 v40, v139 offset:32\n"
"ds_write_b16_d16_hi v40, v139 offset:192\n"
"ds_write_b16 v40, v140 offset:64\n"
"v_mfma_f32_16x16x32_bf16 v[104:107], a[76:79], a[136:139], v[104:107]\n"
"ds_write_b16_d16_hi v40, v140 offset:224\n"
"ds_write_b16 v40, v141 offset:96\n"
"ds_write_b16_d16_hi v40, v141 offset:256\n"
"ds_write_b16 v40, v142 offset:128\n"
"v_mfma_f32_16x16x32_bf16 v[88:91], a[0:3], a[152:155], v[88:91]\n"
"ds_write_b16_d16_hi v40, v142 offset:288\n"
"v_mov_b32_dpp v198, v138 quad_perm:[0,1,2,3] row_mask:0x2 bank_mask:0xf\n"
"v_mov_b32_dpp v202, v139 quad_perm:[0,1,2,3] row_mask:0x2 bank_mask:0xf\n"
"v_mfma_f32_16x16x32_bf16 v[88:91], a[20:23], a[168:171], v[88:91]\n"
"v_mov_b32_dpp v206, v140 quad_perm:[0,1,2,3] row_mask:0x2 bank_mask:0xf\n"
"v_mov_b32_dpp v210, v141 quad_perm:[0,1,2,3] row_mask:0x2 bank_mask:0xf\n"
"v_mov_b32_dpp v214, v142 quad_perm:[0,1,2,3] row_mask:0x2 bank_mask:0xf\n"
"v_mfma_f32_16x16x32_bf16 v[92:95], a[4:7], a[152:155], v[92:95]\n"
"v_permlane16_swap_b32 v182, v139\n"
"v_permlane16_swap_b32 v186, v140\n"
"v_mfma_f32_16x16x32_bf16 v[92:95], a[24:27], a[168:171], v[92:95]\n"
"v_permlane16_swap_b32 v190, v141\n"
"v_permlane16_swap_b32 v194, v142\n"
"v_mfma_f32_16x16x32_bf16 v[96:99], a[8:11], a[152:155], v[96:99]\n"
"v_fmac_f32_dpp v52, v48, v228 row_newbcast:0 row_mask:0xf bank_mask:0xf\n"
"v_fmac_f32_dpp v53, v49, v228 row_newbcast:0 row_mask:0xf bank_mask:0xf\n"
"v_fmac_f32_dpp v54, v50, v228 row_newbcast:0 row_mask:0xf bank_mask:0xf\n"
"v_fmac_f32_dpp v55, v51, v228 row_newbcast:0 row_mask:0xf bank_mask:0xf\n"
"v_mfma_f32_16x16x32_bf16 v[96:99], a[28:31], a[168:171], v[96:99]\n"
"v_cvt_pk_bf16_f32 v180, v52, v53\n"
"v_cvt_pk_bf16_f32 v181, v54, v55\n"
"v_fmac_f32_dpp v56, v48, v230 row_newbcast:0 row_mask:0xf bank_mask:0xf\n"
"v_fmac_f32_dpp v57, v49, v230 row_newbcast:0 row_mask:0xf bank_mask:0xf\n"
"v_mfma_f32_16x16x32_bf16 v[100:103], a[12:15], a[152:155], v[100:103]\n"
"v_fmac_f32_dpp v58, v50, v230 row_newbcast:0 row_mask:0xf bank_mask:0xf\n"
"v_fmac_f32_dpp v59, v51, v230 row_newbcast:0 row_mask:0xf bank_mask:0xf\n"
"v_cvt_pk_bf16_f32 v184, v56, v57\n"
"v_cvt_pk_bf16_f32 v185, v58, v59\n"
"v_mfma_f32_16x16x32_bf16 v[100:103], a[32:35], a[168:171], v[100:103]\n"
"v_fmac_f32_dpp v60, v48, v232 row_newbcast:0 row_mask:0xf bank_mask:0xf\n"
"v_fmac_f32_dpp v61, v49, v232 row_newbcast:0 row_mask:0xf bank_mask:0xf\n"
"v_fmac_f32_dpp v62, v50, v232 row_newbcast:0 row_mask:0xf bank_mask:0xf\n"
"v_fmac_f32_dpp v63, v51, v232 row_newbcast:0 row_mask:0xf bank_mask:0xf\n"
"v_mfma_f32_16x16x32_bf16 v[104:107], a[16:19], a[152:155], v[104:107]\n"
"v_cvt_pk_bf16_f32 v188, v60, v61\n"
"v_cvt_pk_bf16_f32 v189, v62, v63\n"
"v_fmac_f32_dpp v64, v48, v234 row_newbcast:0 row_mask:0xf bank_mask:0xf\n"
"v_fmac_f32_dpp v65, v49, v234 row_newbcast:0 row_mask:0xf bank_mask:0xf\n"
"v_mfma_f32_16x16x32_bf16 v[104:107], a[36:39], a[168:171], v[104:107]\n"
"v_fmac_f32_dpp v66, v50, v234 row_newbcast:0 row_mask:0xf bank_mask:0xf\n"
"v_fmac_f32_dpp v67, v51, v234 row_newbcast:0 row_mask:0xf bank_mask:0xf\n"
"v_cvt_pk_bf16_f32 v192, v64, v65\n"
"v_cvt_pk_bf16_f32 v193, v66, v67\n"
"v_mfma_f32_16x16x32_bf16 v[48:51], v[200:203], v[180:183], 0\n"
"ds_read_b64 v[128:129], v41 offset:320\n"
"ds_read_b64 v[130:131], v41 offset:352\n"
"v_mfma_f32_16x16x32_bf16 v[68:71], v[196:199], v[180:183], v[144:147]\n"
"ds_read_b64 v[132:133], v41 offset:384\n"
"ds_read_b64 v[134:135], v41 offset:416\n"
"v_mfma_f32_16x16x32_bf16 v[52:55], v[204:207], v[184:187], 0\n"
"ds_read_b64 v[136:137], v41 offset:448\n"
"s_mov_b64 vcc, s[48:49]\n"
"v_mfma_f32_16x16x32_bf16 v[72:75], v[200:203], v[184:187], 0\n"
"v_mov_b32 v226, v88\n"
"v_mov_b32 v227, v88\n"
"v_mfma_f32_16x16x32_bf16 v[56:59], v[208:211], v[188:191], 0\n"
"s_nop 0\n"
"v_permlane16_swap_b32 v226, v227\n"
"v_mfma_f32_16x16x32_bf16 v[76:79], v[204:207], v[188:191], 0\n"
"v_mov_b32 v227, v226\n"
"s_nop 0\n"
"v_mfma_f32_16x16x32_bf16 v[60:63], v[212:215], v[192:195], 0\n"
"v_permlane32_swap_b32 v226, v227\n"
"v_mfma_f32_16x16x32_bf16 v[80:83], v[208:211], v[192:195], 0\n"
"v_pk_fma_f32 v[88:89], v[236:237], v[226:227], v[88:89] op_sel_hi:[1,0,1]\n"
"v_pk_mul_f32 v[88:89], v[88:89], v[148:149]\n"
"v_cvt_pk_bf16_f32 v216, v88, v89\n"
"v_pk_fma_f32 v[90:91], v[238:239], v[226:227], v[90:91] op_sel_hi:[1,0,1]\n"
"v_pk_mul_f32 v[90:91], v[90:91], v[150:151]\n"
"v_cvt_pk_bf16_f32 v217, v90, v91\n"
"v_pk_fma_f32 v[92:93], v[240:241], v[226:227], v[92:93] op_sel_hi:[1,0,1]\n"
"v_pk_mul_f32 v[92:93], v[92:93], v[152:153]\n"
"v_cvt_pk_bf16_f32 v218, v92, v93\n"
"v_pk_fma_f32 v[94:95], v[242:243], v[226:227], v[94:95] op_sel_hi:[1,0,1]\n"
"v_pk_mul_f32 v[94:95], v[94:95], v[154:155]\n"
"v_cvt_pk_bf16_f32 v219, v94, v95\n"
"v_pk_fma_f32 v[96:97], v[244:245], v[226:227], v[96:97] op_sel_hi:[1,0,1]\n"
"v_pk_mul_f32 v[96:97], v[96:97], v[156:157]\n"
"v_cvt_pk_bf16_f32 v220, v96, v97\n"
"v_pk_fma_f32 v[98:99], v[246:247], v[226:227], v[98:99] op_sel_hi:[1,0,1]\n"
"v_pk_mul_f32 v[98:99], v[98:99], v[158:159]\n"
"v_cvt_pk_bf16_f32 v221, v98, v99\n"
"v_pk_fma_f32 v[100:101], v[248:249], v[226:227], v[100:101] op_sel_hi:[1,0,1]\n"
"v_pk_mul_f32 v[100:101], v[100:101], v[160:161]\n"
"v_cvt_pk_bf16_f32 v222, v100, v101\n"
"v_pk_fma_f32 v[102:103], v[250:251], v[226:227], v[102:103] op_sel_hi:[1,0,1]\n"
"v_pk_mul_f32 v[102:103], v[102:103], v[162:163]\n"
"v_cvt_pk_bf16_f32 v223, v102, v103\n"
"v_pk_fma_f32 v[104:105], v[252:253], v[226:227], v[104:105] op_sel_hi:[1,0,1]\n"
"v_pk_mul_f32 v[104:105], v[104:105], v[164:165]\n"
"v_cvt_pk_bf16_f32 v224, v104, v105\n"
"v_pk_fma_f32 v[106:107], v[254:255], v[226:227], v[106:107] op_sel_hi:[1,0,1]\n"
"v_pk_mul_f32 v[106:107], v[106:107], v[166:167]\n"
"v_cvt_pk_bf16_f32 v225, v106, v107\n"
"v_cndmask_b32 v48, v48, v49, vcc\n"
"v_cndmask_b32 v68, v68, v69, vcc\n"
"v_cndmask_b32 v108, v68, v48, s[50:51]\n"
"v_cndmask_b32 v50, v50, v51, vcc\n"
"v_cndmask_b32 v70, v70, v71, vcc\n"
"v_cndmask_b32 v112, v70, v50, s[52:53]\n"
"v_cndmask_b32 v52, v52, v53, vcc\n"
"v_cndmask_b32 v72, v72, v73, vcc\n"
"v_cndmask_b32 v109, v72, v52, s[50:51]\n"
"v_cndmask_b32 v54, v54, v55, vcc\n"
"v_cndmask_b32 v74, v74, v75, vcc\n"
"v_cndmask_b32 v113, v74, v54, s[52:53]\n"
"v_cndmask_b32 v56, v56, v57, vcc\n"
"v_cndmask_b32 v76, v76, v77, vcc\n"
"v_cndmask_b32 v110, v76, v56, s[50:51]\n"
"v_cndmask_b32 v58, v58, v59, vcc\n"
"v_cndmask_b32 v78, v78, v79, vcc\n"
"v_cndmask_b32 v114, v78, v58, s[52:53]\n"
"v_cndmask_b32 v60, v60, v61, vcc\n"
"v_cndmask_b32 v80, v80, v81, vcc\n"
"v_cndmask_b32 v111, v80, v60, s[50:51]\n"
"v_cndmask_b32 v62, v62, v63, vcc\n"
"v_cndmask_b32 v82, v82, v83, vcc\n"
"v_cndmask_b32 v115, v82, v62, s[52:53]\n"
"v_permlane32_swap_b32 v108, v110\n"
"v_permlane32_swap_b32 v112, v114\n"
"v_permlane32_swap_b32 v109, v111\n"
"v_permlane32_swap_b32 v113, v115\n"
"s_nop 0\n"
"v_permlane16_swap_b32 v108, v109\n"
"v_permlane16_swap_b32 v112, v113\n"
"v_permlane16_swap_b32 v110, v111\n"
"v_permlane16_swap_b32 v114, v115\n"
"v_max3_f32 v116, v108, v109, v112\n"
"v_max3_f32 v117, v113, v110, v111\n"
"v_max3_f32 v116, v116, v114, v115\n"
"v_max_f32 v116, v116, v117\n"
"v_mov_b32 v117, v143\n"
"v_pk_add_f32 v[108:109], v[108:109], v[116:117] op_sel_hi:[1,0] neg_lo:[0,1] neg_hi:[0,1]\n"
"v_pk_add_f32 v[110:111], v[110:111], v[116:117] op_sel_hi:[1,0] neg_lo:[0,1] neg_hi:[0,1]\n"
"v_pk_add_f32 v[112:113], v[112:113], v[116:117] op_sel_hi:[1,0] neg_lo:[0,1] neg_hi:[0,1]\n"
"v_pk_add_f32 v[114:115], v[114:115], v[116:117] op_sel_hi:[1,0] neg_lo:[0,1] neg_hi:[0,1]\n"
"v_pk_mul_f32 v[108:109], v[108:109], v[116:117] op_sel:[0,1] op_sel_hi:[1,1]\n"
"v_pk_mul_f32 v[110:111], v[110:111], v[116:117] op_sel:[0,1] op_sel_hi:[1,1]\n"
"v_pk_mul_f32 v[112:113], v[112:113], v[116:117] op_sel:[0,1] op_sel_hi:[1,1]\n"
"v_pk_mul_f32 v[114:115], v[114:115], v[116:117] op_sel:[0,1] op_sel_hi:[1,1]\n"
"v_exp_f32 v108, v108\n"
"v_exp_f32 v109, v109\n"
"v_exp_f32 v110, v110\n"
"v_exp_f32 v111, v111\n"
"v_exp_f32 v112, v112\n"
"v_exp_f32 v113, v113\n"
"v_exp_f32 v114, v114\n"
"v_exp_f32 v115, v115\n"
"v_pk_add_f32 v[118:119], v[108:109], v[110:111]\n"
"v_pk_add_f32 v[116:117], v[112:113], v[114:115]\n"
"v_pk_add_f32 v[118:119], v[118:119], v[116:117]\n"
"v_add_f32 v118, v118, v119\n"
"v_rcp_f32 v118, v118\n"
"s_nop 0\n"
"v_pk_mul_f32 v[108:109], v[108:109], v[118:119] op_sel_hi:[1,0]\n"
"v_pk_mul_f32 v[110:111], v[110:111], v[118:119] op_sel_hi:[1,0]\n"
"v_pk_mul_f32 v[112:113], v[112:113], v[118:119] op_sel_hi:[1,0]\n"
"v_pk_mul_f32 v[114:115], v[114:115], v[118:119] op_sel_hi:[1,0]\n"
"v_cvt_pk_bf16_f32 v120, v108, v112\n"
"v_cvt_pk_bf16_f32 v122, v110, v114\n"
"v_cvt_pk_bf16_f32 v121, v109, v113\n"
"v_cvt_pk_bf16_f32 v123, v111, v115\n"
"v_permlane32_swap_b32 v120, v122\n"
"s_nop 0\n"
"v_permlane32_swap_b32 v121, v123\n"
"s_nop 1\n"
"v_permlane16_swap_b32 v120, v121\n"
"v_permlane16_swap_b32 v122, v123\n"
"s_waitcnt lgkmcnt(0)\n"
"v_perm_b32 v180, 0, v120, v36\n"
"v_perm_b32 v181, 0, v120, v37\n"
"v_perm_b32 v182, 0, v120, v38\n"
"v_perm_b32 v183, 0, v120, v39\n"
"v_perm_b32 v184, 0, v121, v36\n"
"v_perm_b32 v185, 0, v121, v37\n"
"v_perm_b32 v186, 0, v121, v38\n"
"v_perm_b32 v187, 0, v121, v39\n"
"v_mfma_f32_16x16x32_bf16 v[48:51], v[216:219], v[180:183], 0\n"
"v_perm_b32 v188, 0, v122, v36\n"
"v_perm_b32 v189, 0, v122, v37\n"
"v_perm_b32 v190, 0, v122, v38\n"
"v_perm_b32 v191, 0, v122, v39\n"
"v_mfma_f32_16x16x32_bf16 v[52:55], v[218:221], v[184:187], 0\n"
"v_perm_b32 v192, 0, v123, v36\n"
"v_perm_b32 v193, 0, v123, v37\n"
"v_perm_b32 v194, 0, v123, v38\n"
"v_perm_b32 v195, 0, v123, v39\n"
"v_mfma_f32_16x16x32_bf16 v[56:59], v[220:223], v[188:191], 0\n"
"s_nop 0\n"
"v_mfma_f32_16x16x32_bf16 v[60:63], v[222:225], v[192:195], 0\n"
"v_mfma_f32_16x16x32_bf16 v[68:71], v[128:131], v[180:183], 0\n"
"v_mfma_f32_16x16x32_bf16 v[72:75], v[130:133], v[184:187], 0\n"
"v_mfma_f32_16x16x32_bf16 v[76:79], v[132:135], v[188:191], 0\n"
"v_mfma_f32_16x16x32_bf16 v[80:83], v[134:137], v[192:195], 0\n"
"v_cvt_pk_bf16_f32 v246, v48, v49\n"
"v_cvt_pk_bf16_f32 v247, v50, v51\n"
"v_cvt_pk_bf16_f32 v248, v52, v53\n"
"v_cvt_pk_bf16_f32 v249, v54, v55\n"
"v_min_u32 v246, -2, v246\n"
"v_min_u32 v248, -2, v248\n"
"v_min_u32 v247, -2, v247\n"
"v_min_u32 v249, -2, v249\n"
"s_add_u32 s13, s56, 0\n"
"s_waitcnt vmcnt(63)\n"
"v_permlane32_swap_b32 v246, v248\n"
"v_permlane32_swap_b32 v247, v249\n"
"buffer_store_dwordx4 v[246:249], v42, s[36:39], s13 offen sc1\n"
"v_cvt_pk_bf16_f32 v250, v56, v57\n"
"v_cvt_pk_bf16_f32 v251, v58, v59\n"
"v_cvt_pk_bf16_f32 v252, v60, v61\n"
"v_cvt_pk_bf16_f32 v253, v62, v63\n"
"v_min_u32 v250, -2, v250\n"
"v_min_u32 v252, -2, v252\n"
"v_min_u32 v251, -2, v251\n"
"v_min_u32 v253, -2, v253\n"
"s_add_u32 s13, s56, 32768\n"
"v_permlane32_swap_b32 v250, v252\n"
"v_permlane32_swap_b32 v251, v253\n"
"buffer_store_dwordx4 v[250:253], v42, s[36:39], s13 offen sc1\n"
"v_cvt_pk_bf16_f32 v138, v68, v69\n"
"v_cvt_pk_bf16_f32 v139, v72, v73\n"
"v_cvt_pk_bf16_f32 v140, v76, v77\n"
"v_cvt_pk_bf16_f32 v141, v80, v81\n"
"v_mov_b32_dpp v138, v139 quad_perm:[0,1,2,3] row_mask:0x2 bank_mask:0xf\n"
"s_nop 0\n"
"v_mov_b32_dpp v140, v141 quad_perm:[0,1,2,3] row_mask:0x8 bank_mask:0xf\n"
"s_nop 1\n"
"v_mov_b32_dpp v138, v140 quad_perm:[0,1,2,3] row_mask:0xc bank_mask:0xf\n"
"v_min_u32 v254, -2, v138\n"
"buffer_store_dword v254, v8, s[36:39], s57 offen sc1\n"
"s_waitcnt vmcnt(3)\n"
"ds_read_b64_tr_b16 a[240:241], v168 offset:0\n"
"ds_read_b64_tr_b16 a[242:243], v168 offset:2048\n"
"ds_read_b64_tr_b16 a[244:245], v169 offset:0\n"
"ds_read_b64_tr_b16 a[246:247], v169 offset:2048\n"
"ds_read_b64_tr_b16 a[248:249], v170 offset:0\n"
"ds_read_b64_tr_b16 a[250:251], v170 offset:2048\n"
"ds_read_b64_tr_b16 a[252:253], v171 offset:0\n"
"ds_read_b64_tr_b16 a[254:255], v171 offset:2048\n"
"s_waitcnt lgkmcnt(0)\n"
"s_add_u32 s13, s93, 0\n"
"buffer_store_dwordx4 a[240:243], v1, s[36:39], s13 offen sc1\n"
"s_add_u32 s13, s93, 18432\n"
"buffer_store_dwordx4 a[244:247], v1, s[36:39], s13 offen sc1\n"
"s_add_u32 s13, s93, 36864\n"
"buffer_store_dwordx4 a[248:251], v1, s[36:39], s13 offen sc1\n"
"s_add_u32 s13, s93, 55296\n"
"buffer_store_dwordx4 a[252:255], v1, s[36:39], s13 offen sc1\n"
"buffer_store_dwordx4 v[172:175], v1, s[36:39], s64 offen offset:0 sc1\n"
"buffer_store_dwordx4 v[172:175], v1, s[36:39], s64 offen offset:1024 sc1\n"
"buffer_store_dwordx2 v[172:173], v176, s[36:39], s69 offen sc1\n"
"v_mov_b32 v28, v176\n"
"v_mov_b32 v29, v177\n"
"v_xor_b32 v133, 32, v47\n"
"v_xor_b32 v134, 64, v47\n"
"v_xor_b32 v135, 96, v47\n"
"v_mov_b32 v128, -1\n"
"v_mov_b32 v129, -1\n"
"v_mov_b32 v130, -1\n"
"v_mov_b32 v131, -1\n"
"s_waitcnt vmcnt(14)\n"
"s_barrier\n"
"ds_read_b64 v[236:237], v47 offset:0\n"
"ds_read_b64 v[238:239], v133 offset:0\n"
"ds_read_b64 v[240:241], v134 offset:0\n"
"ds_read_b64 v[242:243], v135 offset:0\n"
"ds_read_b64 v[244:245], v32 offset:2048\n"
"ds_read_b64_tr_b16 a[0:1], v20 offset:2048\n"
"ds_read_b64_tr_b16 a[2:3], v22 offset:6656\n"
"ds_read_b64_tr_b16 a[4:5], v21 offset:2048\n"
"ds_read_b64_tr_b16 a[6:7], v23 offset:6656\n"
"ds_read_b64_tr_b16 a[8:9], v22 offset:2048\n"
"ds_read_b64_tr_b16 a[10:11], v20 offset:6656\n"
"ds_read_b64_tr_b16 a[12:13], v23 offset:2048\n"
"ds_read_b64_tr_b16 a[14:15], v21 offset:6656\n"
"ds_read_b64_tr_b16 a[16:17], v25 offset:512\n"
"ds_read_b64_tr_b16 a[18:19], v25 offset:1664\n"
"s_waitcnt lgkmcnt(10)\n"
"v_lshlrev_b32 v216, 16, v236\n"
"v_and_b32 v217, s45, v236\n"
"v_lshlrev_b32 v218, 16, v237\n"
"v_and_b32 v219, s45, v237\n"
"v_lshlrev_b32 v220, 16, v238\n"
"v_and_b32 v221, s45, v238\n"
"v_lshlrev_b32 v222, 16, v239\n"
"v_and_b32 v223, s45, v239\n"
"v_lshlrev_b32 v224, 16, v240\n"
"v_and_b32 v225, s45, v240\n"
"v_lshlrev_b32 v226, 16, v241\n"
"v_and_b32 v227, s45, v241\n"
"v_lshlrev_b32 v228, 16, v242\n"
"v_and_b32 v229, s45, v242\n"
"v_lshlrev_b32 v230, 16, v243\n"
"v_and_b32 v231, s45, v243\n"
"v_lshlrev_b32 v232, 16, v244\n"
"v_and_b32 v233, s45, v244\n"
"v_lshlrev_b32 v234, 16, v245\n"
"v_and_b32 v235, s45, v245\n"
"ds_read_b64_tr_b16 a[20:21], v20 offset:38912\n"
"ds_read_b64_tr_b16 a[22:23], v22 offset:43520\n"
"ds_read_b64_tr_b16 a[24:25], v21 offset:38912\n"
"ds_read_b64_tr_b16 a[26:27], v23 offset:43520\n"
"ds_read_b64_tr_b16 a[28:29], v22 offset:38912\n"
"ds_read_b64_tr_b16 a[30:31], v20 offset:43520\n"
"ds_read_b64_tr_b16 a[32:33], v23 offset:38912\n"
"ds_read_b64_tr_b16 a[34:35], v21 offset:43520\n"
"ds_read_b64_tr_b16 a[36:37], v25 offset:9728\n"
"ds_read_b64_tr_b16 a[38:39], v25 offset:10880\n"
"ds_read_b64_tr_b16 a[40:41], v16 offset:0\n"
"ds_read_b64_tr_b16 a[42:43], v16 offset:1024\n"
"ds_read_b64_tr_b16 a[44:45], v17 offset:0\n"
"ds_read_b64_tr_b16 a[46:47], v17 offset:1024\n"
"ds_read_b64_tr_b16 a[48:49], v18 offset:0\n"
"ds_read_b64_tr_b16 a[50:51], v18 offset:1024\n"
"ds_read_b64_tr_b16 a[52:53], v19 offset:0\n"
"ds_read_b64_tr_b16 a[54:55], v19 offset:1024\n"
"ds_read_b64_tr_b16 a[56:57], v24 offset:0\n"
"ds_read_b64_tr_b16 a[58:59], v24 offset:256\n"
"ds_read_b64_tr_b16 a[60:61], v18 offset:4608\n"
"ds_read_b64_tr_b16 a[62:63], v18 offset:5632\n"
"ds_read_b64_tr_b16 a[64:65], v19 offset:4608\n"
"ds_read_b64_tr_b16 a[66:67], v19 offset:5632\n"
"ds_read_b64_tr_b16 a[68:69], v16 offset:4608\n"
"ds_read_b64_tr_b16 a[70:71], v16 offset:5632\n"
"ds_read_b64_tr_b16 a[72:73], v17 offset:4608\n"
"ds_read_b64_tr_b16 a[74:75], v17 offset:5632\n"
"ds_read_b64_tr_b16 a[76:77], v24 offset:1152\n"
"ds_read_b64_tr_b16 a[78:79], v24 offset:1408\n"
"ds_read_b64_tr_b16 a[80:81], v16 offset:9216\n"
"ds_read_b64_tr_b16 a[82:83], v16 offset:10240\n"
"ds_read_b64_tr_b16 a[84:85], v17 offset:9216\n"
"ds_read_b64_tr_b16 a[86:87], v17 offset:10240\n"
"ds_read_b64_tr_b16 a[88:89], v18 offset:9216\n"
"ds_read_b64_tr_b16 a[90:91], v18 offset:10240\n"
"ds_read_b64_tr_b16 a[92:93], v19 offset:9216\n"
"ds_read_b64_tr_b16 a[94:95], v19 offset:10240\n"
"ds_read_b64_tr_b16 a[96:97], v24 offset:2304\n"
"ds_read_b64_tr_b16 a[98:99], v24 offset:2560\n"
"ds_read_b64_tr_b16 a[100:101], v18 offset:13824\n"
"ds_read_b64_tr_b16 a[102:103], v18 offset:14848\n"
"ds_read_b64_tr_b16 a[104:105], v19 offset:13824\n"
"ds_read_b64_tr_b16 a[106:107], v19 offset:14848\n"
"ds_read_b64_tr_b16 a[108:109], v16 offset:13824\n"
"ds_read_b64_tr_b16 a[110:111], v16 offset:14848\n"
"ds_read_b64_tr_b16 a[112:113], v17 offset:13824\n"
"ds_read_b64_tr_b16 a[114:115], v17 offset:14848\n"
"ds_read_b64_tr_b16 a[116:117], v24 offset:3456\n"
"ds_read_b64_tr_b16 a[118:119], v24 offset:3712\n"
"ds_read_b64_tr_b16 a[120:121], v16 offset:18432\n"
"ds_read_b64_tr_b16 a[122:123], v16 offset:19456\n"
"ds_read_b64_tr_b16 a[124:125], v17 offset:18432\n"
"ds_read_b64_tr_b16 a[126:127], v17 offset:19456\n"
"ds_read_b64_tr_b16 a[128:129], v18 offset:18432\n"
"ds_read_b64_tr_b16 a[130:131], v18 offset:19456\n"
"ds_read_b64_tr_b16 a[132:133], v19 offset:18432\n"
"ds_read_b64_tr_b16 a[134:135], v19 offset:19456\n"
"ds_read_b64_tr_b16 a[136:137], v24 offset:4608\n"
"ds_read_b64_tr_b16 a[138:139], v24 offset:4864\n"
"ds_read_b64_tr_b16 a[140:141], v18 offset:23040\n"
"ds_read_b64_tr_b16 a[142:143], v18 offset:24064\n"
"ds_read_b64_tr_b16 a[144:145], v19 offset:23040\n"
"ds_read_b64_tr_b16 a[146:147], v19 offset:24064\n"
"ds_read_b64_tr_b16 a[148:149], v16 offset:23040\n"
"ds_read_b64_tr_b16 a[150:151], v16 offset:24064\n"
"ds_read_b64_tr_b16 a[152:153], v17 offset:23040\n"
"ds_read_b64_tr_b16 a[154:155], v17 offset:24064\n"
"ds_read_b64_tr_b16 a[156:157], v24 offset:5760\n"
"ds_read_b64_tr_b16 a[158:159], v24 offset:6016\n"
"ds_read_b64_tr_b16 a[160:161], v16 offset:27648\n"
"ds_read_b64_tr_b16 a[162:163], v16 offset:28672\n"
"ds_read_b64_tr_b16 a[164:165], v17 offset:27648\n"
"ds_read_b64_tr_b16 a[166:167], v17 offset:28672\n"
"ds_read_b64_tr_b16 a[168:169], v18 offset:27648\n"
"ds_read_b64_tr_b16 a[170:171], v18 offset:28672\n"
"ds_read_b64_tr_b16 a[172:173], v19 offset:27648\n"
"ds_read_b64_tr_b16 a[174:175], v19 offset:28672\n"
"ds_read_b64_tr_b16 a[176:177], v24 offset:6912\n"
"ds_read_b64_tr_b16 a[178:179], v24 offset:7168\n"
"ds_read_b64_tr_b16 a[180:181], v18 offset:32256\n"
"ds_read_b64_tr_b16 a[182:183], v18 offset:33280\n"
"ds_read_b64_tr_b16 a[184:185], v19 offset:32256\n"
"ds_read_b64_tr_b16 a[186:187], v19 offset:33280\n"
"ds_read_b64_tr_b16 a[188:189], v16 offset:32256\n"
"ds_read_b64_tr_b16 a[190:191], v16 offset:33280\n"
"ds_read_b64_tr_b16 a[192:193], v17 offset:32256\n"
"ds_read_b64_tr_b16 a[194:195], v17 offset:33280\n"
"ds_read_b64_tr_b16 a[196:197], v24 offset:8064\n"
"ds_read_b64_tr_b16 a[198:199], v24 offset:8320\n"
"v_add_u32 v16, 36864, v16\n"
"v_add_u32 v17, 36864, v17\n"
"v_add_u32 v18, 36864, v18\n"
"v_add_u32 v19, 36864, v19\n"
"ds_read_b64_tr_b16 a[200:201], v16 offset:0\n"
"ds_read_b64_tr_b16 a[202:203], v16 offset:1024\n"
"ds_read_b64_tr_b16 a[204:205], v17 offset:0\n"
"ds_read_b64_tr_b16 a[206:207], v17 offset:1024\n"
"ds_read_b64_tr_b16 a[208:209], v18 offset:0\n"
"buffer_load_dword v104, v26, s[36:39], s62 offen offset:0 sc1\n"
"buffer_load_dword v105, v26, s[36:39], s62 offen offset:256 sc1\n"
"buffer_load_dword v106, v26, s[36:39], s62 offen offset:512 sc1\n"
"buffer_load_dword v107, v26, s[36:39], s62 offen offset:768 sc1\n"
"buffer_load_dword v108, v27, s[36:39], s62 offen offset:0 sc1\n"
"buffer_load_dword v109, v27, s[36:39], s62 offen offset:256 sc1\n"
"buffer_load_dword v110, v27, s[36:39], s62 offen offset:512 sc1\n"
"buffer_load_dword v111, v27, s[36:39], s62 offen offset:768 sc1\n"
"buffer_load_dwordx4 v[40:43], v1, s[36:39], s58 offen offset:0 sc1\n"
"ds_read_b64_tr_b16 a[210:211], v18 offset:1024\n"
"ds_read_b64_tr_b16 a[212:213], v19 offset:0\n"
"ds_read_b64_tr_b16 a[214:215], v19 offset:1024\n"
"buffer_load_dwordx4 v[44:47], v1, s[36:39], s58 offen offset:1024 sc1\n"
"ds_read_b64_tr_b16 a[216:217], v24 offset:9216\n"
"ds_read_b64_tr_b16 a[218:219], v24 offset:9472\n"
"ds_read_b64_tr_b16 a[220:221], v18 offset:4608\n"
"buffer_load_dwordx4 v[48:51], v1, s[36:39], s58 offen offset:2048 sc1\n"
"ds_read_b64_tr_b16 a[222:223], v18 offset:5632\n"
"ds_read_b64_tr_b16 a[224:225], v19 offset:4608\n"
"ds_read_b64_tr_b16 a[226:227], v19 offset:5632\n"
"buffer_load_dwordx4 v[52:55], v1, s[36:39], s58 offen offset:3072 sc1\n"
"ds_read_b64_tr_b16 a[228:229], v16 offset:4608\n"
"ds_read_b64_tr_b16 a[230:231], v16 offset:5632\n"
"ds_read_b64_tr_b16 a[232:233], v17 offset:4608\n"
"buffer_load_dwordx4 v[56:59], v1, s[36:39], s59 offen offset:0 sc1\n"
"ds_read_b64_tr_b16 a[234:235], v17 offset:5632\n"
"ds_read_b64_tr_b16 a[236:237], v24 offset:10368\n"
"ds_read_b64_tr_b16 a[238:239], v24 offset:10624\n"
"buffer_load_dwordx4 v[60:63], v1, s[36:39], s59 offen offset:1024 sc1\n"
"ds_read_b64_tr_b16 a[240:241], v16 offset:9216\n"
"ds_read_b64_tr_b16 a[242:243], v16 offset:10240\n"
"ds_read_b64_tr_b16 a[244:245], v17 offset:9216\n"
"buffer_load_dwordx4 v[64:67], v1, s[36:39], s59 offen offset:2048 sc1\n"
"ds_read_b64_tr_b16 a[246:247], v17 offset:10240\n"
"ds_read_b64_tr_b16 a[248:249], v18 offset:9216\n"
"ds_read_b64_tr_b16 a[250:251], v18 offset:10240\n"
"buffer_load_dwordx4 v[68:71], v1, s[36:39], s59 offen offset:3072 sc1\n"
"ds_read_b64_tr_b16 a[252:253], v19 offset:9216\n"
"ds_read_b64_tr_b16 a[254:255], v19 offset:10240\n"
"ds_read_b64_tr_b16 v[152:153], v24 offset:11520\n"
"buffer_load_dwordx4 v[72:75], v1, s[36:39], s60 offen offset:0 sc1\n"
"ds_read_b64_tr_b16 v[154:155], v24 offset:11776\n"
"ds_read_b64_tr_b16 v[156:157], v18 offset:13824\n"
"ds_read_b64_tr_b16 v[158:159], v18 offset:14848\n"
"buffer_load_dwordx4 v[76:79], v1, s[36:39], s60 offen offset:1024 sc1\n"
"ds_read_b64_tr_b16 v[160:161], v19 offset:13824\n"
"ds_read_b64_tr_b16 v[162:163], v19 offset:14848\n"
"ds_read_b64_tr_b16 v[164:165], v16 offset:13824\n"
"buffer_load_dwordx4 v[80:83], v1, s[36:39], s60 offen offset:2048 sc1\n"
"ds_read_b64_tr_b16 v[166:167], v16 offset:14848\n"
"ds_read_b64_tr_b16 v[168:169], v17 offset:13824\n"
"ds_read_b64_tr_b16 v[170:171], v17 offset:14848\n"
"buffer_load_dwordx4 v[84:87], v1, s[36:39], s60 offen offset:3072 sc1\n"
"ds_read_b64_tr_b16 v[172:173], v24 offset:12672\n"
"ds_read_b64_tr_b16 v[174:175], v24 offset:12928\n"
"ds_read_b64_tr_b16 v[176:177], v16 offset:18432\n"
"buffer_load_dwordx4 v[88:91], v1, s[36:39], s61 offen offset:0 sc1\n"
"ds_read_b64_tr_b16 v[178:179], v16 offset:19456\n"
"ds_read_b64_tr_b16 v[180:181], v17 offset:18432\n"
"ds_read_b64_tr_b16 v[182:183], v17 offset:19456\n"
"buffer_load_dwordx4 v[92:95], v1, s[36:39], s61 offen offset:1024 sc1\n"
"ds_read_b64_tr_b16 v[184:185], v18 offset:18432\n"
"ds_read_b64_tr_b16 v[186:187], v18 offset:19456\n"
"ds_read_b64_tr_b16 v[188:189], v19 offset:18432\n"
"buffer_load_dwordx4 v[96:99], v1, s[36:39], s61 offen offset:2048 sc1\n"
"ds_read_b64_tr_b16 v[190:191], v19 offset:19456\n"
"ds_read_b64_tr_b16 v[192:193], v24 offset:13824\n"
"ds_read_b64_tr_b16 v[194:195], v24 offset:14080\n"
"buffer_load_dwordx4 v[100:103], v1, s[36:39], s61 offen offset:3072 sc1\n"
"ds_read_b64_tr_b16 v[196:197], v18 offset:23040\n"
"ds_read_b64_tr_b16 v[198:199], v18 offset:24064\n"
"ds_read_b64_tr_b16 v[200:201], v19 offset:23040\n"
"ds_read_b64_tr_b16 v[202:203], v19 offset:24064\n"
"ds_read_b64_tr_b16 v[204:205], v16 offset:23040\n"
"ds_read_b64_tr_b16 v[206:207], v16 offset:24064\n"
"ds_read_b64_tr_b16 v[208:209], v17 offset:23040\n"
"ds_read_b64_tr_b16 v[210:211], v17 offset:24064\n"
"ds_read_b64_tr_b16 v[212:213], v24 offset:14976\n"
"ds_read_b64_tr_b16 v[214:215], v24 offset:15232\n"
"s_waitcnt vmcnt(16) lgkmcnt(15)\n"
"v_max3_u32 v4, v104, v105, v106\n"
"v_max3_u32 v4, v4, v107, v108\n"
"v_max3_u32 v4, v4, v109, v110\n"
"v_max_u32 v4, v4, v111\n"
"v_cmp_eq_u32 vcc, -1, v4\n"
"s_nop 0\n"
"s_cbranch_vccnz LOP8_block_kernel_slow\n"
"LOP8_block_kernel_back:\n"
"v_mfma_f32_16x16x32_bf16 v[216:219], a[0:3], v[104:107], v[216:219]\n"
"s_lshl_b32 s96, s6, 13\n"
"s_add_u32 s96, s96, 129024\n"
"s_cmp_ge_u32 s99, 162\n"
"s_cbranch_scc1 LU2_block_kernel\n"
"s_add_u32 m0, s96, 4096\n"
"s_add_u32 s13, s97, 0\n"
"buffer_load_dwordx4 v3, s[32:35], s13 offen lds\n"
"s_add_u32 m0, s96, 5120\n"
"s_add_u32 s13, s97, 9216\n"
"buffer_load_dwordx4 v3, s[32:35], s13 offen lds\n"
"s_add_u32 m0, s96, 6144\n"
"s_add_u32 s13, s97, 18432\n"
"buffer_load_dwordx4 v3, s[32:35], s13 offen lds\n"
"s_add_u32 m0, s96, 7168\n"
"s_add_u32 s13, s97, 27648\n"
"buffer_load_dwordx4 v3, s[32:35], s13 offen lds\n"
"LU2_block_kernel:\n"
"s_add_u32 m0, s96, 0\n"
"s_add_u32 s13, s22, 0\n"
"buffer_load_dwordx4 v3, s[32:35], s13 offen lds\n"
"v_mfma_f32_16x16x32_bf16 v[216:219], a[20:23], v[108:111], v[216:219]\n"
"s_add_u32 m0, s96, 1024\n"
"s_add_u32 s13, s22, 9216\n"
"buffer_load_dwordx4 v3, s[32:35], s13 offen lds\n"
"v_mfma_f32_16x16x32_bf16 v[220:223], a[4:7], v[104:107], v[220:223]\n"
"v_mfma_f32_16x16x32_bf16 v[220:223], a[24:27], v[108:111], v[220:223]\n"
"s_add_u32 m0, s96, 2048\n"
"s_add_u32 s13, s22, 18432\n"
"buffer_load_dwordx4 v3, s[32:35], s13 offen lds\n"
"v_mfma_f32_16x16x32_bf16 v[224:227], a[8:11], v[104:107], v[224:227]\n"
"s_add_u32 m0, s96, 3072\n"
"s_add_u32 s13, s22, 27648\n"
"buffer_load_dwordx4 v3, s[32:35], s13 offen lds\n"
"v_mfma_f32_16x16x32_bf16 v[224:227], a[28:31], v[108:111], v[224:227]\n"
"v_mfma_f32_16x16x32_bf16 v[228:231], a[12:15], v[104:107], v[228:231]\n"
"v_mfma_f32_16x16x32_bf16 v[228:231], a[32:35], v[108:111], v[228:231]\n"
"v_mfma_f32_16x16x32_bf16 v[232:235], a[16:19], v[104:107], v[232:235]\n"
"v_mfma_f32_16x16x32_bf16 v[232:235], a[36:39], v[108:111], v[232:235]\n"
"s_waitcnt vmcnt(18) lgkmcnt(15)\n"
"v_max3_u32 v4, v40, v41, v42\n"
"v_max3_u32 v4, v4, v43, v44\n"
"v_max3_u32 v4, v4, v45, v46\n"
"v_max_u32 v4, v4, v47\n"
"v_cmp_eq_u32 vcc, -1, v4\n"
"s_nop 0\n"
"s_cbranch_vccnz LOG0_block_kernel_slow\n"
"LOG0_block_kernel_back:\n"
"v_mfma_f32_16x16x32_bf16 v[216:219], a[40:43], v[40:43], v[216:219]\n"
"ds_read_b64_tr_b16 a[0:1], v16 offset:27648\n"
"ds_read_b64_tr_b16 a[2:3], v16 offset:28672\n"
"v_mfma_f32_16x16x32_bf16 v[216:219], a[60:63], v[44:47], v[216:219]\n"
"ds_read_b64_tr_b16 a[4:5], v17 offset:27648\n"
"ds_read_b64_tr_b16 a[6:7], v17 offset:28672\n"
"v_mfma_f32_16x16x32_bf16 v[220:223], a[44:47], v[40:43], v[220:223]\n"
"v_mfma_f32_16x16x32_bf16 v[220:223], a[64:67], v[44:47], v[220:223]\n"
"v_mfma_f32_16x16x32_bf16 v[224:227], a[48:51], v[40:43], v[224:227]\n"
"v_mfma_f32_16x16x32_bf16 v[224:227], a[68:71], v[44:47], v[224:227]\n"
"v_mfma_f32_16x16x32_bf16 v[228:231], a[52:55], v[40:43], v[228:231]\n"
"s_waitcnt vmcnt(16) lgkmcnt(15)\n"
"v_max3_u32 v4, v48, v49, v50\n"
"v_max3_u32 v4, v4, v51, v52\n"
"v_mfma_f32_16x16x32_bf16 v[228:231], a[72:75], v[44:47], v[228:231]\n"
"v_max3_u32 v4, v4, v53, v54\n"
"v_max_u32 v4, v4, v55\n"
"v_mfma_f32_16x16x32_bf16 v[232:235], a[56:59], v[40:43], v[232:235]\n"
"v_cmp_eq_u32 vcc, -1, v4\n"
"v_mfma_f32_16x16x32_bf16 v[232:235], a[76:79], v[44:47], v[232:235]\n"
"s_cbranch_vccnz LOG1_block_kernel_slow\n"
"LOG1_block_kernel_back:\n"
"v_mfma_f32_16x16x32_bf16 v[216:219], a[80:83], v[48:51], v[216:219]\n"
"ds_read_b64_tr_b16 a[8:9], v18 offset:27648\n"
"ds_read_b64_tr_b16 a[10:11], v18 offset:28672\n"
"v_mfma_f32_16x16x32_bf16 v[216:219], a[100:103], v[52:55], v[216:219]\n"
"ds_read_b64_tr_b16 a[12:13], v19 offset:27648\n"
"ds_read_b64_tr_b16 a[14:15], v19 offset:28672\n"
"v_mfma_f32_16x16x32_bf16 v[220:223], a[84:87], v[48:51], v[220:223]\n"
"v_mfma_f32_16x16x32_bf16 v[220:223], a[104:107], v[52:55], v[220:223]\n"
"v_mfma_f32_16x16x32_bf16 v[224:227], a[88:91], v[48:51], v[224:227]\n"
"v_mfma_f32_16x16x32_bf16 v[224:227], a[108:111], v[52:55], v[224:227]\n"
"v_mfma_f32_16x16x32_bf16 v[228:231], a[92:95], v[48:51], v[228:231]\n"
"s_waitcnt vmcnt(14) lgkmcnt(15)\n"
"v_max3_u32 v4, v56, v57, v58\n"
"v_max3_u32 v4, v4, v59, v60\n"
"v_mfma_f32_16x16x32_bf16 v[228:231], a[112:115], v[52:55], v[228:231]\n"
"v_max3_u32 v4, v4, v61, v62\n"
"v_max_u32 v4, v4, v63\n"
"v_mfma_f32_16x16x32_bf16 v[232:235], a[96:99], v[48:51], v[232:235]\n"
"v_cmp_eq_u32 vcc, -1, v4\n"
"v_mfma_f32_16x16x32_bf16 v[232:235], a[116:119], v[52:55], v[232:235]\n"
"s_cbranch_vccnz LOG2_block_kernel_slow\n"
"LOG2_block_kernel_back:\n"
"v_mfma_f32_16x16x32_bf16 v[216:219], a[120:123], v[56:59], v[216:219]\n"
"ds_read_b64_tr_b16 a[16:17], v24 offset:16128\n"
"ds_read_b64_tr_b16 a[18:19], v24 offset:16384\n"
"v_mfma_f32_16x16x32_bf16 v[216:219], a[140:143], v[60:63], v[216:219]\n"
"ds_read_b64_tr_b16 a[20:21], v18 offset:32256\n"
"v_mfma_f32_16x16x32_bf16 v[220:223], a[124:127], v[56:59], v[220:223]\n"
"v_mfma_f32_16x16x32_bf16 v[220:223], a[144:147], v[60:63], v[220:223]\n"
"v_mfma_f32_16x16x32_bf16 v[224:227], a[128:131], v[56:59], v[224:227]\n"
"v_mfma_f32_16x16x32_bf16 v[224:227], a[148:151], v[60:63], v[224:227]\n"
"v_mfma_f32_16x16x32_bf16 v[228:231], a[132:135], v[56:59], v[228:231]\n"
"s_waitcnt vmcnt(12) lgkmcnt(15)\n"
"v_max3_u32 v4, v64, v65, v66\n"
"v_max3_u32 v4, v4, v67, v68\n"
"v_mfma_f32_16x16x32_bf16 v[228:231], a[152:155], v[60:63], v[228:231]\n"
"v_max3_u32 v4, v4, v69, v70\n"
"v_max_u32 v4, v4, v71\n"
"v_mfma_f32_16x16x32_bf16 v[232:235], a[136:139], v[56:59], v[232:235]\n"
"v_cmp_eq_u32 vcc, -1, v4\n"
"v_mfma_f32_16x16x32_bf16 v[232:235], a[156:159], v[60:63], v[232:235]\n"
"s_cbranch_vccnz LOG3_block_kernel_slow\n"
"LOG3_block_kernel_back:\n"
"v_mfma_f32_16x16x32_bf16 v[216:219], a[160:163], v[64:67], v[216:219]\n"
"ds_read_b64_tr_b16 a[22:23], v18 offset:33280\n"
"ds_read_b64_tr_b16 a[24:25], v19 offset:32256\n"
"v_mfma_f32_16x16x32_bf16 v[216:219], a[180:183], v[68:71], v[216:219]\n"
"ds_read_b64_tr_b16 a[26:27], v19 offset:33280\n"
"v_mfma_f32_16x16x32_bf16 v[220:223], a[164:167], v[64:67], v[220:223]\n"
"v_mfma_f32_16x16x32_bf16 v[220:223], a[184:187], v[68:71], v[220:223]\n"
"v_mfma_f32_16x16x32_bf16 v[224:227], a[168:171], v[64:67], v[224:227]\n"
"v_mfma_f32_16x16x32_bf16 v[224:227], a[188:191], v[68:71], v[224:227]\n"
"v_mfma_f32_16x16x32_bf16 v[228:231], a[172:175], v[64:67], v[228:231]\n"
"s_waitcnt vmcnt(10) lgkmcnt(15)\n"
"v_max3_u32 v4, v72, v73, v74\n"
"v_max3_u32 v4, v4, v75, v76\n"
"v_mfma_f32_16x16x32_bf16 v[228:231], a[192:195], v[68:71], v[228:231]\n"
"v_max3_u32 v4, v4, v77, v78\n"
"v_max_u32 v4, v4, v79\n"
"v_mfma_f32_16x16x32_bf16 v[232:235], a[176:179], v[64:67], v[232:235]\n"
"v_cmp_eq_u32 vcc, -1, v4\n"
"v_mfma_f32_16x16x32_bf16 v[232:235], a[196:199], v[68:71], v[232:235]\n"
"s_cbranch_vccnz LOG4_block_kernel_slow\n"
"LOG4_block_kernel_back:\n"
"v_mfma_f32_16x16x32_bf16 v[216:219], a[200:203], v[72:75], v[216:219]\n"
"s_add_u32 s44, s95, 0\n"
"buffer_load_dwordx4 v[112:115], v1, s[36:39], s44 offen offset:0 sc1\n"
"v_mfma_f32_16x16x32_bf16 v[216:219], a[220:223], v[76:79], v[216:219]\n"
"ds_read_b64_tr_b16 a[28:29], v16 offset:32256\n"
"ds_read_b64_tr_b16 a[30:31], v16 offset:33280\n"
"v_mfma_f32_16x16x32_bf16 v[220:223], a[204:207], v[72:75], v[220:223]\n"
"ds_read_b64_tr_b16 a[32:33], v17 offset:32256\n"
"v_mfma_f32_16x16x32_bf16 v[220:223], a[224:227], v[76:79], v[220:223]\n"
"buffer_load_dwordx4 v[116:119], v1, s[36:39], s44 offen offset:1024 sc1\n"
"v_mfma_f32_16x16x32_bf16 v[224:227], a[208:211], v[72:75], v[224:227]\n"
"v_mfma_f32_16x16x32_bf16 v[224:227], a[228:231], v[76:79], v[224:227]\n"
"v_mfma_f32_16x16x32_bf16 v[228:231], a[212:215], v[72:75], v[228:231]\n"
"s_waitcnt vmcnt(10) lgkmcnt(15)\n"
"v_max3_u32 v4, v80, v81, v82\n"
"v_max3_u32 v4, v4, v83, v84\n"
"v_mfma_f32_16x16x32_bf16 v[228:231], a[232:235], v[76:79], v[228:231]\n"
"v_max3_u32 v4, v4, v85, v86\n"
"v_max_u32 v4, v4, v87\n"
"v_mfma_f32_16x16x32_bf16 v[232:235], a[216:219], v[72:75], v[232:235]\n"
"v_cmp_eq_u32 vcc, -1, v4\n"
"v_mfma_f32_16x16x32_bf16 v[232:235], a[236:239], v[76:79], v[232:235]\n"
"s_cbranch_vccnz LOG5_block_kernel_slow\n"
"LOG5_block_kernel_back:\n"
"v_mfma_f32_16x16x32_bf16 v[216:219], a[240:243], v[80:83], v[216:219]\n"
"s_add_u32 s44, s95, 2048\n"
"buffer_load_dwordx4 v[120:123], v1, s[36:39], s44 offen offset:0 sc1\n"
"v_mfma_f32_16x16x32_bf16 v[216:219], v[156:159], v[84:87], v[216:219]\n"
"ds_read_b64_tr_b16 a[34:35], v17 offset:33280\n"
"ds_read_b64_tr_b16 a[36:37], v24 offset:17280\n"
"v_mfma_f32_16x16x32_bf16 v[220:223], a[244:247], v[80:83], v[220:223]\n"
"ds_read_b64_tr_b16 a[38:39], v24 offset:17536\n"
"v_mfma_f32_16x16x32_bf16 v[220:223], v[160:163], v[84:87], v[220:223]\n"
"buffer_load_dwordx4 v[124:127], v1, s[36:39], s44 offen offset:1024 sc1\n"
"v_mfma_f32_16x16x32_bf16 v[224:227], a[248:251], v[80:83], v[224:227]\n"
"v_mfma_f32_16x16x32_bf16 v[224:227], v[164:167], v[84:87], v[224:227]\n"
"v_mfma_f32_16x16x32_bf16 v[228:231], a[252:255], v[80:83], v[228:231]\n"
"s_waitcnt vmcnt(10) lgkmcnt(15)\n"
"v_max3_u32 v4, v88, v89, v90\n"
"v_max3_u32 v4, v4, v91, v92\n"
"v_mfma_f32_16x16x32_bf16 v[228:231], v[168:171], v[84:87], v[228:231]\n"
"v_max3_u32 v4, v4, v93, v94\n"
"v_max_u32 v4, v4, v95\n"
"v_mfma_f32_16x16x32_bf16 v[232:235], v[152:155], v[80:83], v[232:235]\n"
"v_cmp_eq_u32 vcc, -1, v4\n"
"v_mfma_f32_16x16x32_bf16 v[232:235], v[172:175], v[84:87], v[232:235]\n"
"s_cbranch_vccnz LOG6_block_kernel_slow\n"
"LOG6_block_kernel_back:\n"
"v_mfma_f32_16x16x32_bf16 v[216:219], v[176:179], v[88:91], v[216:219]\n"
"s_add_u32 s44, s95, 4096\n"
"buffer_load_dwordx4 v[128:131], v1, s[36:39], s44 offen offset:0 sc1\n"
"v_mfma_f32_16x16x32_bf16 v[216:219], v[196:199], v[92:95], v[216:219]\n"
"v_mfma_f32_16x16x32_bf16 v[220:223], v[180:183], v[88:91], v[220:223]\n"
"v_mfma_f32_16x16x32_bf16 v[220:223], v[200:203], v[92:95], v[220:223]\n"
"buffer_load_dwordx4 v[132:135], v1, s[36:39], s44 offen offset:1024 sc1\n"
"v_mfma_f32_16x16x32_bf16 v[224:227], v[184:187], v[88:91], v[224:227]\n"
"v_mfma_f32_16x16x32_bf16 v[224:227], v[204:207], v[92:95], v[224:227]\n"
"v_mfma_f32_16x16x32_bf16 v[228:231], v[188:191], v[88:91], v[228:231]\n"
"s_waitcnt vmcnt(10) lgkmcnt(0)\n"
"v_max3_u32 v4, v96, v97, v98\n"
"v_max3_u32 v4, v4, v99, v100\n"
"v_mfma_f32_16x16x32_bf16 v[228:231], v[208:211], v[92:95], v[228:231]\n"
"v_max3_u32 v4, v4, v101, v102\n"
"v_max_u32 v4, v4, v103\n"
"v_mfma_f32_16x16x32_bf16 v[232:235], v[192:195], v[88:91], v[232:235]\n"
"v_cmp_eq_u32 vcc, -1, v4\n"
"v_mfma_f32_16x16x32_bf16 v[232:235], v[212:215], v[92:95], v[232:235]\n"
"s_cbranch_vccnz LOG7_block_kernel_slow\n"
"LOG7_block_kernel_back:\n"
"v_mfma_f32_16x16x32_bf16 v[216:219], a[0:3], v[96:99], v[216:219]\n"
"s_lshr_b32 s11, s6, 1\n"
"s_add_u32 s44, s95, 6144\n"
"buffer_load_dwordx4 v[136:139], v1, s[36:39], s44 offen offset:0 sc1\n"
"v_mfma_f32_16x16x32_bf16 v[216:219], a[20:23], v[100:103], v[216:219]\n"
"s_cmp_eq_u32 s11, s7\n"
"s_cselect_b32 s11, -2, 0\n"
"v_mfma_f32_16x16x32_bf16 v[220:223], a[4:7], v[96:99], v[220:223]\n"
"v_mov_b32 v30, 0\n"
"v_mov_b32 v31, 0\n"
"v_mfma_f32_16x16x32_bf16 v[220:223], a[24:27], v[100:103], v[220:223]\n"
"buffer_load_dwordx4 v[140:143], v1, s[36:39], s44 offen offset:1024 sc1\n"
"v_mov_b32 v32, 0x3f803f80\n"
"v_mfma_f32_16x16x32_bf16 v[224:227], a[8:11], v[96:99], v[224:227]\n"
"v_mfma_f32_16x16x32_bf16 v[224:227], a[28:31], v[100:103], v[224:227]\n"
"v_mfma_f32_16x16x32_bf16 v[228:231], a[12:15], v[96:99], v[228:231]\n"
"v_mfma_f32_16x16x32_bf16 v[228:231], a[32:35], v[100:103], v[228:231]\n"
"v_mfma_f32_16x16x32_bf16 v[232:235], a[16:19], v[96:99], v[232:235]\n"
"v_mfma_f32_16x16x32_bf16 v[232:235], a[36:39], v[100:103], v[232:235]\n"
"s_waitcnt vmcnt(36)\n"
"v_cvt_pk_bf16_f32 v246, v216, v217\n"
"v_min_u32 v246, -2, v246\n"
"v_cvt_pk_bf16_f32 v247, v218, v219\n"
"v_min_u32 v247, -2, v247\n"
"v_cvt_pk_bf16_f32 v248, v220, v221\n"
"v_min_u32 v248, -2, v248\n"
"v_cvt_pk_bf16_f32 v249, v222, v223\n"
"v_min_u32 v249, -2, v249\n"
"buffer_store_dwordx4 v[246:249], v1, s[36:39], s64 offen offset:0 sc1\n"
"v_cvt_pk_bf16_f32 v250, v224, v225\n"
"v_min_u32 v250, -2, v250\n"
"v_cvt_pk_bf16_f32 v251, v226, v227\n"
"v_min_u32 v251, -2, v251\n"
"v_cvt_pk_bf16_f32 v252, v228, v229\n"
"v_min_u32 v252, -2, v252\n"
"v_cvt_pk_bf16_f32 v253, v230, v231\n"
"v_min_u32 v253, -2, v253\n"
"buffer_store_dwordx4 v[250:253], v1, s[36:39], s64 offen offset:1024 sc1\n"
"v_cvt_pk_bf16_f32 v254, v232, v233\n"
"v_min_u32 v254, s11, v254\n"
"v_cvt_pk_bf16_f32 v255, v234, v235\n"
"v_min_u32 v255, s11, v255\n"
"v_dot2c_f32_bf16 v30, v246, v32\n"
"v_dot2c_f32_bf16 v31, v246, v246\n"
"v_dot2c_f32_bf16 v30, v247, v32\n"
"v_dot2c_f32_bf16 v31, v247, v247\n"
"v_dot2c_f32_bf16 v30, v248, v32\n"
"v_dot2c_f32_bf16 v31, v248, v248\n"
"v_dot2c_f32_bf16 v30, v249, v32\n"
"v_dot2c_f32_bf16 v31, v249, v249\n"
"v_dot2c_f32_bf16 v30, v250, v32\n"
"v_dot2c_f32_bf16 v31, v250, v250\n"
"v_dot2c_f32_bf16 v30, v251, v32\n"
"v_dot2c_f32_bf16 v31, v251, v251\n"
"v_dot2c_f32_bf16 v30, v252, v32\n"
"v_dot2c_f32_bf16 v31, v252, v252\n"
"v_dot2c_f32_bf16 v30, v253, v32\n"
"v_dot2c_f32_bf16 v31, v253, v253\n"
"v_dot2c_f32_bf16 v30, v254, v32\n"
"v_dot2c_f32_bf16 v31, v254, v254\n"
"v_dot2c_f32_bf16 v30, v255, v32\n"
"v_dot2c_f32_bf16 v31, v255, v255\n"
"buffer_store_dwordx2 v[254:255], v28, s[36:39], s69 offen sc1\n"
"s_nop 1\n"
"v_permlane32_swap_b32 v30, v31\n"
"v_add_f32 v30, v30, v31\n"
"v_mov_b32 v31, v30\n"
"s_nop 1\n"
"v_permlane16_swap_b32 v30, v31\n"
"v_add_f32 v30, v30, v31\n"
"v_min_u32 v30, -2, v30\n"
"buffer_store_dword v30, v29, s[36:39], s69 offen offset:2048 sc1\n"
"v_and_b32 v33, 3, v0\n"
"v_lshlrev_b32 v33, 3, v33\n"
"v_lshrrev_b32 v34, 2, v0\n"
"v_lshl_add_u32 v33, v34, 7, v33\n"
"v_and_b32 v34, 3, v12\n"
"v_lshl_add_u32 v33, v34, 5, v33\n"
"v_add_u32 v216, s96, v33\n"
"v_xor_b32 v217, 32, v216\n"
"v_xor_b32 v218, 64, v216\n"
"v_xor_b32 v219, 96, v216\n"
"s_waitcnt vmcnt(12)\n"
"ds_read_b64_tr_b16 a[240:241], v216 offset:4096\n"
"ds_read_b64_tr_b16 a[242:243], v216 offset:6144\n"
"ds_read_b64_tr_b16 a[244:245], v217 offset:4096\n"
"ds_read_b64_tr_b16 a[246:247], v217 offset:6144\n"
"ds_read_b64_tr_b16 a[248:249], v218 offset:4096\n"
"ds_read_b64_tr_b16 a[250:251], v218 offset:6144\n"
"ds_read_b64_tr_b16 a[252:253], v219 offset:4096\n"
"ds_read_b64_tr_b16 a[254:255], v219 offset:6144\n"
"ds_read_b64_tr_b16 a[224:225], v216 offset:0\n"
"ds_read_b64_tr_b16 a[226:227], v216 offset:2048\n"
"ds_read_b64_tr_b16 a[228:229], v217 offset:0\n"
"ds_read_b64_tr_b16 a[230:231], v217 offset:2048\n"
"ds_read_b64_tr_b16 a[232:233], v218 offset:0\n"
"ds_read_b64_tr_b16 a[234:235], v218 offset:2048\n"
"ds_read_b64_tr_b16 a[236:237], v219 offset:0\n"
"ds_read_b64_tr_b16 a[238:239], v219 offset:2048\n"
"s_mov_b32 s4, s90\n"
"s_mov_b32 s5, s91\n"
"s_and_b32 s7, s5, 1\n"
"s_and_b32 s55, s4, 15\n"
"s_lshl_b32 s54, s7, 1\n"
"s_lshl_b32 s9, s4, 2\n"
"s_add_u32 s9, s9, s6\n"
"s_mul_i32 s63, s9, 18432\n"
"s_mul_i32 s9, s9, 19456\n"
"s_add_u32 s9, s9, 2490368\n"
"s_add_u32 s69, s9, 16384\n"
"s_add_u32 s10, s54, 0\n"
"s_and_b32 s10, s10, 3\n"
"s_lshl_b32 s10, s10, 12\n"
"s_add_u32 s65, s9, s10\n"
"s_add_u32 s10, s54, 1\n"
"s_and_b32 s10, s10, 3\n"
"s_lshl_b32 s10, s10, 12\n"
"s_add_u32 s66, s9, s10\n"
"s_add_u32 s10, s54, 2\n"
"s_and_b32 s10, s10, 3\n"
"s_lshl_b32 s10, s10, 12\n"
"s_add_u32 s67, s9, s10\n"
"s_add_u32 s10, s54, 3\n"
"s_and_b32 s10, s10, 3\n"
"s_lshl_b32 s10, s10, 12\n"
"s_add_u32 s68, s9, s10\n"
"s_lshl_b32 s10, s5, 11\n"
"s_add_u32 s21, s9, s10\n"
"v_mov_b32 v27, v35\n"
"v_and_b32 v33, 1, v14\n"
"v_lshlrev_b32 v33, 7, v33\n"
"v_lshrrev_b32 v34, 5, v0\n"
"v_lshl_add_u32 v33, v34, 6, v33\n"
"v_lshl_add_u32 v24, v15, 2, v33\n"
"s_lshr_b32 s8, s6, 1\n"
"s_lshl_b32 s8, s8, 12\n"
"s_and_b32 s9, s6, 1\n"
"s_lshl_b32 s9, s9, 3\n"
"s_add_u32 s8, s8, s9\n"
"v_add_u32 v16, s8, v1\n"
"v_add_u32 v17, 0x10000, v16\n"
"v_add_u32 v18, 0x10000, v1\n"
"v_bfe_u32 v33, v0, 1, 2\n"
"v_xor_b32 v33, s6, v33\n"
"v_lshlrev_b32 v33, 5, v33\n"
"v_lshl_add_u32 v33, v15, 7, v33\n"
"v_lshl_add_u32 v33, v14, 3, v33\n"
"v_add_u32 v19, 77824, v33\n"
"s_lshl_b32 s9, s6, 3\n"
"s_add_u32 s9, s9, 86016\n"
"v_lshl_add_u32 v34, v34, 4, v15\n"
"v_and_b32 v33, 1, v14\n"
"v_lshlrev_b32 v33, 2, v33\n"
"v_lshl_add_u32 v20, v34, 5, v33\n"
"v_add_u32 v20, s9, v20\n"
"s_lshl_b32 s9, s6, 4\n"
"v_add_u32 v34, s9, v15\n"
"v_mul_u32_u24 v22, 1280, v14\n"
"v_lshl_add_u32 v21, v34, 4, v22\n"
"v_lshl_add_u32 v22, v34, 2, v22\n"
"s_lshr_b32 s8, s6, 1\n"
"s_lshl_b32 s8, s8, 10\n"
"s_and_b32 s10, s6, 1\n"
"s_lshl_b32 s10, s10, 3\n"
"s_add_u32 s8, s8, s10\n"
"s_add_u32 s8, s8, 147456\n"
"v_add_u32 v25, s8, v1\n"
"s_mul_i32 s8, s7, 6144\n"
"s_add_u32 s8, s8, 149504\n"
"v_lshl_add_u32 v33, v34, 3, v33\n"
"v_lshrrev_b32 v34, 5, v0\n"
"v_mul_u32_u24 v34, 3072, v34\n"
"v_add3_u32 v26, v33, v34, s8\n"
"s_lshl_b32 s10, s5, 2\n"
"s_add_u32 s10, s10, s6\n"
"s_lshl_b32 s10, s10, 10\n"
"s_add_u32 s10, s10, 32768\n"
"v_lshl_add_u32 v9, v12, 7, s10\n"
"s_lshl_b32 s9, s6, 10\n"
"s_lshl_b32 s10, s54, 14\n"
"s_add_u32 s9, s9, s10\n"
"v_add_u32 v23, s9, v1\n"
"s_lshr_b32 s12, %2, 3\n"
"s_lshl_b32 s12, s12, 2\n"
"s_add_u32 s12, s12, s6\n"
"s_getpc_b64 s[10:11]\n"
"LPC2_block_kernel:\n"
"s_lshl_b32 s12, s12, 9\n"
"s_cmp_lt_u32 s12, LEND_block_kernel-LPC2_block_kernel-512\n"
"s_cselect_b32 s12, s12, 0\n"
"v_lshl_add_u32 v4, v14, 7, s12\n"
"global_load_dword v35, v4, s[10:11]\n"
"s_waitcnt vmcnt(44)\n"
"s_barrier\n"
"s_cmp_lg_u32 s6, 0\n"
"s_cbranch_scc1 LFB0_block_kernel\n"
"s_add_u32 s13, s20, 32768\n"
"s_mov_b32 exec_lo, 0xffff\n"
"s_mov_b32 exec_hi, 0\n"
"buffer_store_dwordx2 v[6:7], v2, s[36:39], s13 offen sc1\n"
"s_mov_b64 exec, -1\n"
"LFB0_block_kernel:\n"
"buffer_load_dword v36, v24, s[36:39], s69 offen offset:2048 sc1\n"
"buffer_load_dword v37, v24, s[36:39], s69 offen offset:2304 sc1\n"
"buffer_load_dword v38, v24, s[36:39], s69 offen offset:2560 sc1\n"
"buffer_load_dword v39, v24, s[36:39], s69 offen offset:2816 sc1\n"
"s_waitcnt vmcnt(0)\n"
"v_max3_u32 v4, v36, v37, v38\n"
"v_max_u32 v4, v4, v39\n"
"v_cmp_eq_u32 vcc, -1, v4\n"
"s_nop 0\n"
"s_cbranch_vccnz LHS_block_kernel_slow\n"
"LHS_block_kernel_back:\n"
"buffer_load_dwordx4 v[40:43], v1, s[36:39], s65 offen offset:0 sc1\n"
"buffer_load_dwordx4 v[44:47], v1, s[36:39], s65 offen offset:1024 sc1\n"
"buffer_load_dwordx4 v[48:51], v1, s[36:39], s65 offen offset:2048 sc1\n"
"buffer_load_dwordx4 v[52:55], v1, s[36:39], s65 offen offset:3072 sc1\n"
"buffer_load_dwordx4 v[56:59], v1, s[36:39], s66 offen offset:0 sc1\n"
"buffer_load_dwordx4 v[60:63], v1, s[36:39], s66 offen offset:1024 sc1\n"
"buffer_load_dwordx4 v[64:67], v1, s[36:39], s66 offen offset:2048 sc1\n"
"buffer_load_dwordx4 v[68:71], v1, s[36:39], s66 offen offset:3072 sc1\n"
"v_add_f32 v36, v36, v37\n"
"v_add_f32 v38, v38, v39\n"
"v_add_f32 v36, v36, v38\n"
"v_mov_b32 v37, v36\n"
"s_nop 1\n"
"v_permlane16_swap_b32 v36, v37\n"
"v_add_f32 v36, v36, v37\n"
"v_mov_b32 v37, v36\n"
"s_nop 1\n"
"v_permlane32_swap_b32 v36, v37\n"
"v_mul_f32 v28, 0x3ae38e39, v36\n"
"v_mul_f32 v29, 0x3ae38e39, v37\n"
"v_fma_f32 v29, -v28, v28, v29\n"
"v_max_f32 v29, 0, v29\n"
"v_add_f32 v29, 0x3727c5ac, v29\n"
"v_rsq_f32 v30, v29\n"
"s_nop 0\n"
"v_mul_f32 v30, 0xbfb8aa3b, v30\n"
"v_mul_f32_e64 v31, -v28, v30\n"
"s_waitcnt vmcnt(6)\n"
"v_max3_u32 v4, v40, v41, v42\n"
"v_max3_u32 v4, v4, v43, v44\n"
"v_max3_u32 v4, v4, v45, v46\n"
"v_max_u32 v4, v4, v47\n"
"v_cmp_eq_u32 vcc, -1, v4\n"
"s_nop 0\n"
"s_cbranch_vccnz LHG0_block_kernel_slow\n"
"LHG0_block_kernel_back:\n"
"buffer_load_dwordx4 v[72:75], v1, s[36:39], s67 offen offset:0 sc1\n"
"v_lshlrev_b32 v224, 16, v40\n"
"v_lshlrev_b32 v225, 16, v41\n"
"v_pk_fma_f32 v[224:225], v[224:225], v[30:31], v[30:31] op_sel:[0,0,1] op_sel_hi:[1,0,1]\n"
"v_and_b32 v40, s45, v40\n"
"v_and_b32 v41, s45, v41\n"
"v_pk_fma_f32 v[40:41], v[40:41], v[30:31], v[30:31] op_sel:[0,0,1] op_sel_hi:[1,0,1]\n"
"v_lshlrev_b32 v226, 16, v42\n"
"v_cvt_pk_bf16_f32 v40, v224, v40\n"
"v_lshlrev_b32 v227, 16, v43\n"
"v_cvt_pk_bf16_f32 v41, v225, v41\n"
"v_and_b32 v42, s45, v42\n"
"v_pk_fma_f32 v[226:227], v[226:227], v[30:31], v[30:31] op_sel:[0,0,1] op_sel_hi:[1,0,1]\n"
"v_and_b32 v43, s45, v43\n"
"v_pk_fma_f32 v[42:43], v[42:43], v[30:31], v[30:31] op_sel:[0,0,1] op_sel_hi:[1,0,1]\n"
"v_cvt_pk_bf16_f32 v42, v226, v42\n"
"v_cvt_pk_bf16_f32 v43, v227, v43\n"
"ds_write_b128 v23, v[40:43] offset:0\n"
"buffer_load_dwordx4 v[76:79], v1, s[36:39], s67 offen offset:1024 sc1\n"
"v_lshlrev_b32 v228, 16, v44\n"
"v_lshlrev_b32 v229, 16, v45\n"
"v_pk_fma_f32 v[228:229], v[228:229], v[30:31], v[30:31] op_sel:[0,0,1] op_sel_hi:[1,0,1]\n"
"v_and_b32 v44, s45, v44\n"
"v_and_b32 v45, s45, v45\n"
"v_pk_fma_f32 v[44:45], v[44:45], v[30:31], v[30:31] op_sel:[0,0,1] op_sel_hi:[1,0,1]\n"
"v_lshlrev_b32 v230, 16, v46\n"
"v_cvt_pk_bf16_f32 v44, v228, v44\n"
"v_lshlrev_b32 v231, 16, v47\n"
"v_cvt_pk_bf16_f32 v45, v229, v45\n"
"v_and_b32 v46, s45, v46\n"
"v_pk_fma_f32 v[230:231], v[230:231], v[30:31], v[30:31] op_sel:[0,0,1] op_sel_hi:[1,0,1]\n"
"v_and_b32 v47, s45, v47\n"
"v_pk_fma_f32 v[46:47], v[46:47], v[30:31], v[30:31] op_sel:[0,0,1] op_sel_hi:[1,0,1]\n"
"v_cvt_pk_bf16_f32 v46, v230, v46\n"
"v_cvt_pk_bf16_f32 v47, v231, v47\n"
"ds_write_b128 v23, v[44:47] offset:4096\n"
"s_waitcnt vmcnt(6)\n"
"v_max3_u32 v4, v48, v49, v50\n"
"v_max3_u32 v4, v4, v51, v52\n"
"v_max3_u32 v4, v4, v53, v54\n"
"v_max_u32 v4, v4, v55\n"
"v_cmp_eq_u32 vcc, -1, v4\n"
"s_nop 0\n"
"s_cbranch_vccnz LHG1_block_kernel_slow\n"
"LHG1_block_kernel_back:\n"
"buffer_load_dwordx4 v[80:83], v1, s[36:39], s67 offen offset:2048 sc1\n"
"v_lshlrev_b32 v224, 16, v48\n"
"v_lshlrev_b32 v225, 16, v49\n"
"v_pk_fma_f32 v[224:225], v[224:225], v[30:31], v[30:31] op_sel:[0,0,1] op_sel_hi:[1,0,1]\n"
"v_and_b32 v48, s45, v48\n"
"v_and_b32 v49, s45, v49\n"
"v_pk_fma_f32 v[48:49], v[48:49], v[30:31], v[30:31] op_sel:[0,0,1] op_sel_hi:[1,0,1]\n"
"v_lshlrev_b32 v226, 16, v50\n"
"v_cvt_pk_bf16_f32 v48, v224, v48\n"
"v_lshlrev_b32 v227, 16, v51\n"
"v_cvt_pk_bf16_f32 v49, v225, v49\n"
"v_and_b32 v50, s45, v50\n"
"v_pk_fma_f32 v[226:227], v[226:227], v[30:31], v[30:31] op_sel:[0,0,1] op_sel_hi:[1,0,1]\n"
"v_and_b32 v51, s45, v51\n"
"v_pk_fma_f32 v[50:51], v[50:51], v[30:31], v[30:31] op_sel:[0,0,1] op_sel_hi:[1,0,1]\n"
"v_cvt_pk_bf16_f32 v50, v226, v50\n"
"v_cvt_pk_bf16_f32 v51, v227, v51\n"
"ds_write_b128 v23, v[48:51] offset:8192\n"
"buffer_load_dwordx4 v[84:87], v1, s[36:39], s67 offen offset:3072 sc1\n"
"v_lshlrev_b32 v228, 16, v52\n"
"v_lshlrev_b32 v229, 16, v53\n"
"v_pk_fma_f32 v[228:229], v[228:229], v[30:31], v[30:31] op_sel:[0,0,1] op_sel_hi:[1,0,1]\n"
"v_and_b32 v52, s45, v52\n"
"v_and_b32 v53, s45, v53\n"
"v_pk_fma_f32 v[52:53], v[52:53], v[30:31], v[30:31] op_sel:[0,0,1] op_sel_hi:[1,0,1]\n"
"v_lshlrev_b32 v230, 16, v54\n"
"v_cvt_pk_bf16_f32 v52, v228, v52\n"
"v_lshlrev_b32 v231, 16, v55\n"
"v_cvt_pk_bf16_f32 v53, v229, v53\n"
"v_and_b32 v54, s45, v54\n"
"v_pk_fma_f32 v[230:231], v[230:231], v[30:31], v[30:31] op_sel:[0,0,1] op_sel_hi:[1,0,1]\n"
"v_and_b32 v55, s45, v55\n"
"v_pk_fma_f32 v[54:55], v[54:55], v[30:31], v[30:31] op_sel:[0,0,1] op_sel_hi:[1,0,1]\n"
"v_cvt_pk_bf16_f32 v54, v230, v54\n"
"v_cvt_pk_bf16_f32 v55, v231, v55\n"
"ds_write_b128 v23, v[52:55] offset:12288\n"
"s_waitcnt vmcnt(6)\n"
"v_max3_u32 v4, v56, v57, v58\n"
"v_max3_u32 v4, v4, v59, v60\n"
"v_max3_u32 v4, v4, v61, v62\n"
"v_max_u32 v4, v4, v63\n"
"v_cmp_eq_u32 vcc, -1, v4\n"
"s_nop 0\n"
"s_cbranch_vccnz LHG2_block_kernel_slow\n"
"LHG2_block_kernel_back:\n"
"v_add_u32 v23, 0x4000, v23\n"
"v_and_b32 v23, 0xffff, v23\n"
"buffer_load_dwordx4 v[88:91], v1, s[36:39], s68 offen offset:0 sc1\n"
"v_lshlrev_b32 v224, 16, v56\n"
"v_lshlrev_b32 v225, 16, v57\n"
"v_pk_fma_f32 v[224:225], v[224:225], v[30:31], v[30:31] op_sel:[0,0,1] op_sel_hi:[1,0,1]\n"
"v_and_b32 v56, s45, v56\n"
"v_and_b32 v57, s45, v57\n"
"v_pk_fma_f32 v[56:57], v[56:57], v[30:31], v[30:31] op_sel:[0,0,1] op_sel_hi:[1,0,1]\n"
"v_lshlrev_b32 v226, 16, v58\n"
"v_cvt_pk_bf16_f32 v56, v224, v56\n"
"v_lshlrev_b32 v227, 16, v59\n"
"v_cvt_pk_bf16_f32 v57, v225, v57\n"
"v_and_b32 v58, s45, v58\n"
"v_pk_fma_f32 v[226:227], v[226:227], v[30:31], v[30:31] op_sel:[0,0,1] op_sel_hi:[1,0,1]\n"
"v_and_b32 v59, s45, v59\n"
"v_pk_fma_f32 v[58:59], v[58:59], v[30:31], v[30:31] op_sel:[0,0,1] op_sel_hi:[1,0,1]\n"
"v_cvt_pk_bf16_f32 v58, v226, v58\n"
"v_cvt_pk_bf16_f32 v59, v227, v59\n"
"ds_write_b128 v23, v[56:59] offset:0\n"
"buffer_load_dwordx4 v[92:95], v1, s[36:39], s68 offen offset:1024 sc1\n"
"v_lshlrev_b32 v228, 16, v60\n"
"v_lshlrev_b32 v229, 16, v61\n"
"v_pk_fma_f32 v[228:229], v[228:229], v[30:31], v[30:31] op_sel:[0,0,1] op_sel_hi:[1,0,1]\n"
"v_and_b32 v60, s45, v60\n"
"v_and_b32 v61, s45, v61\n"
"v_pk_fma_f32 v[60:61], v[60:61], v[30:31], v[30:31] op_sel:[0,0,1] op_sel_hi:[1,0,1]\n"
"v_lshlrev_b32 v230, 16, v62\n"
"v_cvt_pk_bf16_f32 v60, v228, v60\n"
"v_lshlrev_b32 v231, 16, v63\n"
"v_cvt_pk_bf16_f32 v61, v229, v61\n"
"v_and_b32 v62, s45, v62\n"
"v_pk_fma_f32 v[230:231], v[230:231], v[30:31], v[30:31] op_sel:[0,0,1] op_sel_hi:[1,0,1]\n"
"v_and_b32 v63, s45, v63\n"
"v_pk_fma_f32 v[62:63], v[62:63], v[30:31], v[30:31] op_sel:[0,0,1] op_sel_hi:[1,0,1]\n"
"v_cvt_pk_bf16_f32 v62, v230, v62\n"
"v_cvt_pk_bf16_f32 v63, v231, v63\n"
"ds_write_b128 v23, v[60:63] offset:4096\n"
"s_waitcnt vmcnt(6)\n"
"v_max3_u32 v4, v64, v65, v66\n"
"v_max3_u32 v4, v4, v67, v68\n"
"v_max3_u32 v4, v4, v69, v70\n"
"v_max_u32 v4, v4, v71\n"
"v_cmp_eq_u32 vcc, -1, v4\n"
"s_nop 0\n"
"s_cbranch_vccnz LHG3_block_kernel_slow\n"
"LHG3_block_kernel_back:\n"
"buffer_load_dwordx4 v[96:99], v1, s[36:39], s68 offen offset:2048 sc1\n"
"v_lshlrev_b32 v224, 16, v64\n"
"v_lshlrev_b32 v225, 16, v65\n"
"v_pk_fma_f32 v[224:225], v[224:225], v[30:31], v[30:31] op_sel:[0,0,1] op_sel_hi:[1,0,1]\n"
"v_and_b32 v64, s45, v64\n"
"v_and_b32 v65, s45, v65\n"
"v_pk_fma_f32 v[64:65], v[64:65], v[30:31], v[30:31] op_sel:[0,0,1] op_sel_hi:[1,0,1]\n"
"v_lshlrev_b32 v226, 16, v66\n"
"v_cvt_pk_bf16_f32 v64, v224, v64\n"
"v_lshlrev_b32 v227, 16, v67\n"
"v_cvt_pk_bf16_f32 v65, v225, v65\n"
"v_and_b32 v66, s45, v66\n"
"v_pk_fma_f32 v[226:227], v[226:227], v[30:31], v[30:31] op_sel:[0,0,1] op_sel_hi:[1,0,1]\n"
"v_and_b32 v67, s45, v67\n"
"v_pk_fma_f32 v[66:67], v[66:67], v[30:31], v[30:31] op_sel:[0,0,1] op_sel_hi:[1,0,1]\n"
"v_cvt_pk_bf16_f32 v66, v226, v66\n"
"v_cvt_pk_bf16_f32 v67, v227, v67\n"
"ds_write_b128 v23, v[64:67] offset:8192\n"
"buffer_load_dwordx4 v[100:103], v1, s[36:39], s68 offen offset:3072 sc1\n"
"v_lshlrev_b32 v228, 16, v68\n"
"v_lshlrev_b32 v229, 16, v69\n"
"v_pk_fma_f32 v[228:229], v[228:229], v[30:31], v[30:31] op_sel:[0,0,1] op_sel_hi:[1,0,1]\n"
"v_and_b32 v68, s45, v68\n"
"v_and_b32 v69, s45, v69\n"
"v_pk_fma_f32 v[68:69], v[68:69], v[30:31], v[30:31] op_sel:[0,0,1] op_sel_hi:[1,0,1]\n"
"v_lshlrev_b32 v230, 16, v70\n"
"v_cvt_pk_bf16_f32 v68, v228, v68\n"
"v_lshlrev_b32 v231, 16, v71\n"
"v_cvt_pk_bf16_f32 v69, v229, v69\n"
"v_and_b32 v70, s45, v70\n"
"v_pk_fma_f32 v[230:231], v[230:231], v[30:31], v[30:31] op_sel:[0,0,1] op_sel_hi:[1,0,1]\n"
"v_and_b32 v71, s45, v71\n"
"v_pk_fma_f32 v[70:71], v[70:71], v[30:31], v[30:31] op_sel:[0,0,1] op_sel_hi:[1,0,1]\n"
"v_cvt_pk_bf16_f32 v70, v230, v70\n"
"v_cvt_pk_bf16_f32 v71, v231, v71\n"
"ds_write_b128 v23, v[68:71] offset:12288\n"
"s_waitcnt vmcnt(6)\n"
"v_max3_u32 v4, v72, v73, v74\n"
"v_max3_u32 v4, v4, v75, v76\n"
"v_max3_u32 v4, v4, v77, v78\n"
"v_max_u32 v4, v4, v79\n"
"v_cmp_eq_u32 vcc, -1, v4\n"
"s_nop 0\n"
"s_cbranch_vccnz LHG4_block_kernel_slow\n"
"LHG4_block_kernel_back:\n"
"v_add_u32 v23, 0x4000, v23\n"
"v_and_b32 v23, 0xffff, v23\n"
"buffer_load_dwordx2 v[104:105], v2, s[36:39], s69 offen offset:0 sc1\n"
"buffer_load_dwordx2 v[106:107], v2, s[36:39], s69 offen offset:512 sc1\n"
"v_lshlrev_b32 v224, 16, v72\n"
"v_lshlrev_b32 v225, 16, v73\n"
"v_pk_fma_f32 v[224:225], v[224:225], v[30:31], v[30:31] op_sel:[0,0,1] op_sel_hi:[1,0,1]\n"
"v_and_b32 v72, s45, v72\n"
"v_and_b32 v73, s45, v73\n"
"v_pk_fma_f32 v[72:73], v[72:73], v[30:31], v[30:31] op_sel:[0,0,1] op_sel_hi:[1,0,1]\n"
"v_lshlrev_b32 v226, 16, v74\n"
"v_cvt_pk_bf16_f32 v72, v224, v72\n"
"v_lshlrev_b32 v227, 16, v75\n"
"v_cvt_pk_bf16_f32 v73, v225, v73\n"
"v_and_b32 v74, s45, v74\n"
"v_pk_fma_f32 v[226:227], v[226:227], v[30:31], v[30:31] op_sel:[0,0,1] op_sel_hi:[1,0,1]\n"
"v_and_b32 v75, s45, v75\n"
"v_pk_fma_f32 v[74:75], v[74:75], v[30:31], v[30:31] op_sel:[0,0,1] op_sel_hi:[1,0,1]\n"
"v_cvt_pk_bf16_f32 v74, v226, v74\n"
"v_cvt_pk_bf16_f32 v75, v227, v75\n"
"ds_write_b128 v23, v[72:75] offset:0\n"
"buffer_load_dwordx2 v[108:109], v2, s[36:39], s69 offen offset:1024 sc1\n"
"buffer_load_dwordx2 v[110:111], v2, s[36:39], s69 offen offset:1536 sc1\n"
"v_lshlrev_b32 v228, 16, v76\n"
"v_lshlrev_b32 v229, 16, v77\n"
"v_pk_fma_f32 v[228:229], v[228:229], v[30:31], v[30:31] op_sel:[0,0,1] op_sel_hi:[1,0,1]\n"
"v_and_b32 v76, s45, v76\n"
"v_and_b32 v77, s45, v77\n"
"v_pk_fma_f32 v[76:77], v[76:77], v[30:31], v[30:31] op_sel:[0,0,1] op_sel_hi:[1,0,1]\n"
"v_lshlrev_b32 v230, 16, v78\n"
"v_cvt_pk_bf16_f32 v76, v228, v76\n"
"v_lshlrev_b32 v231, 16, v79\n"
"v_cvt_pk_bf16_f32 v77, v229, v77\n"
"v_and_b32 v78, s45, v78\n"
"v_pk_fma_f32 v[230:231], v[230:231], v[30:31], v[30:31] op_sel:[0,0,1] op_sel_hi:[1,0,1]\n"
"v_and_b32 v79, s45, v79\n"
"v_pk_fma_f32 v[78:79], v[78:79], v[30:31], v[30:31] op_sel:[0,0,1] op_sel_hi:[1,0,1]\n"
"v_cvt_pk_bf16_f32 v78, v230, v78\n"
"v_cvt_pk_bf16_f32 v79, v231, v79\n"
"ds_write_b128 v23, v[76:79] offset:4096\n"
"s_waitcnt vmcnt(8)\n"
"v_max3_u32 v4, v80, v81, v82\n"
"v_max3_u32 v4, v4, v83, v84\n"
"v_max3_u32 v4, v4, v85, v86\n"
"v_max_u32 v4, v4, v87\n"
"v_cmp_eq_u32 vcc, -1, v4\n"
"s_nop 0\n"
"s_cbranch_vccnz LHG5_block_kernel_slow\n"
"LHG5_block_kernel_back:\n"
"s_add_u32 s44, s95, 8192\n"
"buffer_load_dwordx4 v[144:147], v1, s[36:39], s44 offen offset:0 sc1\n"
"buffer_load_dwordx4 v[148:151], v1, s[36:39], s44 offen offset:1024 sc1\n"
"v_lshlrev_b32 v224, 16, v80\n"
"v_lshlrev_b32 v225, 16, v81\n"
"v_pk_fma_f32 v[224:225], v[224:225], v[30:31], v[30:31] op_sel:[0,0,1] op_sel_hi:[1,0,1]\n"
"v_and_b32 v80, s45, v80\n"
"v_and_b32 v81, s45, v81\n"
"v_pk_fma_f32 v[80:81], v[80:81], v[30:31], v[30:31] op_sel:[0,0,1] op_sel_hi:[1,0,1]\n"
"v_lshlrev_b32 v226, 16, v82\n"
"v_cvt_pk_bf16_f32 v80, v224, v80\n"
"v_lshlrev_b32 v227, 16, v83\n"
"v_cvt_pk_bf16_f32 v81, v225, v81\n"
"v_and_b32 v82, s45, v82\n"
"v_pk_fma_f32 v[226:227], v[226:227], v[30:31], v[30:31] op_sel:[0,0,1] op_sel_hi:[1,0,1]\n"
"v_and_b32 v83, s45, v83\n"
"v_pk_fma_f32 v[82:83], v[82:83], v[30:31], v[30:31] op_sel:[0,0,1] op_sel_hi:[1,0,1]\n"
"v_cvt_pk_bf16_f32 v82, v226, v82\n"
"v_cvt_pk_bf16_f32 v83, v227, v83\n"
"ds_write_b128 v23, v[80:83] offset:8192\n"
"s_add_u32 s44, s95, 10240\n"
"buffer_load_dwordx4 v[152:155], v1, s[36:39], s44 offen offset:0 sc1\n"
"buffer_load_dwordx4 v[156:159], v1, s[36:39], s44 offen offset:1024 sc1\n"
"v_lshlrev_b32 v228, 16, v84\n"
"v_lshlrev_b32 v229, 16, v85\n"
"v_pk_fma_f32 v[228:229], v[228:229], v[30:31], v[30:31] op_sel:[0,0,1] op_sel_hi:[1,0,1]\n"
"v_and_b32 v84, s45, v84\n"
"v_and_b32 v85, s45, v85\n"
"v_pk_fma_f32 v[84:85], v[84:85], v[30:31], v[30:31] op_sel:[0,0,1] op_sel_hi:[1,0,1]\n"
"v_lshlrev_b32 v230, 16, v86\n"
"v_cvt_pk_bf16_f32 v84, v228, v84\n"
"v_lshlrev_b32 v231, 16, v87\n"
"v_cvt_pk_bf16_f32 v85, v229, v85\n"
"v_and_b32 v86, s45, v86\n"
"v_pk_fma_f32 v[230:231], v[230:231], v[30:31], v[30:31] op_sel:[0,0,1] op_sel_hi:[1,0,1]\n"
"v_and_b32 v87, s45, v87\n"
"v_pk_fma_f32 v[86:87], v[86:87], v[30:31], v[30:31] op_sel:[0,0,1] op_sel_hi:[1,0,1]\n"
"v_cvt_pk_bf16_f32 v86, v230, v86\n"
"v_cvt_pk_bf16_f32 v87, v231, v87\n"
"ds_write_b128 v23, v[84:87] offset:12288\n"
"s_waitcnt vmcnt(10)\n"
"v_max3_u32 v4, v88, v89, v90\n"
"v_max3_u32 v4, v4, v91, v92\n"
"v_max3_u32 v4, v4, v93, v94\n"
"v_max_u32 v4, v4, v95\n"
"v_cmp_eq_u32 vcc, -1, v4\n"
"s_nop 0\n"
"s_cbranch_vccnz LHG6_block_kernel_slow\n"
"LHG6_block_kernel_back:\n"
"v_add_u32 v23, 0x4000, v23\n"
"v_and_b32 v23, 0xffff, v23\n"
"buffer_load_dwordx2 v[10:11], v9, s[36:39], 0 offen sc1\n"
"s_add_u32 s44, s95, 12288\n"
"buffer_load_dwordx4 v[160:163], v1, s[36:39], s44 offen offset:0 sc1\n"
"buffer_load_dwordx4 v[164:167], v1, s[36:39], s44 offen offset:1024 sc1\n"
"v_lshlrev_b32 v224, 16, v88\n"
"v_lshlrev_b32 v225, 16, v89\n"
"v_pk_fma_f32 v[224:225], v[224:225], v[30:31], v[30:31] op_sel:[0,0,1] op_sel_hi:[1,0,1]\n"
"v_and_b32 v88, s45, v88\n"
"v_and_b32 v89, s45, v89\n"
"v_pk_fma_f32 v[88:89], v[88:89], v[30:31], v[30:31] op_sel:[0,0,1] op_sel_hi:[1,0,1]\n"
"v_lshlrev_b32 v226, 16, v90\n"
"v_cvt_pk_bf16_f32 v88, v224, v88\n"
"v_lshlrev_b32 v227, 16, v91\n"
"v_cvt_pk_bf16_f32 v89, v225, v89\n"
"v_and_b32 v90, s45, v90\n"
"v_pk_fma_f32 v[226:227], v[226:227], v[30:31], v[30:31] op_sel:[0,0,1] op_sel_hi:[1,0,1]\n"
"v_and_b32 v91, s45, v91\n"
"v_pk_fma_f32 v[90:91], v[90:91], v[30:31], v[30:31] op_sel:[0,0,1] op_sel_hi:[1,0,1]\n"
"v_cvt_pk_bf16_f32 v90, v226, v90\n"
"v_cvt_pk_bf16_f32 v91, v227, v91\n"
"ds_write_b128 v23, v[88:91] offset:0\n"
"s_add_u32 s44, s95, 14336\n"
"buffer_load_dwordx4 v[168:171], v1, s[36:39], s44 offen offset:0 sc1\n"
"buffer_load_dwordx4 v[172:175], v1, s[36:39], s44 offen offset:1024 sc1\n"
"v_lshlrev_b32 v228, 16, v92\n"
"v_lshlrev_b32 v229, 16, v93\n"
"v_pk_fma_f32 v[228:229], v[228:229], v[30:31], v[30:31] op_sel:[0,0,1] op_sel_hi:[1,0,1]\n"
"v_and_b32 v92, s45, v92\n"
"v_and_b32 v93, s45, v93\n"
"v_pk_fma_f32 v[92:93], v[92:93], v[30:31], v[30:31] op_sel:[0,0,1] op_sel_hi:[1,0,1]\n"
"v_lshlrev_b32 v230, 16, v94\n"
"v_cvt_pk_bf16_f32 v92, v228, v92\n"
"v_lshlrev_b32 v231, 16, v95\n"
"v_cvt_pk_bf16_f32 v93, v229, v93\n"
"v_and_b32 v94, s45, v94\n"
"v_pk_fma_f32 v[230:231], v[230:231], v[30:31], v[30:31] op_sel:[0,0,1] op_sel_hi:[1,0,1]\n"
"v_and_b32 v95, s45, v95\n"
"v_pk_fma_f32 v[94:95], v[94:95], v[30:31], v[30:31] op_sel:[0,0,1] op_sel_hi:[1,0,1]\n"
"v_cvt_pk_bf16_f32 v94, v230, v94\n"
"v_cvt_pk_bf16_f32 v95, v231, v95\n"
"ds_write_b128 v23, v[92:95] offset:4096\n"
"s_waitcnt vmcnt(13)\n"
"v_max3_u32 v4, v96, v97, v98\n"
"v_max3_u32 v4, v4, v99, v100\n"
"v_max3_u32 v4, v4, v101, v102\n"
"v_max_u32 v4, v4, v103\n"
"v_cmp_eq_u32 vcc, -1, v4\n"
"s_nop 0\n"
"s_cbranch_vccnz LHG7_block_kernel_slow\n"
"LHG7_block_kernel_back:\n"
"s_add_u32 s44, s95, 16384\n"
"buffer_load_dwordx4 v[176:179], v1, s[36:39], s44 offen offset:0 sc1\n"
"buffer_load_dwordx4 v[180:183], v1, s[36:39], s44 offen offset:1024 sc1\n"
"v_lshlrev_b32 v224, 16, v96\n"
"v_lshlrev_b32 v225, 16, v97\n"
"v_pk_fma_f32 v[224:225], v[224:225], v[30:31], v[30:31] op_sel:[0,0,1] op_sel_hi:[1,0,1]\n"
"v_and_b32 v96, s45, v96\n"
"v_and_b32 v97, s45, v97\n"
"v_pk_fma_f32 v[96:97], v[96:97], v[30:31], v[30:31] op_sel:[0,0,1] op_sel_hi:[1,0,1]\n"
"v_lshlrev_b32 v226, 16, v98\n"
"v_cvt_pk_bf16_f32 v96, v224, v96\n"
"v_lshlrev_b32 v227, 16, v99\n"
"v_cvt_pk_bf16_f32 v97, v225, v97\n"
"v_and_b32 v98, s45, v98\n"
"v_pk_fma_f32 v[226:227], v[226:227], v[30:31], v[30:31] op_sel:[0,0,1] op_sel_hi:[1,0,1]\n"
"v_and_b32 v99, s45, v99\n"
"v_pk_fma_f32 v[98:99], v[98:99], v[30:31], v[30:31] op_sel:[0,0,1] op_sel_hi:[1,0,1]\n"
"v_cvt_pk_bf16_f32 v98, v226, v98\n"
"v_cvt_pk_bf16_f32 v99, v227, v99\n"
"ds_write_b128 v23, v[96:99] offset:8192\n"
"s_add_u32 s44, s95, 73728\n"
"buffer_load_dwordx4 v[184:187], v1, s[36:39], s44 offen offset:0 sc1\n"
"buffer_load_dwordx4 v[188:191], v1, s[36:39], s44 offen offset:1024 sc1\n"
"v_lshlrev_b32 v228, 16, v100\n"
"v_lshlrev_b32 v229, 16, v101\n"
"v_pk_fma_f32 v[228:229], v[228:229], v[30:31], v[30:31] op_sel:[0,0,1] op_sel_hi:[1,0,1]\n"
"v_and_b32 v100, s45, v100\n"
"v_and_b32 v101, s45, v101\n"
"v_pk_fma_f32 v[100:101], v[100:101], v[30:31], v[30:31] op_sel:[0,0,1] op_sel_hi:[1,0,1]\n"
"v_lshlrev_b32 v230, 16, v102\n"
"v_cvt_pk_bf16_f32 v100, v228, v100\n"
"v_lshlrev_b32 v231, 16, v103\n"
"v_cvt_pk_bf16_f32 v101, v229, v101\n"
"v_and_b32 v102, s45, v102\n"
"v_pk_fma_f32 v[230:231], v[230:231], v[30:31], v[30:31] op_sel:[0,0,1] op_sel_hi:[1,0,1]\n"
"v_and_b32 v103, s45, v103\n"
"v_pk_fma_f32 v[102:103], v[102:103], v[30:31], v[30:31] op_sel:[0,0,1] op_sel_hi:[1,0,1]\n"
"v_cvt_pk_bf16_f32 v102, v230, v102\n"
"v_cvt_pk_bf16_f32 v103, v231, v103\n"
"ds_write_b128 v23, v[100:103] offset:12288\n"
"s_waitcnt vmcnt(13)\n"
"v_max3_u32 v4, v104, v105, v106\n"
"v_max3_u32 v4, v4, v107, v108\n"
"v_max3_u32 v4, v4, v109, v110\n"
"v_max_u32 v4, v4, v111\n"
"v_cmp_eq_u32 vcc, -1, v4\n"
"s_nop 0\n"
"s_cbranch_vccnz LHG8_block_kernel_slow\n"
"LHG8_block_kernel_back:\n"
"s_lshl_b32 s8, s6, 10\n"
"v_add_u32 v23, s8, v18\n"
"s_add_u32 s44, s95, 75776\n"
"buffer_load_dwordx4 v[192:195], v1, s[36:39], s44 offen offset:0 sc1\n"
"buffer_load_dwordx4 v[196:199], v1, s[36:39], s44 offen offset:1024 sc1\n"
"v_lshlrev_b32 v224, 16, v104\n"
"v_lshlrev_b32 v225, 16, v105\n"
"v_pk_fma_f32 v[224:225], v[224:225], v[30:31], v[30:31] op_sel:[0,0,1] op_sel_hi:[1,0,1]\n"
"v_and_b32 v104, s45, v104\n"
"v_and_b32 v105, s45, v105\n"
"v_pk_fma_f32 v[104:105], v[104:105], v[30:31], v[30:31] op_sel:[0,0,1] op_sel_hi:[1,0,1]\n"
"v_lshlrev_b32 v226, 16, v106\n"
"v_cvt_pk_bf16_f32 v104, v224, v104\n"
"v_lshlrev_b32 v227, 16, v107\n"
"v_cvt_pk_bf16_f32 v105, v225, v105\n"
"v_and_b32 v106, s45, v106\n"
"v_pk_fma_f32 v[226:227], v[226:227], v[30:31], v[30:31] op_sel:[0,0,1] op_sel_hi:[1,0,1]\n"
"v_and_b32 v107, s45, v107\n"
"v_pk_fma_f32 v[106:107], v[106:107], v[30:31], v[30:31] op_sel:[0,0,1] op_sel_hi:[1,0,1]\n"
"v_cvt_pk_bf16_f32 v106, v226, v106\n"
"v_cvt_pk_bf16_f32 v107, v227, v107\n"
"ds_write_b128 v23, v[104:107] offset:0\n"
"s_add_u32 s44, s95, 77824\n"
"buffer_load_dwordx4 v[200:203], v1, s[36:39], s44 offen offset:0 sc1\n"
"buffer_load_dwordx4 v[204:207], v1, s[36:39], s44 offen offset:1024 sc1\n"
"v_lshlrev_b32 v228, 16, v108\n"
"v_lshlrev_b32 v229, 16, v109\n"
"v_pk_fma_f32 v[228:229], v[228:229], v[30:31], v[30:31] op_sel:[0,0,1] op_sel_hi:[1,0,1]\n"
"v_and_b32 v108, s45, v108\n"
"v_and_b32 v109, s45, v109\n"
"v_pk_fma_f32 v[108:109], v[108:109], v[30:31], v[30:31] op_sel:[0,0,1] op_sel_hi:[1,0,1]\n"
"v_lshlrev_b32 v230, 16, v110\n"
"v_cvt_pk_bf16_f32 v108, v228, v108\n"
"v_lshlrev_b32 v231, 16, v111\n"
"v_cvt_pk_bf16_f32 v109, v229, v109\n"
"v_and_b32 v110, s45, v110\n"
"v_pk_fma_f32 v[230:231], v[230:231], v[30:31], v[30:31] op_sel:[0,0,1] op_sel_hi:[1,0,1]\n"
"v_and_b32 v111, s45, v111\n"
"v_pk_fma_f32 v[110:111], v[110:111], v[30:31], v[30:31] op_sel:[0,0,1] op_sel_hi:[1,0,1]\n"
"v_cvt_pk_bf16_f32 v110, v230, v110\n"
"v_cvt_pk_bf16_f32 v111, v231, v111\n"
"ds_write_b128 v23, v[108:111] offset:4096\n"
"s_waitcnt vmcnt(12)\n"
"v_cmp_ne_u64 vcc, v[10:11], v[6:7]\n"
"s_nop 0\n"
"s_cbranch_vccnz LF2_block_kernel_slow\n"
"LF2_block_kernel_back:\n"
"v_add_u32 v9, 32768, v9\n"
"s_waitcnt lgkmcnt(15)\n"
"s_cmp_ge_u32 s99, 162\n"
"s_cbranch_scc1 LU3_block_kernel\n"
"s_add_u32 s13, s98, 0\n"
"buffer_store_dwordx4 a[240:243], v1, s[36:39], s13 offen sc1\n"
"s_add_u32 s13, s98, 18432\n"
"buffer_store_dwordx4 a[244:247], v1, s[36:39], s13 offen sc1\n"
"s_add_u32 s13, s98, 36864\n"
"buffer_store_dwordx4 a[248:251], v1, s[36:39], s13 offen sc1\n"
"s_add_u32 s13, s98, 55296\n"
"buffer_store_dwordx4 a[252:255], v1, s[36:39], s13 offen sc1\n"
"LU3_block_kernel:\n"
"s_add_u32 s13, s23, 0\n"
"buffer_store_dwordx4 a[224:227], v1, s[36:39], s13 offen sc1\n"
"s_add_u32 s13, s23, 18432\n"
"buffer_store_dwordx4 a[228:231], v1, s[36:39], s13 offen sc1\n"
"s_add_u32 s13, s23, 36864\n"
"buffer_store_dwordx4 a[232:235], v1, s[36:39], s13 offen sc1\n"
"s_add_u32 s13, s23, 55296\n"
"buffer_store_dwordx4 a[236:239], v1, s[36:39], s13 offen sc1\n"
"s_waitcnt lgkmcnt(0)\n"
"s_barrier\n"
"ds_read_b128 a[0:3], v1 offset:0\n"
"ds_read_b128 a[4:7], v1 offset:1024\n"
"ds_read_b128 a[8:11], v1 offset:2048\n"
"ds_read_b128 a[12:15], v1 offset:3072\n"
"ds_read_b128 a[16:19], v1 offset:4096\n"
"ds_read_b128 a[20:23], v1 offset:5120\n"
"ds_read_b128 a[24:27], v1 offset:6144\n"
"ds_read_b128 a[28:31], v1 offset:7168\n"
"ds_read_b128 a[32:35], v1 offset:8192\n"
"ds_read_b128 a[36:39], v1 offset:9216\n"
"ds_read_b128 a[40:43], v1 offset:10240\n"
"ds_read_b128 a[44:47], v1 offset:11264\n"
"s_add_u32 s44, s95, 79872\n"
"buffer_load_dwordx4 v[208:211], v1, s[36:39], s44 offen offset:0 sc1\n"
"buffer_load_dwordx4 v[212:215], v1, s[36:39], s44 offen offset:1024 sc1\n"
"s_waitcnt vmcnt(58) lgkmcnt(4)\n"
"v_mfma_f32_16x16x32_bf16 v[28:31], v[112:115], a[0:3], 0\n"
"s_add_u32 s44, s95, 81920\n"
"buffer_load_dwordx4 v[216:219], v1, s[36:39], s44 offen offset:0 sc1\n"
"v_mfma_f32_16x16x32_bf16 v[28:31], v[116:119], a[16:19], v[28:31]\n"
"ds_read_b128 a[48:51], v1 offset:12288\n"
"ds_read_b128 a[52:55], v1 offset:13312\n"
"v_mfma_f32_16x16x32_bf16 v[32:35], v[112:115], a[4:7], 0\n"
"ds_read_b128 a[56:59], v1 offset:14336\n"
"ds_read_b128 a[60:63], v1 offset:15360\n"
"v_mfma_f32_16x16x32_bf16 v[32:35], v[116:119], a[20:23], v[32:35]\n"
"v_mfma_f32_16x16x32_bf16 v[36:39], v[112:115], a[8:11], 0\n"
"buffer_load_dwordx4 v[220:223], v1, s[36:39], s44 offen offset:1024 sc1\n"
"v_mfma_f32_16x16x32_bf16 v[36:39], v[116:119], a[24:27], v[36:39]\n"
"ds_read_b128 a[64:67], v1 offset:16384\n"
"ds_read_b128 a[68:71], v1 offset:17408\n"
"v_mfma_f32_16x16x32_bf16 v[40:43], v[112:115], a[12:15], 0\n"
"ds_read_b128 a[72:75], v1 offset:18432\n"
"ds_read_b128 a[76:79], v1 offset:19456\n"
"v_mfma_f32_16x16x32_bf16 v[40:43], v[116:119], a[28:31], v[40:43]\n"
"s_waitcnt vmcnt(58) lgkmcnt(4)\n"
"v_mfma_f32_16x16x32_bf16 v[28:31], v[120:123], a[32:35], v[28:31]\n"
"s_add_u32 s44, s95, 83968\n"
"buffer_load_dwordx4 v[112:115], v1, s[36:39], s44 offen offset:0 sc1\n"
"v_mfma_f32_16x16x32_bf16 v[28:31], v[124:127], a[48:51], v[28:31]\n"
"ds_read_b128 a[80:83], v1 offset:20480\n"
"ds_read_b128 a[84:87], v1 offset:21504\n"
"v_mfma_f32_16x16x32_bf16 v[32:35], v[120:123], a[36:39], v[32:35]\n"
"ds_read_b128 a[88:91], v1 offset:22528\n"
"ds_read_b128 a[92:95], v1 offset:23552\n"
"v_mfma_f32_16x16x32_bf16 v[32:35], v[124:127], a[52:55], v[32:35]\n"
"v_mfma_f32_16x16x32_bf16 v[36:39], v[120:123], a[40:43], v[36:39]\n"
"buffer_load_dwordx4 v[116:119], v1, s[36:39], s44 offen offset:1024 sc1\n"
"v_mfma_f32_16x16x32_bf16 v[36:39], v[124:127], a[56:59], v[36:39]\n"
"ds_read_b128 a[96:99], v1 offset:24576\n"
"ds_read_b128 a[100:103], v1 offset:25600\n"
"v_mfma_f32_16x16x32_bf16 v[40:43], v[120:123], a[44:47], v[40:43]\n"
"ds_read_b128 a[104:107], v1 offset:26624\n"
"ds_read_b128 a[108:111], v1 offset:27648\n"
"v_mfma_f32_16x16x32_bf16 v[40:43], v[124:127], a[60:63], v[40:43]\n"
"s_waitcnt vmcnt(58) lgkmcnt(4)\n"
"v_mfma_f32_16x16x32_bf16 v[28:31], v[128:131], a[64:67], v[28:31]\n"
"s_add_u32 s44, s95, 86016\n"
"buffer_load_dwordx4 v[120:123], v1, s[36:39], s44 offen offset:0 sc1\n"
"v_mfma_f32_16x16x32_bf16 v[28:31], v[132:135], a[80:83], v[28:31]\n"
"ds_read_b128 a[112:115], v1 offset:28672\n"
"ds_read_b128 a[116:119], v1 offset:29696\n"
"v_mfma_f32_16x16x32_bf16 v[32:35], v[128:131], a[68:71], v[32:35]\n"
"ds_read_b128 a[120:123], v1 offset:30720\n"
"ds_read_b128 a[124:127], v1 offset:31744\n"
"v_mfma_f32_16x16x32_bf16 v[32:35], v[132:135], a[84:87], v[32:35]\n"
"v_mfma_f32_16x16x32_bf16 v[36:39], v[128:131], a[72:75], v[36:39]\n"
"buffer_load_dwordx4 v[124:127], v1, s[36:39], s44 offen offset:1024 sc1\n"
"v_mfma_f32_16x16x32_bf16 v[36:39], v[132:135], a[88:91], v[36:39]\n"
"ds_read_b128 a[128:131], v1 offset:32768\n"
"ds_read_b128 a[132:135], v1 offset:33792\n"
"v_mfma_f32_16x16x32_bf16 v[40:43], v[128:131], a[76:79], v[40:43]\n"
"ds_read_b128 a[136:139], v1 offset:34816\n"
"ds_read_b128 a[140:143], v1 offset:35840\n"
"v_mfma_f32_16x16x32_bf16 v[40:43], v[132:135], a[92:95], v[40:43]\n"
"s_waitcnt vmcnt(58) lgkmcnt(4)\n"
"v_mfma_f32_16x16x32_bf16 v[28:31], v[136:139], a[96:99], v[28:31]\n"
"s_add_u32 s44, s95, 88064\n"
"buffer_load_dwordx4 v[128:131], v1, s[36:39], s44 offen offset:0 sc1\n"
"v_mfma_f32_16x16x32_bf16 v[28:31], v[140:143], a[112:115], v[28:31]\n"
"ds_read_b128 a[144:147], v1 offset:36864\n"
"ds_read_b128 a[148:151], v1 offset:37888\n"
"v_mfma_f32_16x16x32_bf16 v[32:35], v[136:139], a[100:103], v[32:35]\n"
"ds_read_b128 a[152:155], v1 offset:38912\n"
"ds_read_b128 a[156:159], v1 offset:39936\n"
"v_mfma_f32_16x16x32_bf16 v[32:35], v[140:143], a[116:119], v[32:35]\n"
"v_mfma_f32_16x16x32_bf16 v[36:39], v[136:139], a[104:107], v[36:39]\n"
"buffer_load_dwordx4 v[132:135], v1, s[36:39], s44 offen offset:1024 sc1\n"
"v_mfma_f32_16x16x32_bf16 v[36:39], v[140:143], a[120:123], v[36:39]\n"
"ds_read_b128 a[160:163], v1 offset:40960\n"
"ds_read_b128 a[164:167], v1 offset:41984\n"
"v_mfma_f32_16x16x32_bf16 v[40:43], v[136:139], a[108:111], v[40:43]\n"
"ds_read_b128 a[168:171], v1 offset:43008\n"
"ds_read_b128 a[172:175], v1 offset:44032\n"
"v_mfma_f32_16x16x32_bf16 v[40:43], v[140:143], a[124:127], v[40:43]\n"
"s_waitcnt vmcnt(29) lgkmcnt(4)\n"
"v_mfma_f32_16x16x32_bf16 v[28:31], v[144:147], a[128:131], v[28:31]\n"
"s_add_u32 s44, s95, 90112\n"
"buffer_load_dwordx4 v[136:139], v1, s[36:39], s44 offen offset:0 sc1\n"
"v_mfma_f32_16x16x32_bf16 v[28:31], v[148:151], a[144:147], v[28:31]\n"
"ds_read_b128 a[176:179], v1 offset:45056\n"
"ds_read_b128 a[180:183], v1 offset:46080\n"
"v_mfma_f32_16x16x32_bf16 v[32:35], v[144:147], a[132:135], v[32:35]\n"
"ds_read_b128 a[184:187], v1 offset:47104\n"
"ds_read_b128 a[188:191], v1 offset:48128\n"
"v_mfma_f32_16x16x32_bf16 v[32:35], v[148:151], a[148:151], v[32:35]\n"
"v_mfma_f32_16x16x32_bf16 v[36:39], v[144:147], a[136:139], v[36:39]\n"
"buffer_load_dwordx4 v[140:143], v1, s[36:39], s44 offen offset:1024 sc1\n"
"v_mfma_f32_16x16x32_bf16 v[36:39], v[148:151], a[152:155], v[36:39]\n"
"ds_read_b128 a[192:195], v1 offset:49152\n"
"ds_read_b128 a[196:199], v1 offset:50176\n"
"v_mfma_f32_16x16x32_bf16 v[40:43], v[144:147], a[140:143], v[40:43]\n"
"ds_read_b128 a[200:203], v1 offset:51200\n"
"ds_read_b128 a[204:207], v1 offset:52224\n"
"v_mfma_f32_16x16x32_bf16 v[40:43], v[148:151], a[156:159], v[40:43]\n"
"s_waitcnt vmcnt(29) lgkmcnt(4)\n"
"v_mfma_f32_16x16x32_bf16 v[28:31], v[152:155], a[160:163], v[28:31]\n"
"s_add_u32 s44, s95, 147456\n"
"buffer_load_dwordx4 v[144:147], v1, s[36:39], s44 offen offset:0 sc1\n"
"v_mfma_f32_16x16x32_bf16 v[28:31], v[156:159], a[176:179], v[28:31]\n"
"ds_read_b128 a[208:211], v1 offset:53248\n"
"ds_read_b128 a[212:215], v1 offset:54272\n"
"v_mfma_f32_16x16x32_bf16 v[32:35], v[152:155], a[164:167], v[32:35]\n"
"ds_read_b128 a[216:219], v1 offset:55296\n"
"ds_read_b128 a[220:223], v1 offset:56320\n"
"v_mfma_f32_16x16x32_bf16 v[32:35], v[156:159], a[180:183], v[32:35]\n"
"v_mfma_f32_16x16x32_bf16 v[36:39], v[152:155], a[168:171], v[36:39]\n"
"buffer_load_dwordx4 v[148:151], v1, s[36:39], s44 offen offset:1024 sc1\n"
"v_mfma_f32_16x16x32_bf16 v[36:39], v[156:159], a[184:187], v[36:39]\n"
"ds_read_b128 a[224:227], v1 offset:57344\n"
"ds_read_b128 a[228:231], v1 offset:58368\n"
"v_mfma_f32_16x16x32_bf16 v[40:43], v[152:155], a[172:175], v[40:43]\n"
"ds_read_b128 a[232:235], v1 offset:59392\n"
"ds_read_b128 a[236:239], v1 offset:60416\n"
"v_mfma_f32_16x16x32_bf16 v[40:43], v[156:159], a[188:191], v[40:43]\n"
"s_waitcnt vmcnt(28) lgkmcnt(4)\n"
"v_mfma_f32_16x16x32_bf16 v[28:31], v[160:163], a[192:195], v[28:31]\n"
"s_add_u32 s44, s95, 149504\n"
"buffer_load_dwordx4 v[152:155], v1, s[36:39], s44 offen offset:0 sc1\n"
"v_mfma_f32_16x16x32_bf16 v[28:31], v[164:167], a[208:211], v[28:31]\n"
"ds_read_b128 a[240:243], v1 offset:61440\n"
"ds_read_b128 a[244:247], v1 offset:62464\n"
"v_mfma_f32_16x16x32_bf16 v[32:35], v[160:163], a[196:199], v[32:35]\n"
"ds_read_b128 a[248:251], v1 offset:63488\n"
"ds_read_b128 a[252:255], v1 offset:64512\n"
"v_mfma_f32_16x16x32_bf16 v[32:35], v[164:167], a[212:215], v[32:35]\n"
"v_mfma_f32_16x16x32_bf16 v[36:39], v[160:163], a[200:203], v[36:39]\n"
"buffer_load_dwordx4 v[156:159], v1, s[36:39], s44 offen offset:1024 sc1\n"
"v_mfma_f32_16x16x32_bf16 v[36:39], v[164:167], a[216:219], v[36:39]\n"
"ds_read_b128 v[224:227], v18 offset:0\n"
"ds_read_b128 v[228:231], v18 offset:1024\n"
"v_mfma_f32_16x16x32_bf16 v[40:43], v[160:163], a[204:207], v[40:43]\n"
"ds_read_b128 v[232:235], v18 offset:2048\n"
"ds_read_b128 v[236:239], v18 offset:3072\n"
"v_mfma_f32_16x16x32_bf16 v[40:43], v[164:167], a[220:223], v[40:43]\n"
"s_waitcnt vmcnt(28) lgkmcnt(4)\n"
"v_mfma_f32_16x16x32_bf16 v[28:31], v[168:171], a[224:227], v[28:31]\n"
"s_add_u32 s44, s95, 151552\n"
"buffer_load_dwordx4 v[160:163], v1, s[36:39], s44 offen offset:0 sc1\n"
"v_mfma_f32_16x16x32_bf16 v[28:31], v[172:175], a[240:243], v[28:31]\n"
"ds_read_b128 v[240:243], v18 offset:4096\n"
"ds_read_b128 v[244:247], v18 offset:5120\n"
"v_mfma_f32_16x16x32_bf16 v[32:35], v[168:171], a[228:231], v[32:35]\n"
"ds_read_b128 v[248:251], v18 offset:6144\n"
"ds_read_b128 v[252:255], v18 offset:7168\n"
"v_mfma_f32_16x16x32_bf16 v[32:35], v[172:175], a[244:247], v[32:35]\n"
"v_mfma_f32_16x16x32_bf16 v[36:39], v[168:171], a[232:235], v[36:39]\n"
"buffer_load_dwordx4 v[164:167], v1, s[36:39], s44 offen offset:1024 sc1\n"
"v_mfma_f32_16x16x32_bf16 v[36:39], v[172:175], a[248:251], v[36:39]\n"
"v_mfma_f32_16x16x32_bf16 v[40:43], v[168:171], a[236:239], v[40:43]\n"
"v_mfma_f32_16x16x32_bf16 v[40:43], v[172:175], a[252:255], v[40:43]\n"
"s_waitcnt vmcnt(28) lgkmcnt(0)\n"
"v_mfma_f32_16x16x32_bf16 v[28:31], v[176:179], v[224:227], v[28:31]\n"
"s_add_u32 s44, s95, 153600\n"
"buffer_load_dwordx4 v[168:171], v1, s[36:39], s44 offen offset:0 sc1\n"
"v_mfma_f32_16x16x32_bf16 v[44:47], v[180:183], v[240:243], v[28:31]\n"
"v_mfma_f32_16x16x32_bf16 v[32:35], v[176:179], v[228:231], v[32:35]\n"
"v_mfma_f32_16x16x32_bf16 v[48:51], v[180:183], v[244:247], v[32:35]\n"
"v_mfma_f32_16x16x32_bf16 v[36:39], v[176:179], v[232:235], v[36:39]\n"
"buffer_load_dwordx4 v[172:175], v1, s[36:39], s44 offen offset:1024 sc1\n"
"v_mfma_f32_16x16x32_bf16 v[52:55], v[180:183], v[248:251], v[36:39]\n"
"v_mfma_f32_16x16x32_bf16 v[40:43], v[176:179], v[236:239], v[40:43]\n"
"v_mfma_f32_16x16x32_bf16 v[56:59], v[180:183], v[252:255], v[40:43]\n"
"s_waitcnt vmcnt(28)\n"
"v_mfma_f32_16x16x32_bf16 v[28:31], v[184:187], a[0:3], 0\n"
"s_add_u32 s44, s95, 155648\n"
"buffer_load_dwordx4 v[176:179], v1, s[36:39], s44 offen offset:0 sc1\n"
"v_mfma_f32_16x16x32_bf16 v[28:31], v[188:191], a[16:19], v[28:31]\n"
"v_exp_f32 v86, v44\n"
"v_mfma_f32_16x16x32_bf16 v[32:35], v[184:187], a[4:7], 0\n"
"v_exp_f32 v87, v45\n"
"v_mfma_f32_16x16x32_bf16 v[32:35], v[188:191], a[20:23], v[32:35]\n"
"v_add_f32 v86, 1.0, v86\n"
"v_add_f32 v87, 1.0, v87\n"
"v_mfma_f32_16x16x32_bf16 v[36:39], v[184:187], a[8:11], 0\n"
"s_add_u32 s10, s95, 5308416\n"
"buffer_load_dwordx4 v[180:183], v1, s[36:39], s44 offen offset:1024 sc1\n"
"v_mfma_f32_16x16x32_bf16 v[36:39], v[188:191], a[24:27], v[36:39]\n"
"s_add_u32 s83, s10, 589824\n"
"v_rcp_f32 v86, v86\n"
"v_mfma_f32_16x16x32_bf16 v[40:43], v[184:187], a[12:15], 0\n"
"s_mul_i32 s11, s5, 73728\n"
"v_rcp_f32 v87, v87\n"
"v_mfma_f32_16x16x32_bf16 v[40:43], v[188:191], a[28:31], v[40:43]\n"
"s_waitcnt vmcnt(28)\n"
"v_mul_f32 v86, v44, v86\n"
"v_mul_f32 v87, v45, v87\n"
"v_mfma_f32_16x16x32_bf16 v[28:31], v[192:195], a[32:35], v[28:31]\n"
"s_add_u32 s44, s95, 157696\n"
"buffer_load_dwordx4 v[184:187], v1, s[36:39], s44 offen offset:0 sc1\n"
"v_mfma_f32_16x16x32_bf16 v[28:31], v[196:199], a[48:51], v[28:31]\n"
"s_add_u32 s91, s10, s11\n"
"v_exp_f32 v88, v46\n"
"v_mfma_f32_16x16x32_bf16 v[32:35], v[192:195], a[36:39], v[32:35]\n"
"s_mul_i32 s12, s4, 589824\n"
"v_exp_f32 v89, v47\n"
"v_mfma_f32_16x16x32_bf16 v[32:35], v[196:199], a[52:55], v[32:35]\n"
"s_mul_i32 s8, s6, 2304\n"
"v_add_f32 v88, 1.0, v88\n"
"v_add_f32 v89, 1.0, v89\n"
"v_mfma_f32_16x16x32_bf16 v[36:39], v[192:195], a[40:43], v[36:39]\n"
"s_add_u32 s12, s12, s8\n"
"buffer_load_dwordx4 v[188:191], v1, s[36:39], s44 offen offset:1024 sc1\n"
"v_mfma_f32_16x16x32_bf16 v[36:39], v[196:199], a[56:59], v[36:39]\n"
"s_add_u32 s12, s12, 7602176\n"
"v_rcp_f32 v88, v88\n"
"v_mfma_f32_16x16x32_bf16 v[40:43], v[192:195], a[44:47], v[40:43]\n"
"s_add_u32 s11, s11, s12\n"
"v_rcp_f32 v89, v89\n"
"v_mfma_f32_16x16x32_bf16 v[40:43], v[196:199], a[60:63], v[40:43]\n"
"s_waitcnt vmcnt(28)\n"
"v_mul_f32 v88, v46, v88\n"
"v_mul_f32 v89, v47, v89\n"
"v_mfma_f32_16x16x32_bf16 v[28:31], v[200:203], a[64:67], v[28:31]\n"
"s_add_u32 s44, s95, 159744\n"
"buffer_load_dwordx4 v[192:195], v1, s[36:39], s44 offen offset:0 sc1\n"
"v_mfma_f32_16x16x32_bf16 v[28:31], v[204:207], a[80:83], v[28:31]\n"
"s_mul_i32 s8, s5, 9216\n"
"v_cvt_pk_bf16_f32 v60, v86, v87\n"
"v_cvt_pk_bf16_f32 v61, v88, v89\n"
"v_mfma_f32_16x16x32_bf16 v[32:35], v[200:203], a[68:71], v[32:35]\n"
"s_add_u32 s12, s12, s8\n"
"v_exp_f32 v90, v48\n"
"v_mfma_f32_16x16x32_bf16 v[32:35], v[204:207], a[84:87], v[32:35]\n"
"s_mul_i32 s8, s55, 1\n"
"v_exp_f32 v91, v49\n"
"v_mfma_f32_16x16x32_bf16 v[36:39], v[200:203], a[72:75], v[36:39]\n"
"s_mul_i32 s9, s8, 9363\n"
"buffer_load_dwordx4 v[196:199], v1, s[36:39], s44 offen offset:1024 sc1\n"
"v_mfma_f32_16x16x32_bf16 v[36:39], v[204:207], a[88:91], v[36:39]\n"
"s_lshr_b32 s9, s9, 16\n"
"v_add_f32 v90, 1.0, v90\n"
"v_add_f32 v91, 1.0, v91\n"
"v_mfma_f32_16x16x32_bf16 v[40:43], v[200:203], a[76:79], v[40:43]\n"
"s_mul_i32 s9, s9, 7\n"
"v_rcp_f32 v90, v90\n"
"v_mfma_f32_16x16x32_bf16 v[40:43], v[204:207], a[92:95], v[40:43]\n"
"s_waitcnt vmcnt(24)\n"
"v_rcp_f32 v91, v91\n"
"v_mfma_f32_16x16x32_bf16 v[28:31], v[208:211], a[96:99], v[28:31]\n"
"s_add_u32 s44, s95, 161792\n"
"buffer_load_dwordx4 v[200:203], v1, s[36:39], s44 offen offset:0 sc1\n"
"v_mfma_f32_16x16x32_bf16 v[28:31], v[212:215], a[112:115], v[28:31]\n"
"s_sub_u32 s8, s8, s9\n"
"v_mul_f32 v90, v48, v90\n"
"v_mul_f32 v91, v49, v91\n"
"v_mfma_f32_16x16x32_bf16 v[32:35], v[208:211], a[100:103], v[32:35]\n"
"s_add_u32 s8, s8, 1\n"
"v_exp_f32 v92, v50\n"
"v_mfma_f32_16x16x32_bf16 v[32:35], v[212:215], a[116:119], v[32:35]\n"
"s_add_u32 s9, s5, s8\n"
"v_exp_f32 v93, v51\n"
"v_mfma_f32_16x16x32_bf16 v[36:39], v[208:211], a[104:107], v[36:39]\n"
"s_and_b32 s9, s9, 7\n"
"buffer_load_dwordx4 v[204:207], v1, s[36:39], s44 offen offset:1024 sc1\n"
"v_mfma_f32_16x16x32_bf16 v[36:39], v[212:215], a[120:123], v[36:39]\n"
"s_waitcnt vmcnt(28)\n"
"v_add_f32 v92, 1.0, v92\n"
"v_add_f32 v93, 1.0, v93\n"
"v_mfma_f32_16x16x32_bf16 v[40:43], v[208:211], a[108:111], v[40:43]\n"
"s_barrier\n"
"v_rcp_f32 v92, v92\n"
"v_mfma_f32_16x16x32_bf16 v[40:43], v[212:215], a[124:127], v[40:43]\n"
"s_waitcnt vmcnt(24)\n"
"v_rcp_f32 v93, v93\n"
"v_mfma_f32_16x16x32_bf16 v[28:31], v[216:219], a[128:131], v[28:31]\n"
"s_add_u32 s44, s95, 163840\n"
"buffer_load_dwordx4 v[208:211], v1, s[36:39], s44 offen offset:0 sc1\n"
"s_cmp_lg_u32 s6, 0\n"
"s_cbranch_scc1 LFC0_block_kernel\n"
"s_add_u32 s13, s20, 65536\n"
"s_mov_b32 exec_lo, 0xffff\n"
"s_mov_b32 exec_hi, 0\n"
"buffer_store_dwordx2 v[6:7], v2, s[36:39], s13 offen sc1\n"
"s_mov_b64 exec, -1\n"
"LFC0_block_kernel:\n"
"v_mfma_f32_16x16x32_bf16 v[28:31], v[220:223], a[144:147], v[28:31]\n"
"s_mul_i32 s9, s9, 73728\n"
"v_mul_f32 v92, v50, v92\n"
"v_mul_f32 v93, v51, v93\n"
"v_mfma_f32_16x16x32_bf16 v[32:35], v[216:219], a[132:135], v[32:35]\n"
"s_add_u32 s84, s10, s9\n"
"ds_write_b64 v16, v[60:61] offset:0\n"
"v_cvt_pk_bf16_f32 v62, v90, v91\n"
"v_mfma_f32_16x16x32_bf16 v[32:35], v[220:223], a[148:151], v[32:35]\n"
"s_add_u32 s25, s12, s9\n"
"v_cvt_pk_bf16_f32 v63, v92, v93\n"
"ds_write_b64 v16, v[62:63] offset:1024\n"
"v_mfma_f32_16x16x32_bf16 v[36:39], v[216:219], a[136:139], v[36:39]\n"
"s_sub_u32 s9, s5, s8\n"
"buffer_load_dwordx4 v[212:215], v1, s[36:39], s44 offen offset:1024 sc1\n"
"v_mfma_f32_16x16x32_bf16 v[36:39], v[220:223], a[152:155], v[36:39]\n"
"s_and_b32 s9, s9, 7\n"
"v_exp_f32 v86, v52\n"
"v_mfma_f32_16x16x32_bf16 v[40:43], v[216:219], a[140:143], v[40:43]\n"
"s_mul_i32 s9, s9, 9216\n"
"v_exp_f32 v87, v53\n"
"v_mfma_f32_16x16x32_bf16 v[40:43], v[220:223], a[156:159], v[40:43]\n"
"s_waitcnt vmcnt(24)\n"
"v_add_f32 v86, 1.0, v86\n"
"v_add_f32 v87, 1.0, v87\n"
"v_mfma_f32_16x16x32_bf16 v[28:31], v[112:115], a[160:163], v[28:31]\n"
"s_add_u32 s44, s95, 221184\n"
"buffer_load_dwordx4 v[216:219], v1, s[36:39], s44 offen offset:0 sc1\n"
"v_mfma_f32_16x16x32_bf16 v[28:31], v[116:119], a[176:179], v[28:31]\n"
"s_add_u32 s56, s11, s9\n"
"v_rcp_f32 v86, v86\n"
"v_mfma_f32_16x16x32_bf16 v[32:35], v[112:115], a[164:167], v[32:35]\n"
"s_add_u32 s8, s8, 1\n"
"v_rcp_f32 v87, v87\n"
"v_mfma_f32_16x16x32_bf16 v[32:35], v[116:119], a[180:183], v[32:35]\n"
"s_cmp_eq_u32 s8, 8\n"
"s_cselect_b32 s8, 1, s8\n"
"v_mul_f32 v86, v52, v86\n"
"v_mul_f32 v87, v53, v87\n"
"v_mfma_f32_16x16x32_bf16 v[36:39], v[112:115], a[168:171], v[36:39]\n"
"s_add_u32 s9, s5, s8\n"
"buffer_load_dwordx4 v[220:223], v1, s[36:39], s44 offen offset:1024 sc1\n"
"v_mfma_f32_16x16x32_bf16 v[36:39], v[116:119], a[184:187], v[36:39]\n"
"s_and_b32 s9, s9, 7\n"
"v_exp_f32 v88, v54\n"
"v_mfma_f32_16x16x32_bf16 v[40:43], v[112:115], a[172:175], v[40:43]\n"
"s_mul_i32 s9, s9, 73728\n"
"v_exp_f32 v89, v55\n"
"v_mfma_f32_16x16x32_bf16 v[40:43], v[116:119], a[188:191], v[40:43]\n"
"s_waitcnt vmcnt(24)\n"
"v_add_f32 v88, 1.0, v88\n"
"v_add_f32 v89, 1.0, v89\n"
"v_mfma_f32_16x16x32_bf16 v[28:31], v[120:123], a[192:195], v[28:31]\n"
"s_add_u32 s44, s95, 223232\n"
"buffer_load_dwordx4 v[112:115], v1, s[36:39], s44 offen offset:0 sc1\n"
"v_mfma_f32_16x16x32_bf16 v[28:31], v[124:127], a[208:211], v[28:31]\n"
"s_add_u32 s85, s10, s9\n"
"v_rcp_f32 v88, v88\n"
"v_mfma_f32_16x16x32_bf16 v[32:35], v[120:123], a[196:199], v[32:35]\n"
"s_add_u32 s26, s12, s9\n"
"v_rcp_f32 v89, v89\n"
"v_mfma_f32_16x16x32_bf16 v[32:35], v[124:127], a[212:215], v[32:35]\n"
"s_sub_u32 s9, s5, s8\n"
"v_mul_f32 v88, v54, v88\n"
"v_mul_f32 v89, v55, v89\n"
"v_mfma_f32_16x16x32_bf16 v[36:39], v[120:123], a[200:203], v[36:39]\n"
"s_and_b32 s9, s9, 7\n"
"buffer_load_dwordx4 v[116:119], v1, s[36:39], s44 offen offset:1024 sc1\n"
"v_mfma_f32_16x16x32_bf16 v[36:39], v[124:127], a[216:219], v[36:39]\n"
"s_mul_i32 s9, s9, 9216\n"
"v_cvt_pk_bf16_f32 v64, v86, v87\n"
"v_cvt_pk_bf16_f32 v65, v88, v89\n"
"v_mfma_f32_16x16x32_bf16 v[40:43], v[120:123], a[204:207], v[40:43]\n"
"s_add_u32 s57, s11, s9\n"
"v_exp_f32 v90, v56\n"
"v_mfma_f32_16x16x32_bf16 v[40:43], v[124:127], a[220:223], v[40:43]\n"
"s_waitcnt vmcnt(24)\n"
"v_exp_f32 v91, v57\n"
"v_mfma_f32_16x16x32_bf16 v[28:31], v[128:131], a[224:227], v[28:31]\n"
"s_add_u32 s44, s95, 225280\n"
"buffer_load_dwordx4 v[120:123], v1, s[36:39], s44 offen offset:0 sc1\n"
"v_mfma_f32_16x16x32_bf16 v[28:31], v[132:135], a[240:243], v[28:31]\n"
"s_add_u32 s8, s8, 1\n"
"v_add_f32 v90, 1.0, v90\n"
"v_add_f32 v91, 1.0, v91\n"
"v_mfma_f32_16x16x32_bf16 v[32:35], v[128:131], a[228:231], v[32:35]\n"
"s_cmp_eq_u32 s8, 8\n"
"s_cselect_b32 s8, 1, s8\n"
"v_rcp_f32 v90, v90\n"
"v_mfma_f32_16x16x32_bf16 v[32:35], v[132:135], a[244:247], v[32:35]\n"
"s_add_u32 s9, s5, s8\n"
"v_rcp_f32 v91, v91\n"
"v_mfma_f32_16x16x32_bf16 v[36:39], v[128:131], a[232:235], v[36:39]\n"
"s_and_b32 s9, s9, 7\n"
"buffer_load_dwordx4 v[124:127], v1, s[36:39], s44 offen offset:1024 sc1\n"
"v_mfma_f32_16x16x32_bf16 v[36:39], v[132:135], a[248:251], v[36:39]\n"
"s_mul_i32 s9, s9, 73728\n"
"v_mul_f32 v90, v56, v90\n"
"v_mul_f32 v91, v57, v91\n"
"v_mfma_f32_16x16x32_bf16 v[40:43], v[128:131], a[236:239], v[40:43]\n"
"s_add_u32 s86, s10, s9\n"
"v_exp_f32 v92, v58\n"
"v_mfma_f32_16x16x32_bf16 v[40:43], v[132:135], a[252:255], v[40:43]\n"
"s_waitcnt vmcnt(24)\n"
"v_exp_f32 v93, v59\n"
"v_mfma_f32_16x16x32_bf16 v[28:31], v[136:139], v[224:227], v[28:31]\n"
"s_add_u32 s44, s95, 227328\n"
"buffer_load_dwordx4 v[128:131], v1, s[36:39], s44 offen offset:0 sc1\n"
"v_mfma_f32_16x16x32_bf16 v[44:47], v[140:143], v[240:243], v[28:31]\n"
"v_add_f32 v92, 1.0, v92\n"
"v_add_f32 v93, 1.0, v93\n"
"v_mfma_f32_16x16x32_bf16 v[32:35], v[136:139], v[228:231], v[32:35]\n"
"v_rcp_f32 v92, v92\n"
"v_mfma_f32_16x16x32_bf16 v[48:51], v[140:143], v[244:247], v[32:35]\n"
"v_rcp_f32 v93, v93\n"
"v_mfma_f32_16x16x32_bf16 v[36:39], v[136:139], v[232:235], v[36:39]\n"
"buffer_load_dwordx4 v[132:135], v1, s[36:39], s44 offen offset:1024 sc1\n"
"v_mfma_f32_16x16x32_bf16 v[52:55], v[140:143], v[248:251], v[36:39]\n"
"v_mul_f32 v92, v58, v92\n"
"v_mul_f32 v93, v59, v93\n"
"v_mfma_f32_16x16x32_bf16 v[40:43], v[136:139], v[236:239], v[40:43]\n"
"ds_write_b64 v16, v[64:65] offset:2048\n"
"v_cvt_pk_bf16_f32 v66, v90, v91\n"
"v_mfma_f32_16x16x32_bf16 v[56:59], v[140:143], v[252:255], v[40:43]\n"
"s_waitcnt vmcnt(24)\n"
"v_cvt_pk_bf16_f32 v67, v92, v93\n"
"ds_write_b64 v16, v[66:67] offset:3072\n"
"v_mfma_f32_16x16x32_bf16 v[28:31], v[144:147], a[0:3], 0\n"
"s_add_u32 s44, s95, 229376\n"
"buffer_load_dwordx4 v[136:139], v1, s[36:39], s44 offen offset:0 sc1\n"
"v_mfma_f32_16x16x32_bf16 v[28:31], v[148:151], a[16:19], v[28:31]\n"
"v_exp_f32 v86, v44\n"
"v_mfma_f32_16x16x32_bf16 v[32:35], v[144:147], a[4:7], 0\n"
"v_exp_f32 v87, v45\n"
"v_mfma_f32_16x16x32_bf16 v[32:35], v[148:151], a[20:23], v[32:35]\n"
"v_add_f32 v86, 1.0, v86\n"
"v_add_f32 v87, 1.0, v87\n"
"v_mfma_f32_16x16x32_bf16 v[36:39], v[144:147], a[8:11], 0\n"
"s_add_u32 s27, s12, s9\n"
"buffer_load_dwordx4 v[140:143], v1, s[36:39], s44 offen offset:1024 sc1\n"
"v_mfma_f32_16x16x32_bf16 v[36:39], v[148:151], a[24:27], v[36:39]\n"
"s_sub_u32 s9, s5, s8\n"
"v_rcp_f32 v86, v86\n"
"v_mfma_f32_16x16x32_bf16 v[40:43], v[144:147], a[12:15], 0\n"
"s_and_b32 s9, s9, 7\n"
"v_rcp_f32 v87, v87\n"
"v_mfma_f32_16x16x32_bf16 v[40:43], v[148:151], a[28:31], v[40:43]\n"
"s_waitcnt vmcnt(24)\n"
"v_mul_f32 v86, v44, v86\n"
"v_mul_f32 v87, v45, v87\n"
"v_mfma_f32_16x16x32_bf16 v[28:31], v[152:155], a[32:35], v[28:31]\n"
"s_add_u32 s44, s95, 231424\n"
"buffer_load_dwordx4 v[144:147], v1, s[36:39], s44 offen offset:0 sc1\n"
"v_mfma_f32_16x16x32_bf16 v[28:31], v[156:159], a[48:51], v[28:31]\n"
"s_mul_i32 s9, s9, 9216\n"
"v_exp_f32 v88, v46\n"
"v_mfma_f32_16x16x32_bf16 v[32:35], v[152:155], a[36:39], v[32:35]\n"
"s_add_u32 s58, s11, s9\n"
"v_exp_f32 v89, v47\n"
"v_mfma_f32_16x16x32_bf16 v[32:35], v[156:159], a[52:55], v[32:35]\n"
"s_add_u32 s8, s8, 1\n"
"v_add_f32 v88, 1.0, v88\n"
"v_add_f32 v89, 1.0, v89\n"
"v_mfma_f32_16x16x32_bf16 v[36:39], v[152:155], a[40:43], v[36:39]\n"
"s_cmp_eq_u32 s8, 8\n"
"s_cselect_b32 s8, 1, s8\n"
"buffer_load_dwordx4 v[148:151], v1, s[36:39], s44 offen offset:1024 sc1\n"
"v_mfma_f32_16x16x32_bf16 v[36:39], v[156:159], a[56:59], v[36:39]\n"
"s_add_u32 s9, s5, s8\n"
"v_rcp_f32 v88, v88\n"
"v_mfma_f32_16x16x32_bf16 v[40:43], v[152:155], a[44:47], v[40:43]\n"
"s_and_b32 s9, s9, 7\n"
"v_rcp_f32 v89, v89\n"
"v_mfma_f32_16x16x32_bf16 v[40:43], v[156:159], a[60:63], v[40:43]\n"
"s_waitcnt vmcnt(24)\n"
"v_mul_f32 v88, v46, v88\n"
"v_mul_f32 v89, v47, v89\n"
"v_mfma_f32_16x16x32_bf16 v[28:31], v[160:163], a[64:67], v[28:31]\n"
"s_add_u32 s44, s95, 233472\n"
"buffer_load_dwordx4 v[152:155], v1, s[36:39], s44 offen offset:0 sc1\n"
"v_mfma_f32_16x16x32_bf16 v[28:31], v[164:167], a[80:83], v[28:31]\n"
"s_mul_i32 s9, s9, 73728\n"
"v_cvt_pk_bf16_f32 v60, v86, v87\n"
"v_cvt_pk_bf16_f32 v61, v88, v89\n"
"v_mfma_f32_16x16x32_bf16 v[32:35], v[160:163], a[68:71], v[32:35]\n"
"s_add_u32 s87, s10, s9\n"
"v_exp_f32 v90, v48\n"
"v_mfma_f32_16x16x32_bf16 v[32:35], v[164:167], a[84:87], v[32:35]\n"
"s_add_u32 s28, s12, s9\n"
"v_exp_f32 v91, v49\n"
"v_mfma_f32_16x16x32_bf16 v[36:39], v[160:163], a[72:75], v[36:39]\n"
"s_sub_u32 s9, s5, s8\n"
"buffer_load_dwordx4 v[156:159], v1, s[36:39], s44 offen offset:1024 sc1\n"
"v_mfma_f32_16x16x32_bf16 v[36:39], v[164:167], a[88:91], v[36:39]\n"
"s_and_b32 s9, s9, 7\n"
"v_add_f32 v90, 1.0, v90\n"
"v_add_f32 v91, 1.0, v91\n"
"v_mfma_f32_16x16x32_bf16 v[40:43], v[160:163], a[76:79], v[40:43]\n"
"s_mul_i32 s9, s9, 9216\n"
"v_rcp_f32 v90, v90\n"
"v_mfma_f32_16x16x32_bf16 v[40:43], v[164:167], a[92:95], v[40:43]\n"
"s_waitcnt vmcnt(24)\n"
"v_rcp_f32 v91, v91\n"
"v_mfma_f32_16x16x32_bf16 v[28:31], v[168:171], a[96:99], v[28:31]\n"
"s_add_u32 s44, s95, 235520\n"
"buffer_load_dwordx4 v[160:163], v1, s[36:39], s44 offen offset:0 sc1\n"
"v_mfma_f32_16x16x32_bf16 v[28:31], v[172:175], a[112:115], v[28:31]\n"
"s_add_u32 s59, s11, s9\n"
"v_mul_f32 v90, v48, v90\n"
"v_mul_f32 v91, v49, v91\n"
"v_mfma_f32_16x16x32_bf16 v[32:35], v[168:171], a[100:103], v[32:35]\n"
"s_add_u32 s8, s8, 1\n"
"v_exp_f32 v92, v50\n"
"v_mfma_f32_16x16x32_bf16 v[32:35], v[172:175], a[116:119], v[32:35]\n"
"s_cmp_eq_u32 s8, 8\n"
"s_cselect_b32 s8, 1, s8\n"
"v_exp_f32 v93, v51\n"
"v_mfma_f32_16x16x32_bf16 v[36:39], v[168:171], a[104:107], v[36:39]\n"
"s_add_u32 s9, s5, s8\n"
"buffer_load_dwordx4 v[164:167], v1, s[36:39], s44 offen offset:1024 sc1\n"
"v_mfma_f32_16x16x32_bf16 v[36:39], v[172:175], a[120:123], v[36:39]\n"
"s_and_b32 s9, s9, 7\n"
"v_add_f32 v92, 1.0, v92\n"
"v_add_f32 v93, 1.0, v93\n"
"v_mfma_f32_16x16x32_bf16 v[40:43], v[168:171], a[108:111], v[40:43]\n"
"s_mul_i32 s9, s9, 73728\n"
"v_rcp_f32 v92, v92\n"
"v_mfma_f32_16x16x32_bf16 v[40:43], v[172:175], a[124:127], v[40:43]\n"
"s_waitcnt vmcnt(24)\n"
"v_rcp_f32 v93, v93\n"
"v_mfma_f32_16x16x32_bf16 v[28:31], v[176:179], a[128:131], v[28:31]\n"
"s_add_u32 s44, s95, 237568\n"
"buffer_load_dwordx4 v[168:171], v1, s[36:39], s44 offen offset:0 sc1\n"
"v_mfma_f32_16x16x32_bf16 v[28:31], v[180:183], a[144:147], v[28:31]\n"
"s_add_u32 s88, s10, s9\n"
"v_mul_f32 v92, v50, v92\n"
"v_mul_f32 v93, v51, v93\n"
"v_mfma_f32_16x16x32_bf16 v[32:35], v[176:179], a[132:135], v[32:35]\n"
"s_add_u32 s29, s12, s9\n"
"ds_write_b64 v16, v[60:61] offset:8192\n"
"v_cvt_pk_bf16_f32 v62, v90, v91\n"
"v_mfma_f32_16x16x32_bf16 v[32:35], v[180:183], a[148:151], v[32:35]\n"
"s_sub_u32 s9, s5, s8\n"
"v_cvt_pk_bf16_f32 v63, v92, v93\n"
"ds_write_b64 v16, v[62:63] offset:9216\n"
"v_mfma_f32_16x16x32_bf16 v[36:39], v[176:179], a[136:139], v[36:39]\n"
"s_and_b32 s9, s9, 7\n"
"buffer_load_dwordx4 v[172:175], v1, s[36:39], s44 offen offset:1024 sc1\n"
"v_mfma_f32_16x16x32_bf16 v[36:39], v[180:183], a[152:155], v[36:39]\n"
"s_mul_i32 s9, s9, 9216\n"
"v_exp_f32 v86, v52\n"
"v_mfma_f32_16x16x32_bf16 v[40:43], v[176:179], a[140:143], v[40:43]\n"
"s_add_u32 s60, s11, s9\n"
"v_exp_f32 v87, v53\n"
"v_mfma_f32_16x16x32_bf16 v[40:43], v[180:183], a[156:159], v[40:43]\n"
"s_waitcnt vmcnt(24)\n"
"v_add_f32 v86, 1.0, v86\n"
"v_add_f32 v87, 1.0, v87\n"
"v_mfma_f32_16x16x32_bf16 v[28:31], v[184:187], a[160:163], v[28:31]\n"
"s_add_u32 s44, s95, 294912\n"
"buffer_load_dwordx4 v[176:179], v1, s[36:39], s44 offen offset:0 sc1\n"
"v_mfma_f32_16x16x32_bf16 v[28:31], v[188:191], a[176:179], v[28:31]\n"
"s_add_u32 s8, s8, 1\n"
"v_rcp_f32 v86, v86\n"
"v_mfma_f32_16x16x32_bf16 v[32:35], v[184:187], a[164:167], v[32:35]\n"
"s_cmp_eq_u32 s8, 8\n"
"s_cselect_b32 s8, 1, s8\n"
"v_rcp_f32 v87, v87\n"
"v_mfma_f32_16x16x32_bf16 v[32:35], v[188:191], a[180:183], v[32:35]\n"
"s_add_u32 s9, s5, s8\n"
"v_mul_f32 v86, v52, v86\n"
"v_mul_f32 v87, v53, v87\n"
"v_mfma_f32_16x16x32_bf16 v[36:39], v[184:187], a[168:171], v[36:39]\n"
"s_and_b32 s9, s9, 7\n"
"buffer_load_dwordx4 v[180:183], v1, s[36:39], s44 offen offset:1024 sc1\n"
"v_mfma_f32_16x16x32_bf16 v[36:39], v[188:191], a[184:187], v[36:39]\n"
"s_mul_i32 s9, s9, 73728\n"
"v_exp_f32 v88, v54\n"
"v_mfma_f32_16x16x32_bf16 v[40:43], v[184:187], a[172:175], v[40:43]\n"
"s_add_u32 s89, s10, s9\n"
"v_exp_f32 v89, v55\n"
"v_mfma_f32_16x16x32_bf16 v[40:43], v[188:191], a[188:191], v[40:43]\n"
"s_waitcnt vmcnt(24)\n"
"v_add_f32 v88, 1.0, v88\n"
"v_add_f32 v89, 1.0, v89\n"
"v_mfma_f32_16x16x32_bf16 v[28:31], v[192:195], a[192:195], v[28:31]\n"
"s_add_u32 s44, s95, 296960\n"
"buffer_load_dwordx4 v[184:187], v1, s[36:39], s44 offen offset:0 sc1\n"
"v_mfma_f32_16x16x32_bf16 v[28:31], v[196:199], a[208:211], v[28:31]\n"
"s_add_u32 s30, s12, s9\n"
"v_rcp_f32 v88, v88\n"
"v_mfma_f32_16x16x32_bf16 v[32:35], v[192:195], a[196:199], v[32:35]\n"
"s_sub_u32 s9, s5, s8\n"
"v_rcp_f32 v89, v89\n"
"v_mfma_f32_16x16x32_bf16 v[32:35], v[196:199], a[212:215], v[32:35]\n"
"s_and_b32 s9, s9, 7\n"
"v_mul_f32 v88, v54, v88\n"
"v_mul_f32 v89, v55, v89\n"
"v_mfma_f32_16x16x32_bf16 v[36:39], v[192:195], a[200:203], v[36:39]\n"
"s_mul_i32 s9, s9, 9216\n"
"buffer_load_dwordx4 v[188:191], v1, s[36:39], s44 offen offset:1024 sc1\n"
"v_mfma_f32_16x16x32_bf16 v[36:39], v[196:199], a[216:219], v[36:39]\n"
"s_add_u32 s61, s11, s9\n"
"v_cvt_pk_bf16_f32 v64, v86, v87\n"
"v_cvt_pk_bf16_f32 v65, v88, v89\n"
"v_mfma_f32_16x16x32_bf16 v[40:43], v[192:195], a[204:207], v[40:43]\n"
"s_add_u32 s8, s8, 1\n"
"v_exp_f32 v90, v56\n"
"v_mfma_f32_16x16x32_bf16 v[40:43], v[196:199], a[220:223], v[40:43]\n"
"s_waitcnt vmcnt(24)\n"
"v_exp_f32 v91, v57\n"
"v_mfma_f32_16x16x32_bf16 v[28:31], v[200:203], a[224:227], v[28:31]\n"
"s_add_u32 s44, s95, 299008\n"
"buffer_load_dwordx4 v[192:195], v1, s[36:39], s44 offen offset:0 sc1\n"
"v_mfma_f32_16x16x32_bf16 v[28:31], v[204:207], a[240:243], v[28:31]\n"
"s_cmp_eq_u32 s8, 8\n"
"s_cselect_b32 s8, 1, s8\n"
"v_add_f32 v90, 1.0, v90\n"
"v_add_f32 v91, 1.0, v91\n"
"v_mfma_f32_16x16x32_bf16 v[32:35], v[200:203], a[228:231], v[32:35]\n"
"s_add_u32 s9, s5, s8\n"
"v_rcp_f32 v90, v90\n"
"v_mfma_f32_16x16x32_bf16 v[32:35], v[204:207], a[244:247], v[32:35]\n"
"s_and_b32 s9, s9, 7\n"
"v_rcp_f32 v91, v91\n"
"v_mfma_f32_16x16x32_bf16 v[36:39], v[200:203], a[232:235], v[36:39]\n"
"s_mul_i32 s9, s9, 73728\n"
"buffer_load_dwordx4 v[196:199], v1, s[36:39], s44 offen offset:1024 sc1\n"
"v_mfma_f32_16x16x32_bf16 v[36:39], v[204:207], a[248:251], v[36:39]\n"
"s_add_u32 s90, s10, s9\n"
"v_mul_f32 v90, v56, v90\n"
"v_mul_f32 v91, v57, v91\n"
"v_mfma_f32_16x16x32_bf16 v[40:43], v[200:203], a[236:239], v[40:43]\n"
"s_add_u32 s31, s12, s9\n"
"v_exp_f32 v92, v58\n"
"v_mfma_f32_16x16x32_bf16 v[40:43], v[204:207], a[252:255], v[40:43]\n"
"s_waitcnt vmcnt(24)\n"
"v_exp_f32 v93, v59\n"
"v_mfma_f32_16x16x32_bf16 v[28:31], v[208:211], v[224:227], v[28:31]\n"
"s_add_u32 s44, s95, 301056\n"
"buffer_load_dwordx4 v[200:203], v1, s[36:39], s44 offen offset:0 sc1\n"
"v_mfma_f32_16x16x32_bf16 v[44:47], v[212:215], v[240:243], v[28:31]\n"
"v_add_f32 v92, 1.0, v92\n"
"v_add_f32 v93, 1.0, v93\n"
"v_mfma_f32_16x16x32_bf16 v[32:35], v[208:211], v[228:231], v[32:35]\n"
"v_rcp_f32 v92, v92\n"
"v_mfma_f32_16x16x32_bf16 v[48:51], v[212:215], v[244:247], v[32:35]\n"
"v_rcp_f32 v93, v93\n"
"v_mfma_f32_16x16x32_bf16 v[36:39], v[208:211], v[232:235], v[36:39]\n"
"buffer_load_dwordx4 v[204:207], v1, s[36:39], s44 offen offset:1024 sc1\n"
"v_mfma_f32_16x16x32_bf16 v[52:55], v[212:215], v[248:251], v[36:39]\n"
"v_mul_f32 v92, v58, v92\n"
"v_mul_f32 v93, v59, v93\n"
"v_mfma_f32_16x16x32_bf16 v[40:43], v[208:211], v[236:239], v[40:43]\n"
"ds_write_b64 v16, v[64:65] offset:10240\n"
"v_cvt_pk_bf16_f32 v66, v90, v91\n"
"v_mfma_f32_16x16x32_bf16 v[56:59], v[212:215], v[252:255], v[40:43]\n"
"s_waitcnt vmcnt(24)\n"
"v_cvt_pk_bf16_f32 v67, v92, v93\n"
"ds_write_b64 v16, v[66:67] offset:11264\n"
"v_mfma_f32_16x16x32_bf16 v[28:31], v[216:219], a[0:3], 0\n"
"s_add_u32 s44, s95, 303104\n"
"buffer_load_dwordx4 v[208:211], v1, s[36:39], s44 offen offset:0 sc1\n"
"v_mfma_f32_16x16x32_bf16 v[28:31], v[220:223], a[16:19], v[28:31]\n"
"v_exp_f32 v86, v44\n"
"v_mfma_f32_16x16x32_bf16 v[32:35], v[216:219], a[4:7], 0\n"
"v_exp_f32 v87, v45\n"
"v_mfma_f32_16x16x32_bf16 v[32:35], v[220:223], a[20:23], v[32:35]\n"
"v_add_f32 v86, 1.0, v86\n"
"v_add_f32 v87, 1.0, v87\n"
"v_mfma_f32_16x16x32_bf16 v[36:39], v[216:219], a[8:11], 0\n"
"s_sub_u32 s9, s5, s8\n"
"buffer_load_dwordx4 v[212:215], v1, s[36:39], s44 offen offset:1024 sc1\n"
"v_mfma_f32_16x16x32_bf16 v[36:39], v[220:223], a[24:27], v[36:39]\n"
"s_and_b32 s9, s9, 7\n"
"v_rcp_f32 v86, v86\n"
"v_mfma_f32_16x16x32_bf16 v[40:43], v[216:219], a[12:15], 0\n"
"s_mul_i32 s9, s9, 9216\n"
"v_rcp_f32 v87, v87\n"
"v_mfma_f32_16x16x32_bf16 v[40:43], v[220:223], a[28:31], v[40:43]\n"
"s_waitcnt vmcnt(24)\n"
"v_mul_f32 v86, v44, v86\n"
"v_mul_f32 v87, v45, v87\n"
"v_mfma_f32_16x16x32_bf16 v[28:31], v[112:115], a[32:35], v[28:31]\n"
"s_add_u32 s44, s95, 305152\n"
"buffer_load_dwordx4 v[216:219], v1, s[36:39], s44 offen offset:0 sc1\n"
"v_mfma_f32_16x16x32_bf16 v[28:31], v[116:119], a[48:51], v[28:31]\n"
"s_add_u32 s62, s11, s9\n"
"v_exp_f32 v88, v46\n"
"v_mfma_f32_16x16x32_bf16 v[32:35], v[112:115], a[36:39], v[32:35]\n"
"s_mul_i32 s11, s4, 81920\n"
"v_exp_f32 v89, v47\n"
"v_mfma_f32_16x16x32_bf16 v[32:35], v[116:119], a[52:55], v[32:35]\n"
"s_add_u32 s11, s11, 4980736\n"
"v_add_f32 v88, 1.0, v88\n"
"v_add_f32 v89, 1.0, v89\n"
"v_mfma_f32_16x16x32_bf16 v[36:39], v[112:115], a[40:43], v[36:39]\n"
"s_mul_i32 s12, s5, 10240\n"
"buffer_load_dwordx4 v[220:223], v1, s[36:39], s44 offen offset:1024 sc1\n"
"v_mfma_f32_16x16x32_bf16 v[36:39], v[116:119], a[56:59], v[36:39]\n"
"s_add_u32 s66, s11, s12\n"
"v_rcp_f32 v88, v88\n"
"v_mfma_f32_16x16x32_bf16 v[40:43], v[112:115], a[44:47], v[40:43]\n"
"s_add_u32 s67, s66, 5120\n"
"v_rcp_f32 v89, v89\n"
"v_mfma_f32_16x16x32_bf16 v[40:43], v[116:119], a[60:63], v[40:43]\n"
"s_waitcnt vmcnt(24)\n"
"v_mul_f32 v88, v46, v88\n"
"v_mul_f32 v89, v47, v89\n"
"v_mfma_f32_16x16x32_bf16 v[28:31], v[120:123], a[64:67], v[28:31]\n"
"s_add_u32 s44, s95, 307200\n"
"buffer_load_dwordx4 v[112:115], v1, s[36:39], s44 offen offset:0 sc1\n"
"v_mfma_f32_16x16x32_bf16 v[28:31], v[124:127], a[80:83], v[28:31]\n"
"s_mul_i32 s12, s6, 20480\n"
"v_cvt_pk_bf16_f32 v60, v86, v87\n"
"v_cvt_pk_bf16_f32 v61, v88, v89\n"
"v_mfma_f32_16x16x32_bf16 v[32:35], v[120:123], a[68:71], v[32:35]\n"
"s_add_u32 s11, s11, s12\n"
"v_exp_f32 v90, v48\n"
"v_mfma_f32_16x16x32_bf16 v[32:35], v[124:127], a[84:87], v[32:35]\n"
"s_mul_i32 s12, s5, 1280\n"
"v_exp_f32 v91, v49\n"
"v_mfma_f32_16x16x32_bf16 v[36:39], v[120:123], a[72:75], v[36:39]\n"
"s_add_u32 s64, s11, s12\n"
"buffer_load_dwordx4 v[116:119], v1, s[36:39], s44 offen offset:1024 sc1\n"
"v_mfma_f32_16x16x32_bf16 v[36:39], v[124:127], a[88:91], v[36:39]\n"
"s_add_u32 s65, s64, 10240\n"
"v_add_f32 v90, 1.0, v90\n"
"v_add_f32 v91, 1.0, v91\n"
"v_mfma_f32_16x16x32_bf16 v[40:43], v[120:123], a[76:79], v[40:43]\n"
"v_rcp_f32 v90, v90\n"
"v_mfma_f32_16x16x32_bf16 v[40:43], v[124:127], a[92:95], v[40:43]\n"
"s_waitcnt vmcnt(24)\n"
"v_rcp_f32 v91, v91\n"
"v_mfma_f32_16x16x32_bf16 v[28:31], v[128:131], a[96:99], v[28:31]\n"
"s_add_u32 s44, s95, 309248\n"
"buffer_load_dwordx4 v[120:123], v1, s[36:39], s44 offen offset:0 sc1\n"
"v_mfma_f32_16x16x32_bf16 v[28:31], v[132:135], a[112:115], v[28:31]\n"
"v_mul_f32 v90, v48, v90\n"
"v_mul_f32 v91, v49, v91\n"
"v_mfma_f32_16x16x32_bf16 v[32:35], v[128:131], a[100:103], v[32:35]\n"
"v_exp_f32 v92, v50\n"
"v_mfma_f32_16x16x32_bf16 v[32:35], v[132:135], a[116:119], v[32:35]\n"
"v_exp_f32 v93, v51\n"
"v_mfma_f32_16x16x32_bf16 v[36:39], v[128:131], a[104:107], v[36:39]\n"
"buffer_load_dwordx4 v[124:127], v1, s[36:39], s44 offen offset:1024 sc1\n"
"v_mfma_f32_16x16x32_bf16 v[36:39], v[132:135], a[120:123], v[36:39]\n"
"v_add_f32 v92, 1.0, v92\n"
"v_add_f32 v93, 1.0, v93\n"
"v_mfma_f32_16x16x32_bf16 v[40:43], v[128:131], a[108:111], v[40:43]\n"
"v_rcp_f32 v92, v92\n"
"v_mfma_f32_16x16x32_bf16 v[40:43], v[132:135], a[124:127], v[40:43]\n"
"s_waitcnt vmcnt(24)\n"
"v_rcp_f32 v93, v93\n"
"v_mfma_f32_16x16x32_bf16 v[28:31], v[136:139], a[128:131], v[28:31]\n"
"s_add_u32 s44, s95, 311296\n"
"buffer_load_dwordx4 v[128:131], v1, s[36:39], s44 offen offset:0 sc1\n"
"v_mfma_f32_16x16x32_bf16 v[28:31], v[140:143], a[144:147], v[28:31]\n"
"v_mul_f32 v92, v50, v92\n"
"v_mul_f32 v93, v51, v93\n"
"v_mfma_f32_16x16x32_bf16 v[32:35], v[136:139], a[132:135], v[32:35]\n"
"ds_write_b64 v16, v[60:61] offset:16384\n"
"v_cvt_pk_bf16_f32 v62, v90, v91\n"
"v_mfma_f32_16x16x32_bf16 v[32:35], v[140:143], a[148:151], v[32:35]\n"
"v_cvt_pk_bf16_f32 v63, v92, v93\n"
"ds_write_b64 v16, v[62:63] offset:17408\n"
"v_mfma_f32_16x16x32_bf16 v[36:39], v[136:139], a[136:139], v[36:39]\n"
"buffer_load_dwordx4 v[132:135], v1, s[36:39], s44 offen offset:1024 sc1\n"
"v_mfma_f32_16x16x32_bf16 v[36:39], v[140:143], a[152:155], v[36:39]\n"
"v_exp_f32 v86, v52\n"
"v_mfma_f32_16x16x32_bf16 v[40:43], v[136:139], a[140:143], v[40:43]\n"
"v_exp_f32 v87, v53\n"
"v_mfma_f32_16x16x32_bf16 v[40:43], v[140:143], a[156:159], v[40:43]\n"
"s_waitcnt vmcnt(24)\n"
"v_add_f32 v86, 1.0, v86\n"
"v_add_f32 v87, 1.0, v87\n"
"v_mfma_f32_16x16x32_bf16 v[28:31], v[144:147], a[160:163], v[28:31]\n"
"s_add_u32 s44, s95, 368640\n"
"buffer_load_dwordx4 v[136:139], v1, s[36:39], s44 offen offset:0 sc1\n"
"v_mfma_f32_16x16x32_bf16 v[28:31], v[148:151], a[176:179], v[28:31]\n"
"v_rcp_f32 v86, v86\n"
"v_mfma_f32_16x16x32_bf16 v[32:35], v[144:147], a[164:167], v[32:35]\n"
"v_rcp_f32 v87, v87\n"
"v_mfma_f32_16x16x32_bf16 v[32:35], v[148:151], a[180:183], v[32:35]\n"
"v_mul_f32 v86, v52, v86\n"
"v_mul_f32 v87, v53, v87\n"
"v_mfma_f32_16x16x32_bf16 v[36:39], v[144:147], a[168:171], v[36:39]\n"
"buffer_load_dwordx4 v[140:143], v1, s[36:39], s44 offen offset:1024 sc1\n"
"v_mfma_f32_16x16x32_bf16 v[36:39], v[148:151], a[184:187], v[36:39]\n"
"v_exp_f32 v88, v54\n"
"v_mfma_f32_16x16x32_bf16 v[40:43], v[144:147], a[172:175], v[40:43]\n"
"v_exp_f32 v89, v55\n"
"v_mfma_f32_16x16x32_bf16 v[40:43], v[148:151], a[188:191], v[40:43]\n"
"s_waitcnt vmcnt(24)\n"
"v_add_f32 v88, 1.0, v88\n"
"v_add_f32 v89, 1.0, v89\n"
"v_mfma_f32_16x16x32_bf16 v[28:31], v[152:155], a[192:195], v[28:31]\n"
"s_add_u32 s44, s95, 370688\n"
"buffer_load_dwordx4 v[144:147], v1, s[36:39], s44 offen offset:0 sc1\n"
"v_mfma_f32_16x16x32_bf16 v[28:31], v[156:159], a[208:211], v[28:31]\n"
"v_rcp_f32 v88, v88\n"
"v_mfma_f32_16x16x32_bf16 v[32:35], v[152:155], a[196:199], v[32:35]\n"
"v_rcp_f32 v89, v89\n"
"v_mfma_f32_16x16x32_bf16 v[32:35], v[156:159], a[212:215], v[32:35]\n"
"v_mul_f32 v88, v54, v88\n"
"v_mul_f32 v89, v55, v89\n"
"v_mfma_f32_16x16x32_bf16 v[36:39], v[152:155], a[200:203], v[36:39]\n"
"buffer_load_dwordx4 v[148:151], v1, s[36:39], s44 offen offset:1024 sc1\n"
"v_mfma_f32_16x16x32_bf16 v[36:39], v[156:159], a[216:219], v[36:39]\n"
"v_cvt_pk_bf16_f32 v64, v86, v87\n"
"v_cvt_pk_bf16_f32 v65, v88, v89\n"
"v_mfma_f32_16x16x32_bf16 v[40:43], v[152:155], a[204:207], v[40:43]\n"
"v_exp_f32 v90, v56\n"
"v_mfma_f32_16x16x32_bf16 v[40:43], v[156:159], a[220:223], v[40:43]\n"
"s_waitcnt vmcnt(24)\n"
"v_exp_f32 v91, v57\n"
"v_mfma_f32_16x16x32_bf16 v[28:31], v[160:163], a[224:227], v[28:31]\n"
"s_add_u32 s44, s95, 372736\n"
"buffer_load_dwordx4 v[152:155], v1, s[36:39], s44 offen offset:0 sc1\n"
"v_mfma_f32_16x16x32_bf16 v[28:31], v[164:167], a[240:243], v[28:31]\n"
"v_add_f32 v90, 1.0, v90\n"
"v_add_f32 v91, 1.0, v91\n"
"v_mfma_f32_16x16x32_bf16 v[32:35], v[160:163], a[228:231], v[32:35]\n"
"v_rcp_f32 v90, v90\n"
"v_mfma_f32_16x16x32_bf16 v[32:35], v[164:167], a[244:247], v[32:35]\n"
"v_rcp_f32 v91, v91\n"
"v_mfma_f32_16x16x32_bf16 v[36:39], v[160:163], a[232:235], v[36:39]\n"
"buffer_load_dwordx4 v[156:159], v1, s[36:39], s44 offen offset:1024 sc1\n"
"v_mfma_f32_16x16x32_bf16 v[36:39], v[164:167], a[248:251], v[36:39]\n"
"v_mul_f32 v90, v56, v90\n"
"v_mul_f32 v91, v57, v91\n"
"v_mfma_f32_16x16x32_bf16 v[40:43], v[160:163], a[236:239], v[40:43]\n"
"v_exp_f32 v92, v58\n"
"v_mfma_f32_16x16x32_bf16 v[40:43], v[164:167], a[252:255], v[40:43]\n"
"s_waitcnt vmcnt(24)\n"
"v_exp_f32 v93, v59\n"
"v_mfma_f32_16x16x32_bf16 v[28:31], v[168:171], v[224:227], v[28:31]\n"
"s_add_u32 s44, s95, 374784\n"
"buffer_load_dwordx4 v[160:163], v1, s[36:39], s44 offen offset:0 sc1\n"
"v_mfma_f32_16x16x32_bf16 v[44:47], v[172:175], v[240:243], v[28:31]\n"
"v_add_f32 v92, 1.0, v92\n"
"v_add_f32 v93, 1.0, v93\n"
"v_mfma_f32_16x16x32_bf16 v[32:35], v[168:171], v[228:231], v[32:35]\n"
"v_rcp_f32 v92, v92\n"
"v_mfma_f32_16x16x32_bf16 v[48:51], v[172:175], v[244:247], v[32:35]\n"
"v_rcp_f32 v93, v93\n"
"v_mfma_f32_16x16x32_bf16 v[36:39], v[168:171], v[232:235], v[36:39]\n"
"buffer_load_dwordx4 v[164:167], v1, s[36:39], s44 offen offset:1024 sc1\n"
"v_mfma_f32_16x16x32_bf16 v[52:55], v[172:175], v[248:251], v[36:39]\n"
"v_mul_f32 v92, v58, v92\n"
"v_mul_f32 v93, v59, v93\n"
"v_mfma_f32_16x16x32_bf16 v[40:43], v[168:171], v[236:239], v[40:43]\n"
"ds_write_b64 v16, v[64:65] offset:18432\n"
"v_cvt_pk_bf16_f32 v66, v90, v91\n"
"v_mfma_f32_16x16x32_bf16 v[56:59], v[172:175], v[252:255], v[40:43]\n"
"s_waitcnt vmcnt(24)\n"
"v_cvt_pk_bf16_f32 v67, v92, v93\n"
"ds_write_b64 v16, v[66:67] offset:19456\n"
"v_mfma_f32_16x16x32_bf16 v[28:31], v[176:179], a[0:3], 0\n"
"s_add_u32 s44, s95, 376832\n"
"buffer_load_dwordx4 v[168:171], v1, s[36:39], s44 offen offset:0 sc1\n"
"v_mfma_f32_16x16x32_bf16 v[28:31], v[180:183], a[16:19], v[28:31]\n"
"v_exp_f32 v86, v44\n"
"v_mfma_f32_16x16x32_bf16 v[32:35], v[176:179], a[4:7], 0\n"
"v_exp_f32 v87, v45\n"
"v_mfma_f32_16x16x32_bf16 v[32:35], v[180:183], a[20:23], v[32:35]\n"
"v_add_f32 v86, 1.0, v86\n"
"v_add_f32 v87, 1.0, v87\n"
"v_mfma_f32_16x16x32_bf16 v[36:39], v[176:179], a[8:11], 0\n"
"buffer_load_dwordx4 v[172:175], v1, s[36:39], s44 offen offset:1024 sc1\n"
"v_mfma_f32_16x16x32_bf16 v[36:39], v[180:183], a[24:27], v[36:39]\n"
"v_rcp_f32 v86, v86\n"
"v_mfma_f32_16x16x32_bf16 v[40:43], v[176:179], a[12:15], 0\n"
"v_rcp_f32 v87, v87\n"
"v_mfma_f32_16x16x32_bf16 v[40:43], v[180:183], a[28:31], v[40:43]\n"
"s_waitcnt vmcnt(24)\n"
"v_mul_f32 v86, v44, v86\n"
"v_mul_f32 v87, v45, v87\n"
"v_mfma_f32_16x16x32_bf16 v[28:31], v[184:187], a[32:35], v[28:31]\n"
"s_add_u32 s44, s95, 378880\n"
"buffer_load_dwordx4 v[176:179], v1, s[36:39], s44 offen offset:0 sc1\n"
"v_mfma_f32_16x16x32_bf16 v[28:31], v[188:191], a[48:51], v[28:31]\n"
"v_exp_f32 v88, v46\n"
"v_mfma_f32_16x16x32_bf16 v[32:35], v[184:187], a[36:39], v[32:35]\n"
"v_exp_f32 v89, v47\n"
"v_mfma_f32_16x16x32_bf16 v[32:35], v[188:191], a[52:55], v[32:35]\n"
"v_add_f32 v88, 1.0, v88\n"
"v_add_f32 v89, 1.0, v89\n"
"v_mfma_f32_16x16x32_bf16 v[36:39], v[184:187], a[40:43], v[36:39]\n"
"buffer_load_dwordx4 v[180:183], v1, s[36:39], s44 offen offset:1024 sc1\n"
"v_mfma_f32_16x16x32_bf16 v[36:39], v[188:191], a[56:59], v[36:39]\n"
"v_rcp_f32 v88, v88\n"
"v_mfma_f32_16x16x32_bf16 v[40:43], v[184:187], a[44:47], v[40:43]\n"
"v_rcp_f32 v89, v89\n"
"v_mfma_f32_16x16x32_bf16 v[40:43], v[188:191], a[60:63], v[40:43]\n"
"s_waitcnt vmcnt(24)\n"
"v_mul_f32 v88, v46, v88\n"
"v_mul_f32 v89, v47, v89\n"
"v_mfma_f32_16x16x32_bf16 v[28:31], v[192:195], a[64:67], v[28:31]\n"
"s_add_u32 s44, s95, 380928\n"
"buffer_load_dwordx4 v[184:187], v1, s[36:39], s44 offen offset:0 sc1\n"
"v_mfma_f32_16x16x32_bf16 v[28:31], v[196:199], a[80:83], v[28:31]\n"
"v_cvt_pk_bf16_f32 v60, v86, v87\n"
"v_cvt_pk_bf16_f32 v61, v88, v89\n"
"v_mfma_f32_16x16x32_bf16 v[32:35], v[192:195], a[68:71], v[32:35]\n"
"v_exp_f32 v90, v48\n"
"v_mfma_f32_16x16x32_bf16 v[32:35], v[196:199], a[84:87], v[32:35]\n"
"v_exp_f32 v91, v49\n"
"v_mfma_f32_16x16x32_bf16 v[36:39], v[192:195], a[72:75], v[36:39]\n"
"buffer_load_dwordx4 v[188:191], v1, s[36:39], s44 offen offset:1024 sc1\n"
"v_mfma_f32_16x16x32_bf16 v[36:39], v[196:199], a[88:91], v[36:39]\n"
"v_add_f32 v90, 1.0, v90\n"
"v_add_f32 v91, 1.0, v91\n"
"v_mfma_f32_16x16x32_bf16 v[40:43], v[192:195], a[76:79], v[40:43]\n"
"v_rcp_f32 v90, v90\n"
"v_mfma_f32_16x16x32_bf16 v[40:43], v[196:199], a[92:95], v[40:43]\n"
"s_waitcnt vmcnt(24)\n"
"v_rcp_f32 v91, v91\n"
"v_mfma_f32_16x16x32_bf16 v[28:31], v[200:203], a[96:99], v[28:31]\n"
"s_add_u32 s44, s95, 382976\n"
"buffer_load_dwordx4 v[192:195], v1, s[36:39], s44 offen offset:0 sc1\n"
"v_mfma_f32_16x16x32_bf16 v[28:31], v[204:207], a[112:115], v[28:31]\n"
"v_mul_f32 v90, v48, v90\n"
"v_mul_f32 v91, v49, v91\n"
"v_mfma_f32_16x16x32_bf16 v[32:35], v[200:203], a[100:103], v[32:35]\n"
"v_exp_f32 v92, v50\n"
"v_mfma_f32_16x16x32_bf16 v[32:35], v[204:207], a[116:119], v[32:35]\n"
"v_exp_f32 v93, v51\n"
"v_mfma_f32_16x16x32_bf16 v[36:39], v[200:203], a[104:107], v[36:39]\n"
"buffer_load_dwordx4 v[196:199], v1, s[36:39], s44 offen offset:1024 sc1\n"
"v_mfma_f32_16x16x32_bf16 v[36:39], v[204:207], a[120:123], v[36:39]\n"
"v_add_f32 v92, 1.0, v92\n"
"v_add_f32 v93, 1.0, v93\n"
"v_mfma_f32_16x16x32_bf16 v[40:43], v[200:203], a[108:111], v[40:43]\n"
"v_rcp_f32 v92, v92\n"
"v_mfma_f32_16x16x32_bf16 v[40:43], v[204:207], a[124:127], v[40:43]\n"
"s_waitcnt vmcnt(24)\n"
"v_rcp_f32 v93, v93\n"
"v_mfma_f32_16x16x32_bf16 v[28:31], v[208:211], a[128:131], v[28:31]\n"
"s_add_u32 s44, s95, 385024\n"
"buffer_load_dwordx4 v[200:203], v1, s[36:39], s44 offen offset:0 sc1\n"
"v_mfma_f32_16x16x32_bf16 v[28:31], v[212:215], a[144:147], v[28:31]\n"
"v_mul_f32 v92, v50, v92\n"
"v_mul_f32 v93, v51, v93\n"
"v_mfma_f32_16x16x32_bf16 v[32:35], v[208:211], a[132:135], v[32:35]\n"
"ds_write_b64 v16, v[60:61] offset:24576\n"
"v_cvt_pk_bf16_f32 v62, v90, v91\n"
"v_mfma_f32_16x16x32_bf16 v[32:35], v[212:215], a[148:151], v[32:35]\n"
"v_cvt_pk_bf16_f32 v63, v92, v93\n"
"ds_write_b64 v16, v[62:63] offset:25600\n"
"v_mfma_f32_16x16x32_bf16 v[36:39], v[208:211], a[136:139], v[36:39]\n"
"buffer_load_dwordx4 v[204:207], v1, s[36:39], s44 offen offset:1024 sc1\n"
"v_mfma_f32_16x16x32_bf16 v[36:39], v[212:215], a[152:155], v[36:39]\n"
"v_exp_f32 v86, v52\n"
"v_mfma_f32_16x16x32_bf16 v[40:43], v[208:211], a[140:143], v[40:43]\n"
"v_exp_f32 v87, v53\n"
"v_mfma_f32_16x16x32_bf16 v[40:43], v[212:215], a[156:159], v[40:43]\n"
"s_waitcnt vmcnt(24)\n"
"v_add_f32 v86, 1.0, v86\n"
"v_add_f32 v87, 1.0, v87\n"
"v_mfma_f32_16x16x32_bf16 v[28:31], v[216:219], a[160:163], v[28:31]\n"
"s_add_u32 s44, s95, 442368\n"
"buffer_load_dwordx4 v[208:211], v1, s[36:39], s44 offen offset:0 sc1\n"
"v_mfma_f32_16x16x32_bf16 v[28:31], v[220:223], a[176:179], v[28:31]\n"
"v_rcp_f32 v86, v86\n"
"v_mfma_f32_16x16x32_bf16 v[32:35], v[216:219], a[164:167], v[32:35]\n"
"v_rcp_f32 v87, v87\n"
"v_mfma_f32_16x16x32_bf16 v[32:35], v[220:223], a[180:183], v[32:35]\n"
"v_mul_f32 v86, v52, v86\n"
"v_mul_f32 v87, v53, v87\n"
"v_mfma_f32_16x16x32_bf16 v[36:39], v[216:219], a[168:171], v[36:39]\n"
"buffer_load_dwordx4 v[212:215], v1, s[36:39], s44 offen offset:1024 sc1\n"
"v_mfma_f32_16x16x32_bf16 v[36:39], v[220:223], a[184:187], v[36:39]\n"
"v_exp_f32 v88, v54\n"
"v_mfma_f32_16x16x32_bf16 v[40:43], v[216:219], a[172:175], v[40:43]\n"
"v_exp_f32 v89, v55\n"
"v_mfma_f32_16x16x32_bf16 v[40:43], v[220:223], a[188:191], v[40:43]\n"
"s_waitcnt vmcnt(24)\n"
"v_add_f32 v88, 1.0, v88\n"
"v_add_f32 v89, 1.0, v89\n"
"v_mfma_f32_16x16x32_bf16 v[28:31], v[112:115], a[192:195], v[28:31]\n"
"s_add_u32 s44, s95, 444416\n"
"buffer_load_dwordx4 v[216:219], v1, s[36:39], s44 offen offset:0 sc1\n"
"v_mfma_f32_16x16x32_bf16 v[28:31], v[116:119], a[208:211], v[28:31]\n"
"v_rcp_f32 v88, v88\n"
"v_mfma_f32_16x16x32_bf16 v[32:35], v[112:115], a[196:199], v[32:35]\n"
"v_rcp_f32 v89, v89\n"
"v_mfma_f32_16x16x32_bf16 v[32:35], v[116:119], a[212:215], v[32:35]\n"
"v_mul_f32 v88, v54, v88\n"
"v_mul_f32 v89, v55, v89\n"
"v_mfma_f32_16x16x32_bf16 v[36:39], v[112:115], a[200:203], v[36:39]\n"
"buffer_load_dwordx4 v[220:223], v1, s[36:39], s44 offen offset:1024 sc1\n"
"v_mfma_f32_16x16x32_bf16 v[36:39], v[116:119], a[216:219], v[36:39]\n"
"v_cvt_pk_bf16_f32 v64, v86, v87\n"
"v_cvt_pk_bf16_f32 v65, v88, v89\n"
"v_mfma_f32_16x16x32_bf16 v[40:43], v[112:115], a[204:207], v[40:43]\n"
"v_exp_f32 v90, v56\n"
"v_mfma_f32_16x16x32_bf16 v[40:43], v[116:119], a[220:223], v[40:43]\n"
"s_waitcnt vmcnt(24)\n"
"v_exp_f32 v91, v57\n"
"v_mfma_f32_16x16x32_bf16 v[28:31], v[120:123], a[224:227], v[28:31]\n"
"s_add_u32 s44, s95, 446464\n"
"buffer_load_dwordx4 v[112:115], v1, s[36:39], s44 offen offset:0 sc1\n"
"v_mfma_f32_16x16x32_bf16 v[28:31], v[124:127], a[240:243], v[28:31]\n"
"v_add_f32 v90, 1.0, v90\n"
"v_add_f32 v91, 1.0, v91\n"
"v_mfma_f32_16x16x32_bf16 v[32:35], v[120:123], a[228:231], v[32:35]\n"
"v_rcp_f32 v90, v90\n"
"v_mfma_f32_16x16x32_bf16 v[32:35], v[124:127], a[244:247], v[32:35]\n"
"v_rcp_f32 v91, v91\n"
"v_mfma_f32_16x16x32_bf16 v[36:39], v[120:123], a[232:235], v[36:39]\n"
"buffer_load_dwordx4 v[116:119], v1, s[36:39], s44 offen offset:1024 sc1\n"
"v_mfma_f32_16x16x32_bf16 v[36:39], v[124:127], a[248:251], v[36:39]\n"
"v_mul_f32 v90, v56, v90\n"
"v_mul_f32 v91, v57, v91\n"
"v_mfma_f32_16x16x32_bf16 v[40:43], v[120:123], a[236:239], v[40:43]\n"
"v_exp_f32 v92, v58\n"
"v_mfma_f32_16x16x32_bf16 v[40:43], v[124:127], a[252:255], v[40:43]\n"
"s_waitcnt vmcnt(24)\n"
"v_exp_f32 v93, v59\n"
"v_mfma_f32_16x16x32_bf16 v[28:31], v[128:131], v[224:227], v[28:31]\n"
"s_add_u32 s44, s95, 448512\n"
"buffer_load_dwordx4 v[120:123], v1, s[36:39], s44 offen offset:0 sc1\n"
"v_mfma_f32_16x16x32_bf16 v[44:47], v[132:135], v[240:243], v[28:31]\n"
"v_add_f32 v92, 1.0, v92\n"
"v_add_f32 v93, 1.0, v93\n"
"v_mfma_f32_16x16x32_bf16 v[32:35], v[128:131], v[228:231], v[32:35]\n"
"v_rcp_f32 v92, v92\n"
"v_mfma_f32_16x16x32_bf16 v[48:51], v[132:135], v[244:247], v[32:35]\n"
"v_rcp_f32 v93, v93\n"
"v_mfma_f32_16x16x32_bf16 v[36:39], v[128:131], v[232:235], v[36:39]\n"
"buffer_load_dwordx4 v[124:127], v1, s[36:39], s44 offen offset:1024 sc1\n"
"v_mfma_f32_16x16x32_bf16 v[52:55], v[132:135], v[248:251], v[36:39]\n"
"v_mul_f32 v92, v58, v92\n"
"v_mul_f32 v93, v59, v93\n"
"v_mfma_f32_16x16x32_bf16 v[40:43], v[128:131], v[236:239], v[40:43]\n"
"ds_write_b64 v16, v[64:65] offset:26624\n"
"v_cvt_pk_bf16_f32 v66, v90, v91\n"
"v_mfma_f32_16x16x32_bf16 v[56:59], v[132:135], v[252:255], v[40:43]\n"
"s_waitcnt vmcnt(24)\n"
"v_cvt_pk_bf16_f32 v67, v92, v93\n"
"ds_write_b64 v16, v[66:67] offset:27648\n"
"v_mfma_f32_16x16x32_bf16 v[28:31], v[136:139], a[0:3], 0\n"
"s_add_u32 s44, s95, 450560\n"
"buffer_load_dwordx4 v[128:131], v1, s[36:39], s44 offen offset:0 sc1\n"
"v_mfma_f32_16x16x32_bf16 v[28:31], v[140:143], a[16:19], v[28:31]\n"
"v_exp_f32 v86, v44\n"
"v_mfma_f32_16x16x32_bf16 v[32:35], v[136:139], a[4:7], 0\n"
"v_exp_f32 v87, v45\n"
"v_mfma_f32_16x16x32_bf16 v[32:35], v[140:143], a[20:23], v[32:35]\n"
"v_add_f32 v86, 1.0, v86\n"
"v_add_f32 v87, 1.0, v87\n"
"v_mfma_f32_16x16x32_bf16 v[36:39], v[136:139], a[8:11], 0\n"
"buffer_load_dwordx4 v[132:135], v1, s[36:39], s44 offen offset:1024 sc1\n"
"v_mfma_f32_16x16x32_bf16 v[36:39], v[140:143], a[24:27], v[36:39]\n"
"v_rcp_f32 v86, v86\n"
"v_mfma_f32_16x16x32_bf16 v[40:43], v[136:139], a[12:15], 0\n"
"v_rcp_f32 v87, v87\n"
"v_mfma_f32_16x16x32_bf16 v[40:43], v[140:143], a[28:31], v[40:43]\n"
"s_waitcnt vmcnt(24)\n"
"v_mul_f32 v86, v44, v86\n"
"v_mul_f32 v87, v45, v87\n"
"v_mfma_f32_16x16x32_bf16 v[28:31], v[144:147], a[32:35], v[28:31]\n"
"s_add_u32 s44, s95, 452608\n"
"buffer_load_dwordx4 v[136:139], v1, s[36:39], s44 offen offset:0 sc1\n"
"v_mfma_f32_16x16x32_bf16 v[28:31], v[148:151], a[48:51], v[28:31]\n"
"v_exp_f32 v88, v46\n"
"v_mfma_f32_16x16x32_bf16 v[32:35], v[144:147], a[36:39], v[32:35]\n"
"v_exp_f32 v89, v47\n"
"v_mfma_f32_16x16x32_bf16 v[32:35], v[148:151], a[52:55], v[32:35]\n"
"v_add_f32 v88, 1.0, v88\n"
"v_add_f32 v89, 1.0, v89\n"
"v_mfma_f32_16x16x32_bf16 v[36:39], v[144:147], a[40:43], v[36:39]\n"
"buffer_load_dwordx4 v[140:143], v1, s[36:39], s44 offen offset:1024 sc1\n"
"v_mfma_f32_16x16x32_bf16 v[36:39], v[148:151], a[56:59], v[36:39]\n"
"v_rcp_f32 v88, v88\n"
"v_mfma_f32_16x16x32_bf16 v[40:43], v[144:147], a[44:47], v[40:43]\n"
"v_rcp_f32 v89, v89\n"
"v_mfma_f32_16x16x32_bf16 v[40:43], v[148:151], a[60:63], v[40:43]\n"
"s_waitcnt vmcnt(24)\n"
"v_mul_f32 v88, v46, v88\n"
"v_mul_f32 v89, v47, v89\n"
"v_mfma_f32_16x16x32_bf16 v[28:31], v[152:155], a[64:67], v[28:31]\n"
"s_add_u32 s44, s95, 454656\n"
"buffer_load_dwordx4 v[144:147], v1, s[36:39], s44 offen offset:0 sc1\n"
"v_mfma_f32_16x16x32_bf16 v[28:31], v[156:159], a[80:83], v[28:31]\n"
"v_cvt_pk_bf16_f32 v60, v86, v87\n"
"v_cvt_pk_bf16_f32 v61, v88, v89\n"
"v_mfma_f32_16x16x32_bf16 v[32:35], v[152:155], a[68:71], v[32:35]\n"
"v_exp_f32 v90, v48\n"
"v_mfma_f32_16x16x32_bf16 v[32:35], v[156:159], a[84:87], v[32:35]\n"
"v_exp_f32 v91, v49\n"
"v_mfma_f32_16x16x32_bf16 v[36:39], v[152:155], a[72:75], v[36:39]\n"
"buffer_load_dwordx4 v[148:151], v1, s[36:39], s44 offen offset:1024 sc1\n"
"v_mfma_f32_16x16x32_bf16 v[36:39], v[156:159], a[88:91], v[36:39]\n"
"v_add_f32 v90, 1.0, v90\n"
"v_add_f32 v91, 1.0, v91\n"
"v_mfma_f32_16x16x32_bf16 v[40:43], v[152:155], a[76:79], v[40:43]\n"
"v_rcp_f32 v90, v90\n"
"v_mfma_f32_16x16x32_bf16 v[40:43], v[156:159], a[92:95], v[40:43]\n"
"s_waitcnt vmcnt(24)\n"
"v_rcp_f32 v91, v91\n"
"v_mfma_f32_16x16x32_bf16 v[28:31], v[160:163], a[96:99], v[28:31]\n"
"s_add_u32 s44, s95, 456704\n"
"buffer_load_dwordx4 v[152:155], v1, s[36:39], s44 offen offset:0 sc1\n"
"v_mfma_f32_16x16x32_bf16 v[28:31], v[164:167], a[112:115], v[28:31]\n"
"v_mul_f32 v90, v48, v90\n"
"v_mul_f32 v91, v49, v91\n"
"v_mfma_f32_16x16x32_bf16 v[32:35], v[160:163], a[100:103], v[32:35]\n"
"v_exp_f32 v92, v50\n"
"v_mfma_f32_16x16x32_bf16 v[32:35], v[164:167], a[116:119], v[32:35]\n"
"v_exp_f32 v93, v51\n"
"v_mfma_f32_16x16x32_bf16 v[36:39], v[160:163], a[104:107], v[36:39]\n"
"buffer_load_dwordx4 v[156:159], v1, s[36:39], s44 offen offset:1024 sc1\n"
"v_mfma_f32_16x16x32_bf16 v[36:39], v[164:167], a[120:123], v[36:39]\n"
"v_add_f32 v92, 1.0, v92\n"
"v_add_f32 v93, 1.0, v93\n"
"v_mfma_f32_16x16x32_bf16 v[40:43], v[160:163], a[108:111], v[40:43]\n"
"v_rcp_f32 v92, v92\n"
"v_mfma_f32_16x16x32_bf16 v[40:43], v[164:167], a[124:127], v[40:43]\n"
"s_waitcnt vmcnt(24)\n"
"v_rcp_f32 v93, v93\n"
"v_mfma_f32_16x16x32_bf16 v[28:31], v[168:171], a[128:131], v[28:31]\n"
"s_add_u32 s44, s95, 458752\n"
"buffer_load_dwordx4 v[160:163], v1, s[36:39], s44 offen offset:0 sc1\n"
"v_mfma_f32_16x16x32_bf16 v[28:31], v[172:175], a[144:147], v[28:31]\n"
"v_mul_f32 v92, v50, v92\n"
"v_mul_f32 v93, v51, v93\n"
"v_mfma_f32_16x16x32_bf16 v[32:35], v[168:171], a[132:135], v[32:35]\n"
"ds_write_b64 v16, v[60:61] offset:32768\n"
"v_cvt_pk_bf16_f32 v62, v90, v91\n"
"v_mfma_f32_16x16x32_bf16 v[32:35], v[172:175], a[148:151], v[32:35]\n"
"v_cvt_pk_bf16_f32 v63, v92, v93\n"
"ds_write_b64 v16, v[62:63] offset:33792\n"
"v_mfma_f32_16x16x32_bf16 v[36:39], v[168:171], a[136:139], v[36:39]\n"
"buffer_load_dwordx4 v[164:167], v1, s[36:39], s44 offen offset:1024 sc1\n"
"v_mfma_f32_16x16x32_bf16 v[36:39], v[172:175], a[152:155], v[36:39]\n"
"v_exp_f32 v86, v52\n"
"v_mfma_f32_16x16x32_bf16 v[40:43], v[168:171], a[140:143], v[40:43]\n"
"v_exp_f32 v87, v53\n"
"v_mfma_f32_16x16x32_bf16 v[40:43], v[172:175], a[156:159], v[40:43]\n"
"s_waitcnt vmcnt(24)\n"
"v_add_f32 v86, 1.0, v86\n"
"v_add_f32 v87, 1.0, v87\n"
"v_mfma_f32_16x16x32_bf16 v[28:31], v[176:179], a[160:163], v[28:31]\n"
"s_add_u32 s44, s95, 516096\n"
"buffer_load_dwordx4 v[168:171], v1, s[36:39], s44 offen offset:0 sc1\n"
"v_mfma_f32_16x16x32_bf16 v[28:31], v[180:183], a[176:179], v[28:31]\n"
"v_rcp_f32 v86, v86\n"
"v_mfma_f32_16x16x32_bf16 v[32:35], v[176:179], a[164:167], v[32:35]\n"
"v_rcp_f32 v87, v87\n"
"v_mfma_f32_16x16x32_bf16 v[32:35], v[180:183], a[180:183], v[32:35]\n"
"v_mul_f32 v86, v52, v86\n"
"v_mul_f32 v87, v53, v87\n"
"v_mfma_f32_16x16x32_bf16 v[36:39], v[176:179], a[168:171], v[36:39]\n"
"buffer_load_dwordx4 v[172:175], v1, s[36:39], s44 offen offset:1024 sc1\n"
"v_mfma_f32_16x16x32_bf16 v[36:39], v[180:183], a[184:187], v[36:39]\n"
"v_exp_f32 v88, v54\n"
"v_mfma_f32_16x16x32_bf16 v[40:43], v[176:179], a[172:175], v[40:43]\n"
"v_exp_f32 v89, v55\n"
"v_mfma_f32_16x16x32_bf16 v[40:43], v[180:183], a[188:191], v[40:43]\n"
"s_waitcnt vmcnt(24)\n"
"v_add_f32 v88, 1.0, v88\n"
"v_add_f32 v89, 1.0, v89\n"
"v_mfma_f32_16x16x32_bf16 v[28:31], v[184:187], a[192:195], v[28:31]\n"
"s_add_u32 s44, s95, 518144\n"
"buffer_load_dwordx4 v[176:179], v1, s[36:39], s44 offen offset:0 sc1\n"
"v_mfma_f32_16x16x32_bf16 v[28:31], v[188:191], a[208:211], v[28:31]\n"
"v_rcp_f32 v88, v88\n"
"v_mfma_f32_16x16x32_bf16 v[32:35], v[184:187], a[196:199], v[32:35]\n"
"v_rcp_f32 v89, v89\n"
"v_mfma_f32_16x16x32_bf16 v[32:35], v[188:191], a[212:215], v[32:35]\n"
"v_mul_f32 v88, v54, v88\n"
"v_mul_f32 v89, v55, v89\n"
"v_mfma_f32_16x16x32_bf16 v[36:39], v[184:187], a[200:203], v[36:39]\n"
"buffer_load_dwordx4 v[180:183], v1, s[36:39], s44 offen offset:1024 sc1\n"
"v_mfma_f32_16x16x32_bf16 v[36:39], v[188:191], a[216:219], v[36:39]\n"
"v_cvt_pk_bf16_f32 v64, v86, v87\n"
"v_cvt_pk_bf16_f32 v65, v88, v89\n"
"v_mfma_f32_16x16x32_bf16 v[40:43], v[184:187], a[204:207], v[40:43]\n"
"v_exp_f32 v90, v56\n"
"v_mfma_f32_16x16x32_bf16 v[40:43], v[188:191], a[220:223], v[40:43]\n"
"s_waitcnt vmcnt(24)\n"
"v_exp_f32 v91, v57\n"
"v_mfma_f32_16x16x32_bf16 v[28:31], v[192:195], a[224:227], v[28:31]\n"
"s_add_u32 s44, s95, 520192\n"
"buffer_load_dwordx4 v[184:187], v1, s[36:39], s44 offen offset:0 sc1\n"
"v_mfma_f32_16x16x32_bf16 v[28:31], v[196:199], a[240:243], v[28:31]\n"
"v_add_f32 v90, 1.0, v90\n"
"v_add_f32 v91, 1.0, v91\n"
"v_mfma_f32_16x16x32_bf16 v[32:35], v[192:195], a[228:231], v[32:35]\n"
"v_rcp_f32 v90, v90\n"
"v_mfma_f32_16x16x32_bf16 v[32:35], v[196:199], a[244:247], v[32:35]\n"
"v_rcp_f32 v91, v91\n"
"v_mfma_f32_16x16x32_bf16 v[36:39], v[192:195], a[232:235], v[36:39]\n"
"buffer_load_dwordx4 v[188:191], v1, s[36:39], s44 offen offset:1024 sc1\n"
"v_mfma_f32_16x16x32_bf16 v[36:39], v[196:199], a[248:251], v[36:39]\n"
"v_mul_f32 v90, v56, v90\n"
"v_mul_f32 v91, v57, v91\n"
"v_mfma_f32_16x16x32_bf16 v[40:43], v[192:195], a[236:239], v[40:43]\n"
"v_exp_f32 v92, v58\n"
"v_mfma_f32_16x16x32_bf16 v[40:43], v[196:199], a[252:255], v[40:43]\n"
"s_waitcnt vmcnt(24)\n"
"v_exp_f32 v93, v59\n"
"v_mfma_f32_16x16x32_bf16 v[28:31], v[200:203], v[224:227], v[28:31]\n"
"s_add_u32 s44, s95, 522240\n"
"buffer_load_dwordx4 v[192:195], v1, s[36:39], s44 offen offset:0 sc1\n"
"v_mfma_f32_16x16x32_bf16 v[44:47], v[204:207], v[240:243], v[28:31]\n"
"v_add_f32 v92, 1.0, v92\n"
"v_add_f32 v93, 1.0, v93\n"
"v_mfma_f32_16x16x32_bf16 v[32:35], v[200:203], v[228:231], v[32:35]\n"
"v_rcp_f32 v92, v92\n"
"v_mfma_f32_16x16x32_bf16 v[48:51], v[204:207], v[244:247], v[32:35]\n"
"v_rcp_f32 v93, v93\n"
"v_mfma_f32_16x16x32_bf16 v[36:39], v[200:203], v[232:235], v[36:39]\n"
"buffer_load_dwordx4 v[196:199], v1, s[36:39], s44 offen offset:1024 sc1\n"
"v_mfma_f32_16x16x32_bf16 v[52:55], v[204:207], v[248:251], v[36:39]\n"
"v_mul_f32 v92, v58, v92\n"
"v_mul_f32 v93, v59, v93\n"
"v_mfma_f32_16x16x32_bf16 v[40:43], v[200:203], v[236:239], v[40:43]\n"
"ds_write_b64 v16, v[64:65] offset:34816\n"
"v_cvt_pk_bf16_f32 v66, v90, v91\n"
"v_mfma_f32_16x16x32_bf16 v[56:59], v[204:207], v[252:255], v[40:43]\n"
"s_waitcnt vmcnt(24)\n"
"v_cvt_pk_bf16_f32 v67, v92, v93\n"
"ds_write_b64 v16, v[66:67] offset:35840\n"
"v_mfma_f32_16x16x32_bf16 v[28:31], v[208:211], a[0:3], 0\n"
"s_add_u32 s44, s95, 524288\n"
"buffer_load_dwordx4 v[200:203], v1, s[36:39], s44 offen offset:0 sc1\n"
"buffer_load_dwordx2 v[10:11], v9, s[36:39], 0 offen sc1\n"
"v_mfma_f32_16x16x32_bf16 v[28:31], v[212:215], a[16:19], v[28:31]\n"
"v_exp_f32 v86, v44\n"
"v_mfma_f32_16x16x32_bf16 v[32:35], v[208:211], a[4:7], 0\n"
"v_exp_f32 v87, v45\n"
"v_mfma_f32_16x16x32_bf16 v[32:35], v[212:215], a[20:23], v[32:35]\n"
"v_add_f32 v86, 1.0, v86\n"
"v_add_f32 v87, 1.0, v87\n"
"v_mfma_f32_16x16x32_bf16 v[36:39], v[208:211], a[8:11], 0\n"
"buffer_load_dwordx4 v[204:207], v1, s[36:39], s44 offen offset:1024 sc1\n"
"v_mfma_f32_16x16x32_bf16 v[36:39], v[212:215], a[24:27], v[36:39]\n"
"v_rcp_f32 v86, v86\n"
"v_mfma_f32_16x16x32_bf16 v[40:43], v[208:211], a[12:15], 0\n"
"v_rcp_f32 v87, v87\n"
"v_mfma_f32_16x16x32_bf16 v[40:43], v[212:215], a[28:31], v[40:43]\n"
"s_waitcnt vmcnt(25)\n"
"v_mul_f32 v86, v44, v86\n"
"v_mul_f32 v87, v45, v87\n"
"v_mfma_f32_16x16x32_bf16 v[28:31], v[216:219], a[32:35], v[28:31]\n"
"s_add_u32 s44, s95, 526336\n"
"buffer_load_dwordx4 v[208:211], v1, s[36:39], s44 offen offset:0 sc1\n"
"s_mul_i32 s13, s6, 3072\n"
"s_add_u32 m0, s13, 147456\n"
"s_lshr_b32 s13, s5, 1\n"
"buffer_load_dwordx4 v1, s[36:39], s21 offen sc1 lds\n"
"v_mfma_f32_16x16x32_bf16 v[28:31], v[220:223], a[48:51], v[28:31]\n"
"v_exp_f32 v88, v46\n"
"v_mfma_f32_16x16x32_bf16 v[32:35], v[216:219], a[36:39], v[32:35]\n"
"v_exp_f32 v89, v47\n"
"v_mfma_f32_16x16x32_bf16 v[32:35], v[220:223], a[52:55], v[32:35]\n"
"v_add_f32 v88, 1.0, v88\n"
"v_add_f32 v89, 1.0, v89\n"
"v_mfma_f32_16x16x32_bf16 v[36:39], v[216:219], a[40:43], v[36:39]\n"
"buffer_load_dwordx4 v[212:215], v1, s[36:39], s44 offen offset:1024 sc1\n"
"s_lshl_b32 s13, s13, 9\n"
"buffer_load_dwordx4 v1, s[36:39], s21 offen offset:1024 sc1 lds\n"
"v_mfma_f32_16x16x32_bf16 v[36:39], v[220:223], a[56:59], v[36:39]\n"
"v_rcp_f32 v88, v88\n"
"v_mfma_f32_16x16x32_bf16 v[40:43], v[216:219], a[44:47], v[40:43]\n"
"v_rcp_f32 v89, v89\n"
"v_mfma_f32_16x16x32_bf16 v[40:43], v[220:223], a[60:63], v[40:43]\n"
"s_waitcnt vmcnt(27)\n"
"v_mul_f32 v88, v46, v88\n"
"v_mul_f32 v89, v47, v89\n"
"v_mfma_f32_16x16x32_bf16 v[28:31], v[112:115], a[64:67], v[28:31]\n"
"s_add_u32 s44, s95, 528384\n"
"buffer_load_dwordx4 v[216:219], v1, s[36:39], s44 offen offset:0 sc1\n"
"s_addk_i32 m0, 2048\n"
"s_add_u32 s13, s13, s69\n"
"buffer_load_dwordx4 v1, s[36:39], s13 offen sc1 lds\n"
"v_mfma_f32_16x16x32_bf16 v[28:31], v[116:119], a[80:83], v[28:31]\n"
"v_cvt_pk_bf16_f32 v60, v86, v87\n"
"v_cvt_pk_bf16_f32 v61, v88, v89\n"
"v_mfma_f32_16x16x32_bf16 v[32:35], v[112:115], a[68:71], v[32:35]\n"
"v_exp_f32 v90, v48\n"
"v_mfma_f32_16x16x32_bf16 v[32:35], v[116:119], a[84:87], v[32:35]\n"
"v_exp_f32 v91, v49\n"
"v_mfma_f32_16x16x32_bf16 v[36:39], v[112:115], a[72:75], v[36:39]\n"
"buffer_load_dwordx4 v[220:223], v1, s[36:39], s44 offen offset:1024 sc1\n"
"v_mfma_f32_16x16x32_bf16 v[36:39], v[116:119], a[88:91], v[36:39]\n"
"v_add_f32 v90, 1.0, v90\n"
"v_add_f32 v91, 1.0, v91\n"
"v_mfma_f32_16x16x32_bf16 v[40:43], v[112:115], a[76:79], v[40:43]\n"
"v_rcp_f32 v90, v90\n"
"v_mfma_f32_16x16x32_bf16 v[40:43], v[116:119], a[92:95], v[40:43]\n"
"s_waitcnt vmcnt(28)\n"
"v_rcp_f32 v91, v91\n"
"v_mfma_f32_16x16x32_bf16 v[28:31], v[120:123], a[96:99], v[28:31]\n"
"s_add_u32 s44, s95, 530432\n"
"buffer_load_dwordx4 v[112:115], v1, s[36:39], s44 offen offset:0 sc1\n"
"v_mfma_f32_16x16x32_bf16 v[28:31], v[124:127], a[112:115], v[28:31]\n"
"v_mul_f32 v90, v48, v90\n"
"v_mul_f32 v91, v49, v91\n"
"v_mfma_f32_16x16x32_bf16 v[32:35], v[120:123], a[100:103], v[32:35]\n"
"v_exp_f32 v92, v50\n"
"v_mfma_f32_16x16x32_bf16 v[32:35], v[124:127], a[116:119], v[32:35]\n"
"v_exp_f32 v93, v51\n"
"v_mfma_f32_16x16x32_bf16 v[36:39], v[120:123], a[104:107], v[36:39]\n"
"buffer_load_dwordx4 v[116:119], v1, s[36:39], s44 offen offset:1024 sc1\n"
"v_mfma_f32_16x16x32_bf16 v[36:39], v[124:127], a[120:123], v[36:39]\n"
"v_add_f32 v92, 1.0, v92\n"
"v_add_f32 v93, 1.0, v93\n"
"v_mfma_f32_16x16x32_bf16 v[40:43], v[120:123], a[108:111], v[40:43]\n"
"v_rcp_f32 v92, v92\n"
"v_mfma_f32_16x16x32_bf16 v[40:43], v[124:127], a[124:127], v[40:43]\n"
"s_waitcnt vmcnt(28)\n"
"v_rcp_f32 v93, v93\n"
"v_mfma_f32_16x16x32_bf16 v[28:31], v[128:131], a[128:131], v[28:31]\n"
"s_add_u32 s44, s95, 532480\n"
"buffer_load_dwordx4 v[120:123], v1, s[36:39], s44 offen offset:0 sc1\n"
"v_mfma_f32_16x16x32_bf16 v[28:31], v[132:135], a[144:147], v[28:31]\n"
"v_mul_f32 v92, v50, v92\n"
"v_mul_f32 v93, v51, v93\n"
"v_mfma_f32_16x16x32_bf16 v[32:35], v[128:131], a[132:135], v[32:35]\n"
"ds_write_b64 v16, v[60:61] offset:40960\n"
"v_cvt_pk_bf16_f32 v62, v90, v91\n"
"v_mfma_f32_16x16x32_bf16 v[32:35], v[132:135], a[148:151], v[32:35]\n"
"v_cvt_pk_bf16_f32 v63, v92, v93\n"
"ds_write_b64 v16, v[62:63] offset:41984\n"
"v_mfma_f32_16x16x32_bf16 v[36:39], v[128:131], a[136:139], v[36:39]\n"
"buffer_load_dwordx4 v[124:127], v1, s[36:39], s44 offen offset:1024 sc1\n"
"v_mfma_f32_16x16x32_bf16 v[36:39], v[132:135], a[152:155], v[36:39]\n"
"v_exp_f32 v86, v52\n"
"v_mfma_f32_16x16x32_bf16 v[40:43], v[128:131], a[140:143], v[40:43]\n"
"v_exp_f32 v87, v53\n"
"v_mfma_f32_16x16x32_bf16 v[40:43], v[132:135], a[156:159], v[40:43]\n"
"s_waitcnt vmcnt(28)\n"
"v_add_f32 v86, 1.0, v86\n"
"v_add_f32 v87, 1.0, v87\n"
"v_mfma_f32_16x16x32_bf16 v[28:31], v[136:139], a[160:163], v[28:31]\n"
"s_add_u32 s44, s95, 589824\n"
"buffer_load_dwordx4 v[128:131], v1, s[36:39], s44 offen offset:0 sc1\n"
"v_mfma_f32_16x16x32_bf16 v[28:31], v[140:143], a[176:179], v[28:31]\n"
"v_rcp_f32 v86, v86\n"
"v_mfma_f32_16x16x32_bf16 v[32:35], v[136:139], a[164:167], v[32:35]\n"
"v_rcp_f32 v87, v87\n"
"v_mfma_f32_16x16x32_bf16 v[32:35], v[140:143], a[180:183], v[32:35]\n"
"v_mul_f32 v86, v52, v86\n"
"v_mul_f32 v87, v53, v87\n"
"v_mfma_f32_16x16x32_bf16 v[36:39], v[136:139], a[168:171], v[36:39]\n"
"buffer_load_dwordx4 v[132:135], v1, s[36:39], s44 offen offset:1024 sc1\n"
"v_mfma_f32_16x16x32_bf16 v[36:39], v[140:143], a[184:187], v[36:39]\n"
"v_exp_f32 v88, v54\n"
"v_mfma_f32_16x16x32_bf16 v[40:43], v[136:139], a[172:175], v[40:43]\n"
"v_exp_f32 v89, v55\n"
"v_mfma_f32_16x16x32_bf16 v[40:43], v[140:143], a[188:191], v[40:43]\n"
"s_waitcnt vmcnt(28)\n"
"v_add_f32 v88, 1.0, v88\n"
"v_add_f32 v89, 1.0, v89\n"
"v_mfma_f32_16x16x32_bf16 v[28:31], v[144:147], a[192:195], v[28:31]\n"
"s_add_u32 s44, s95, 591872\n"
"buffer_load_dwordx4 v[136:139], v1, s[36:39], s44 offen offset:0 sc1\n"
"v_mfma_f32_16x16x32_bf16 v[28:31], v[148:151], a[208:211], v[28:31]\n"
"v_rcp_f32 v88, v88\n"
"v_mfma_f32_16x16x32_bf16 v[32:35], v[144:147], a[196:199], v[32:35]\n"
"v_rcp_f32 v89, v89\n"
"v_mfma_f32_16x16x32_bf16 v[32:35], v[148:151], a[212:215], v[32:35]\n"
"v_mul_f32 v88, v54, v88\n"
"v_mul_f32 v89, v55, v89\n"
"v_mfma_f32_16x16x32_bf16 v[36:39], v[144:147], a[200:203], v[36:39]\n"
"buffer_load_dwordx4 v[140:143], v1, s[36:39], s44 offen offset:1024 sc1\n"
"v_mfma_f32_16x16x32_bf16 v[36:39], v[148:151], a[216:219], v[36:39]\n"
"v_cvt_pk_bf16_f32 v64, v86, v87\n"
"v_cvt_pk_bf16_f32 v65, v88, v89\n"
"v_mfma_f32_16x16x32_bf16 v[40:43], v[144:147], a[204:207], v[40:43]\n"
"v_exp_f32 v90, v56\n"
"v_mfma_f32_16x16x32_bf16 v[40:43], v[148:151], a[220:223], v[40:43]\n"
"s_waitcnt vmcnt(28)\n"
"v_exp_f32 v91, v57\n"
"v_mfma_f32_16x16x32_bf16 v[28:31], v[152:155], a[224:227], v[28:31]\n"
"s_add_u32 s44, s95, 593920\n"
"buffer_load_dwordx4 v[144:147], v1, s[36:39], s44 offen offset:0 sc1\n"
"v_mfma_f32_16x16x32_bf16 v[28:31], v[156:159], a[240:243], v[28:31]\n"
"v_add_f32 v90, 1.0, v90\n"
"v_add_f32 v91, 1.0, v91\n"
"v_mfma_f32_16x16x32_bf16 v[32:35], v[152:155], a[228:231], v[32:35]\n"
"v_rcp_f32 v90, v90\n"
"v_mfma_f32_16x16x32_bf16 v[32:35], v[156:159], a[244:247], v[32:35]\n"
"v_rcp_f32 v91, v91\n"
"v_mfma_f32_16x16x32_bf16 v[36:39], v[152:155], a[232:235], v[36:39]\n"
"buffer_load_dwordx4 v[148:151], v1, s[36:39], s44 offen offset:1024 sc1\n"
"v_mfma_f32_16x16x32_bf16 v[36:39], v[156:159], a[248:251], v[36:39]\n"
"v_mul_f32 v90, v56, v90\n"
"v_mul_f32 v91, v57, v91\n"
"v_mfma_f32_16x16x32_bf16 v[40:43], v[152:155], a[236:239], v[40:43]\n"
"v_exp_f32 v92, v58\n"
"v_mfma_f32_16x16x32_bf16 v[40:43], v[156:159], a[252:255], v[40:43]\n"
"s_waitcnt vmcnt(28)\n"
"v_exp_f32 v93, v59\n"
"v_mfma_f32_16x16x32_bf16 v[28:31], v[160:163], v[224:227], v[28:31]\n"
"s_add_u32 s44, s95, 595968\n"
"buffer_load_dwordx4 v[152:155], v1, s[36:39], s44 offen offset:0 sc1\n"
"v_mfma_f32_16x16x32_bf16 v[44:47], v[164:167], v[240:243], v[28:31]\n"
"v_add_f32 v92, 1.0, v92\n"
"v_add_f32 v93, 1.0, v93\n"
"v_mfma_f32_16x16x32_bf16 v[32:35], v[160:163], v[228:231], v[32:35]\n"
"v_rcp_f32 v92, v92\n"
"v_mfma_f32_16x16x32_bf16 v[48:51], v[164:167], v[244:247], v[32:35]\n"
"v_rcp_f32 v93, v93\n"
"v_mfma_f32_16x16x32_bf16 v[36:39], v[160:163], v[232:235], v[36:39]\n"
"buffer_load_dwordx4 v[156:159], v1, s[36:39], s44 offen offset:1024 sc1\n"
"v_mfma_f32_16x16x32_bf16 v[52:55], v[164:167], v[248:251], v[36:39]\n"
"v_mul_f32 v92, v58, v92\n"
"v_mul_f32 v93, v59, v93\n"
"v_mfma_f32_16x16x32_bf16 v[40:43], v[160:163], v[236:239], v[40:43]\n"
"ds_write_b64 v16, v[64:65] offset:43008\n"
"v_cvt_pk_bf16_f32 v66, v90, v91\n"
"v_mfma_f32_16x16x32_bf16 v[56:59], v[164:167], v[252:255], v[40:43]\n"
"s_waitcnt vmcnt(28)\n"
"v_cvt_pk_bf16_f32 v67, v92, v93\n"
"ds_write_b64 v16, v[66:67] offset:44032\n"
"v_mfma_f32_16x16x32_bf16 v[28:31], v[168:171], a[0:3], 0\n"
"s_add_u32 s44, s95, 598016\n"
"buffer_load_dwordx4 v[160:163], v1, s[36:39], s44 offen offset:0 sc1\n"
"v_mfma_f32_16x16x32_bf16 v[28:31], v[172:175], a[16:19], v[28:31]\n"
"v_exp_f32 v86, v44\n"
"v_mfma_f32_16x16x32_bf16 v[32:35], v[168:171], a[4:7], 0\n"
"v_exp_f32 v87, v45\n"
"v_mfma_f32_16x16x32_bf16 v[32:35], v[172:175], a[20:23], v[32:35]\n"
"v_add_f32 v86, 1.0, v86\n"
"v_add_f32 v87, 1.0, v87\n"
"v_mfma_f32_16x16x32_bf16 v[36:39], v[168:171], a[8:11], 0\n"
"buffer_load_dwordx4 v[164:167], v1, s[36:39], s44 offen offset:1024 sc1\n"
"v_mfma_f32_16x16x32_bf16 v[36:39], v[172:175], a[24:27], v[36:39]\n"
"v_rcp_f32 v86, v86\n"
"v_mfma_f32_16x16x32_bf16 v[40:43], v[168:171], a[12:15], 0\n"
"v_rcp_f32 v87, v87\n"
"v_mfma_f32_16x16x32_bf16 v[40:43], v[172:175], a[28:31], v[40:43]\n"
"s_waitcnt vmcnt(28)\n"
"v_mul_f32 v86, v44, v86\n"
"v_mul_f32 v87, v45, v87\n"
"v_mfma_f32_16x16x32_bf16 v[28:31], v[176:179], a[32:35], v[28:31]\n"
"s_add_u32 s44, s95, 600064\n"
"buffer_load_dwordx4 v[168:171], v1, s[36:39], s44 offen offset:0 sc1\n"
"v_mfma_f32_16x16x32_bf16 v[28:31], v[180:183], a[48:51], v[28:31]\n"
"v_exp_f32 v88, v46\n"
"v_mfma_f32_16x16x32_bf16 v[32:35], v[176:179], a[36:39], v[32:35]\n"
"v_exp_f32 v89, v47\n"
"v_mfma_f32_16x16x32_bf16 v[32:35], v[180:183], a[52:55], v[32:35]\n"
"v_add_f32 v88, 1.0, v88\n"
"v_add_f32 v89, 1.0, v89\n"
"v_mfma_f32_16x16x32_bf16 v[36:39], v[176:179], a[40:43], v[36:39]\n"
"buffer_load_dwordx4 v[172:175], v1, s[36:39], s44 offen offset:1024 sc1\n"
"v_mfma_f32_16x16x32_bf16 v[36:39], v[180:183], a[56:59], v[36:39]\n"
"v_rcp_f32 v88, v88\n"
"v_mfma_f32_16x16x32_bf16 v[40:43], v[176:179], a[44:47], v[40:43]\n"
"v_rcp_f32 v89, v89\n"
"v_mfma_f32_16x16x32_bf16 v[40:43], v[180:183], a[60:63], v[40:43]\n"
"s_waitcnt vmcnt(28)\n"
"v_mul_f32 v88, v46, v88\n"
"v_mul_f32 v89, v47, v89\n"
"v_mfma_f32_16x16x32_bf16 v[28:31], v[184:187], a[64:67], v[28:31]\n"
"s_add_u32 s44, s95, 602112\n"
"buffer_load_dwordx4 v[176:179], v1, s[36:39], s44 offen offset:0 sc1\n"
"v_mfma_f32_16x16x32_bf16 v[28:31], v[188:191], a[80:83], v[28:31]\n"
"v_cvt_pk_bf16_f32 v60, v86, v87\n"
"v_cvt_pk_bf16_f32 v61, v88, v89\n"
"v_mfma_f32_16x16x32_bf16 v[32:35], v[184:187], a[68:71], v[32:35]\n"
"v_exp_f32 v90, v48\n"
"v_mfma_f32_16x16x32_bf16 v[32:35], v[188:191], a[84:87], v[32:35]\n"
"v_exp_f32 v91, v49\n"
"v_mfma_f32_16x16x32_bf16 v[36:39], v[184:187], a[72:75], v[36:39]\n"
"buffer_load_dwordx4 v[180:183], v1, s[36:39], s44 offen offset:1024 sc1\n"
"v_mfma_f32_16x16x32_bf16 v[36:39], v[188:191], a[88:91], v[36:39]\n"
"v_add_f32 v90, 1.0, v90\n"
"v_add_f32 v91, 1.0, v91\n"
"v_mfma_f32_16x16x32_bf16 v[40:43], v[184:187], a[76:79], v[40:43]\n"
"v_rcp_f32 v90, v90\n"
"v_mfma_f32_16x16x32_bf16 v[40:43], v[188:191], a[92:95], v[40:43]\n"
"s_waitcnt vmcnt(28)\n"
"v_rcp_f32 v91, v91\n"
"v_mfma_f32_16x16x32_bf16 v[28:31], v[192:195], a[96:99], v[28:31]\n"
"s_add_u32 s44, s95, 604160\n"
"buffer_load_dwordx4 v[184:187], v1, s[36:39], s44 offen offset:0 sc1\n"
"v_mfma_f32_16x16x32_bf16 v[28:31], v[196:199], a[112:115], v[28:31]\n"
"v_mul_f32 v90, v48, v90\n"
"v_mul_f32 v91, v49, v91\n"
"v_mfma_f32_16x16x32_bf16 v[32:35], v[192:195], a[100:103], v[32:35]\n"
"v_exp_f32 v92, v50\n"
"v_mfma_f32_16x16x32_bf16 v[32:35], v[196:199], a[116:119], v[32:35]\n"
"v_exp_f32 v93, v51\n"
"v_mfma_f32_16x16x32_bf16 v[36:39], v[192:195], a[104:107], v[36:39]\n"
"buffer_load_dwordx4 v[188:191], v1, s[36:39], s44 offen offset:1024 sc1\n"
"v_mfma_f32_16x16x32_bf16 v[36:39], v[196:199], a[120:123], v[36:39]\n"
"v_add_f32 v92, 1.0, v92\n"
"v_add_f32 v93, 1.0, v93\n"
"v_mfma_f32_16x16x32_bf16 v[40:43], v[192:195], a[108:111], v[40:43]\n"
"v_rcp_f32 v92, v92\n"
"v_mfma_f32_16x16x32_bf16 v[40:43], v[196:199], a[124:127], v[40:43]\n"
"s_waitcnt vmcnt(27)\n"
"v_rcp_f32 v93, v93\n"
"v_mfma_f32_16x16x32_bf16 v[28:31], v[200:203], a[128:131], v[28:31]\n"
"s_add_u32 s44, s95, 606208\n"
"buffer_load_dwordx4 v[192:195], v1, s[36:39], s44 offen offset:0 sc1\n"
"s_waitcnt vmcnt(29)\n"
"v_cmp_ne_u64 vcc, v[10:11], v[6:7]\n"
"s_nop 0\n"
"s_cbranch_vccnz LFC_block_kernel_slow\n"
"LFC_block_kernel_back:\n"
"s_waitcnt vmcnt(22) lgkmcnt(0)\n"
"s_barrier\n"
"s_or_b32 s13, %2, s6\n"
"s_cbranch_scc1 LEP_block_kernel\n"
"s_mov_b32 exec_lo, 0xffff\n"
"s_mov_b32 exec_hi, 0\n"
"buffer_store_dwordx2 v[6:7], v2, s[36:39], 0 offen sc1\n"
"s_mov_b64 exec, -1\n"
"LEP_block_kernel:\n"
"v_mfma_f32_16x16x32_bf16 v[28:31], v[204:207], a[144:147], v[28:31]\n"
"v_mul_f32 v92, v50, v92\n"
"v_mul_f32 v93, v51, v93\n"
"v_mfma_f32_16x16x32_bf16 v[32:35], v[200:203], a[132:135], v[32:35]\n"
"ds_write_b64 v16, v[60:61] offset:49152\n"
"v_cvt_pk_bf16_f32 v62, v90, v91\n"
"v_mfma_f32_16x16x32_bf16 v[32:35], v[204:207], a[148:151], v[32:35]\n"
"v_cvt_pk_bf16_f32 v63, v92, v93\n"
"ds_write_b64 v16, v[62:63] offset:50176\n"
"v_mfma_f32_16x16x32_bf16 v[36:39], v[200:203], a[136:139], v[36:39]\n"
"buffer_load_dwordx4 v[196:199], v1, s[36:39], s44 offen offset:1024 sc1\n"
"v_mfma_f32_16x16x32_bf16 v[36:39], v[204:207], a[152:155], v[36:39]\n"
"v_exp_f32 v86, v52\n"
"v_mfma_f32_16x16x32_bf16 v[40:43], v[200:203], a[140:143], v[40:43]\n"
"v_exp_f32 v87, v53\n"
"v_mfma_f32_16x16x32_bf16 v[40:43], v[204:207], a[156:159], v[40:43]\n"
"s_waitcnt vmcnt(26)\n"
"v_add_f32 v86, 1.0, v86\n"
"v_add_f32 v87, 1.0, v87\n"
"v_mfma_f32_16x16x32_bf16 v[28:31], v[208:211], a[160:163], v[28:31]\n"
"s_add_u32 s44, s83, 0\n"
"buffer_load_dwordx4 v[200:203], v1, s[36:39], s44 offen offset:0 sc1\n"
"v_mfma_f32_16x16x32_bf16 v[28:31], v[212:215], a[176:179], v[28:31]\n"
"v_rcp_f32 v86, v86\n"
"v_mfma_f32_16x16x32_bf16 v[32:35], v[208:211], a[164:167], v[32:35]\n"
"v_rcp_f32 v87, v87\n"
"v_mfma_f32_16x16x32_bf16 v[32:35], v[212:215], a[180:183], v[32:35]\n"
"v_mul_f32 v86, v52, v86\n"
"v_mul_f32 v87, v53, v87\n"
"v_mfma_f32_16x16x32_bf16 v[36:39], v[208:211], a[168:171], v[36:39]\n"
"buffer_load_dwordx4 v[204:207], v1, s[36:39], s44 offen offset:1024 sc1\n"
"v_mfma_f32_16x16x32_bf16 v[36:39], v[212:215], a[184:187], v[36:39]\n"
"v_exp_f32 v88, v54\n"
"v_mfma_f32_16x16x32_bf16 v[40:43], v[208:211], a[172:175], v[40:43]\n"
"v_exp_f32 v89, v55\n"
"v_mfma_f32_16x16x32_bf16 v[40:43], v[212:215], a[188:191], v[40:43]\n"
"s_waitcnt vmcnt(24)\n"
"v_add_f32 v88, 1.0, v88\n"
"v_add_f32 v89, 1.0, v89\n"
"v_mfma_f32_16x16x32_bf16 v[28:31], v[216:219], a[192:195], v[28:31]\n"
"s_add_u32 s44, s83, 2048\n"
"buffer_load_dwordx4 v[208:211], v1, s[36:39], s44 offen offset:0 sc1\n"
"v_mfma_f32_16x16x32_bf16 v[28:31], v[220:223], a[208:211], v[28:31]\n"
"v_rcp_f32 v88, v88\n"
"v_mfma_f32_16x16x32_bf16 v[32:35], v[216:219], a[196:199], v[32:35]\n"
"v_rcp_f32 v89, v89\n"
"v_mfma_f32_16x16x32_bf16 v[32:35], v[220:223], a[212:215], v[32:35]\n"
"v_mul_f32 v88, v54, v88\n"
"v_mul_f32 v89, v55, v89\n"
"v_mfma_f32_16x16x32_bf16 v[36:39], v[216:219], a[200:203], v[36:39]\n"
"buffer_load_dwordx4 v[212:215], v1, s[36:39], s44 offen offset:1024 sc1\n"
"v_mfma_f32_16x16x32_bf16 v[36:39], v[220:223], a[216:219], v[36:39]\n"
"v_cvt_pk_bf16_f32 v64, v86, v87\n"
"v_cvt_pk_bf16_f32 v65, v88, v89\n"
"v_mfma_f32_16x16x32_bf16 v[40:43], v[216:219], a[204:207], v[40:43]\n"
"v_exp_f32 v90, v56\n"
"v_mfma_f32_16x16x32_bf16 v[40:43], v[220:223], a[220:223], v[40:43]\n"
"s_waitcnt vmcnt(24)\n"
"v_exp_f32 v91, v57\n"
"v_mfma_f32_16x16x32_bf16 v[28:31], v[112:115], a[224:227], v[28:31]\n"
"s_add_u32 s44, s83, 4096\n"
"buffer_load_dwordx4 v[216:219], v1, s[36:39], s44 offen offset:0 sc1\n"
"v_mfma_f32_16x16x32_bf16 v[28:31], v[116:119], a[240:243], v[28:31]\n"
"v_add_f32 v90, 1.0, v90\n"
"v_add_f32 v91, 1.0, v91\n"
"v_mfma_f32_16x16x32_bf16 v[32:35], v[112:115], a[228:231], v[32:35]\n"
"v_rcp_f32 v90, v90\n"
"v_mfma_f32_16x16x32_bf16 v[32:35], v[116:119], a[244:247], v[32:35]\n"
"v_rcp_f32 v91, v91\n"
"v_mfma_f32_16x16x32_bf16 v[36:39], v[112:115], a[232:235], v[36:39]\n"
"buffer_load_dwordx4 v[220:223], v1, s[36:39], s44 offen offset:1024 sc1\n"
"v_mfma_f32_16x16x32_bf16 v[36:39], v[116:119], a[248:251], v[36:39]\n"
"v_mul_f32 v90, v56, v90\n"
"v_mul_f32 v91, v57, v91\n"
"v_mfma_f32_16x16x32_bf16 v[40:43], v[112:115], a[236:239], v[40:43]\n"
"v_exp_f32 v92, v58\n"
"v_mfma_f32_16x16x32_bf16 v[40:43], v[116:119], a[252:255], v[40:43]\n"
"s_waitcnt vmcnt(24)\n"
"v_exp_f32 v93, v59\n"
"v_mfma_f32_16x16x32_bf16 v[28:31], v[120:123], v[224:227], v[28:31]\n"
"s_add_u32 s44, s83, 6144\n"
"buffer_load_dwordx4 v[112:115], v1, s[36:39], s44 offen offset:0 sc1\n"
"v_mfma_f32_16x16x32_bf16 v[44:47], v[124:127], v[240:243], v[28:31]\n"
"v_add_f32 v92, 1.0, v92\n"
"v_add_f32 v93, 1.0, v93\n"
"v_mfma_f32_16x16x32_bf16 v[32:35], v[120:123], v[228:231], v[32:35]\n"
"v_rcp_f32 v92, v92\n"
"v_mfma_f32_16x16x32_bf16 v[48:51], v[124:127], v[244:247], v[32:35]\n"
"v_rcp_f32 v93, v93\n"
"v_mfma_f32_16x16x32_bf16 v[36:39], v[120:123], v[232:235], v[36:39]\n"
"buffer_load_dwordx4 v[116:119], v1, s[36:39], s44 offen offset:1024 sc1\n"
"v_mfma_f32_16x16x32_bf16 v[52:55], v[124:127], v[248:251], v[36:39]\n"
"v_mul_f32 v92, v58, v92\n"
"v_mul_f32 v93, v59, v93\n"
"v_mfma_f32_16x16x32_bf16 v[40:43], v[120:123], v[236:239], v[40:43]\n"
"ds_write_b64 v16, v[64:65] offset:51200\n"
"v_cvt_pk_bf16_f32 v66, v90, v91\n"
"v_mfma_f32_16x16x32_bf16 v[56:59], v[124:127], v[252:255], v[40:43]\n"
"s_waitcnt vmcnt(24)\n"
"v_cvt_pk_bf16_f32 v67, v92, v93\n"
"ds_write_b64 v16, v[66:67] offset:52224\n"
"v_mfma_f32_16x16x32_bf16 v[28:31], v[128:131], a[0:3], 0\n"
"s_add_u32 s44, s83, 8192\n"
"buffer_load_dwordx4 v[120:123], v1, s[36:39], s44 offen offset:0 sc1\n"
"v_mfma_f32_16x16x32_bf16 v[28:31], v[132:135], a[16:19], v[28:31]\n"
"v_exp_f32 v86, v44\n"
"v_mfma_f32_16x16x32_bf16 v[32:35], v[128:131], a[4:7], 0\n"
"v_exp_f32 v87, v45\n"
"v_mfma_f32_16x16x32_bf16 v[32:35], v[132:135], a[20:23], v[32:35]\n"
"v_add_f32 v86, 1.0, v86\n"
"v_add_f32 v87, 1.0, v87\n"
"v_mfma_f32_16x16x32_bf16 v[36:39], v[128:131], a[8:11], 0\n"
"buffer_load_dwordx4 v[124:127], v1, s[36:39], s44 offen offset:1024 sc1\n"
"v_mfma_f32_16x16x32_bf16 v[36:39], v[132:135], a[24:27], v[36:39]\n"
"v_rcp_f32 v86, v86\n"
"v_mfma_f32_16x16x32_bf16 v[40:43], v[128:131], a[12:15], 0\n"
"v_rcp_f32 v87, v87\n"
"v_mfma_f32_16x16x32_bf16 v[40:43], v[132:135], a[28:31], v[40:43]\n"
"s_waitcnt vmcnt(24)\n"
"v_mul_f32 v86, v44, v86\n"
"v_mul_f32 v87, v45, v87\n"
"v_mfma_f32_16x16x32_bf16 v[28:31], v[136:139], a[32:35], v[28:31]\n"
"s_add_u32 s44, s83, 10240\n"
"buffer_load_dwordx4 v[128:131], v1, s[36:39], s44 offen offset:0 sc1\n"
"v_mfma_f32_16x16x32_bf16 v[28:31], v[140:143], a[48:51], v[28:31]\n"
"v_exp_f32 v88, v46\n"
"v_mfma_f32_16x16x32_bf16 v[32:35], v[136:139], a[36:39], v[32:35]\n"
"v_exp_f32 v89, v47\n"
"v_mfma_f32_16x16x32_bf16 v[32:35], v[140:143], a[52:55], v[32:35]\n"
"v_add_f32 v88, 1.0, v88\n"
"v_add_f32 v89, 1.0, v89\n"
"v_mfma_f32_16x16x32_bf16 v[36:39], v[136:139], a[40:43], v[36:39]\n"
"buffer_load_dwordx4 v[132:135], v1, s[36:39], s44 offen offset:1024 sc1\n"
"v_mfma_f32_16x16x32_bf16 v[36:39], v[140:143], a[56:59], v[36:39]\n"
"v_rcp_f32 v88, v88\n"
"v_mfma_f32_16x16x32_bf16 v[40:43], v[136:139], a[44:47], v[40:43]\n"
"v_rcp_f32 v89, v89\n"
"v_mfma_f32_16x16x32_bf16 v[40:43], v[140:143], a[60:63], v[40:43]\n"
"s_waitcnt vmcnt(24)\n"
"v_mul_f32 v88, v46, v88\n"
"v_mul_f32 v89, v47, v89\n"
"v_mfma_f32_16x16x32_bf16 v[28:31], v[144:147], a[64:67], v[28:31]\n"
"s_add_u32 s44, s83, 12288\n"
"buffer_load_dwordx4 v[136:139], v1, s[36:39], s44 offen offset:0 sc1\n"
"v_mfma_f32_16x16x32_bf16 v[28:31], v[148:151], a[80:83], v[28:31]\n"
"v_cvt_pk_bf16_f32 v60, v86, v87\n"
"v_cvt_pk_bf16_f32 v61, v88, v89\n"
"v_mfma_f32_16x16x32_bf16 v[32:35], v[144:147], a[68:71], v[32:35]\n"
"v_exp_f32 v90, v48\n"
"v_mfma_f32_16x16x32_bf16 v[32:35], v[148:151], a[84:87], v[32:35]\n"
"v_exp_f32 v91, v49\n"
"v_mfma_f32_16x16x32_bf16 v[36:39], v[144:147], a[72:75], v[36:39]\n"
"buffer_load_dwordx4 v[140:143], v1, s[36:39], s44 offen offset:1024 sc1\n"
"v_mfma_f32_16x16x32_bf16 v[36:39], v[148:151], a[88:91], v[36:39]\n"
"v_add_f32 v90, 1.0, v90\n"
"v_add_f32 v91, 1.0, v91\n"
"v_mfma_f32_16x16x32_bf16 v[40:43], v[144:147], a[76:79], v[40:43]\n"
"v_rcp_f32 v90, v90\n"
"v_mfma_f32_16x16x32_bf16 v[40:43], v[148:151], a[92:95], v[40:43]\n"
"s_waitcnt vmcnt(24)\n"
"v_rcp_f32 v91, v91\n"
"v_mfma_f32_16x16x32_bf16 v[28:31], v[152:155], a[96:99], v[28:31]\n"
"s_add_u32 s44, s83, 14336\n"
"buffer_load_dwordx4 v[144:147], v1, s[36:39], s44 offen offset:0 sc1\n"
"v_mfma_f32_16x16x32_bf16 v[28:31], v[156:159], a[112:115], v[28:31]\n"
"v_mul_f32 v90, v48, v90\n"
"v_mul_f32 v91, v49, v91\n"
"v_mfma_f32_16x16x32_bf16 v[32:35], v[152:155], a[100:103], v[32:35]\n"
"v_exp_f32 v92, v50\n"
"v_mfma_f32_16x16x32_bf16 v[32:35], v[156:159], a[116:119], v[32:35]\n"
"v_exp_f32 v93, v51\n"
"v_mfma_f32_16x16x32_bf16 v[36:39], v[152:155], a[104:107], v[36:39]\n"
"buffer_load_dwordx4 v[148:151], v1, s[36:39], s44 offen offset:1024 sc1\n"
"v_mfma_f32_16x16x32_bf16 v[36:39], v[156:159], a[120:123], v[36:39]\n"
"v_add_f32 v92, 1.0, v92\n"
"v_add_f32 v93, 1.0, v93\n"
"v_mfma_f32_16x16x32_bf16 v[40:43], v[152:155], a[108:111], v[40:43]\n"
"v_rcp_f32 v92, v92\n"
"v_mfma_f32_16x16x32_bf16 v[40:43], v[156:159], a[124:127], v[40:43]\n"
"s_waitcnt vmcnt(24)\n"
"v_rcp_f32 v93, v93\n"
"v_mfma_f32_16x16x32_bf16 v[28:31], v[160:163], a[128:131], v[28:31]\n"
"s_add_u32 s44, s83, 16384\n"
"buffer_load_dwordx4 v[152:155], v1, s[36:39], s44 offen offset:0 sc1\n"
"v_mfma_f32_16x16x32_bf16 v[28:31], v[164:167], a[144:147], v[28:31]\n"
"v_mul_f32 v92, v50, v92\n"
"v_mul_f32 v93, v51, v93\n"
"v_mfma_f32_16x16x32_bf16 v[32:35], v[160:163], a[132:135], v[32:35]\n"
"ds_write_b64 v16, v[60:61] offset:57344\n"
"v_cvt_pk_bf16_f32 v62, v90, v91\n"
"v_mfma_f32_16x16x32_bf16 v[32:35], v[164:167], a[148:151], v[32:35]\n"
"v_cvt_pk_bf16_f32 v63, v92, v93\n"
"ds_write_b64 v16, v[62:63] offset:58368\n"
"v_mfma_f32_16x16x32_bf16 v[36:39], v[160:163], a[136:139], v[36:39]\n"
"buffer_load_dwordx4 v[156:159], v1, s[36:39], s44 offen offset:1024 sc1\n"
"v_mfma_f32_16x16x32_bf16 v[36:39], v[164:167], a[152:155], v[36:39]\n"
"v_exp_f32 v86, v52\n"
"v_mfma_f32_16x16x32_bf16 v[40:43], v[160:163], a[140:143], v[40:43]\n"
"v_exp_f32 v87, v53\n"
"v_mfma_f32_16x16x32_bf16 v[40:43], v[164:167], a[156:159], v[40:43]\n"
"s_waitcnt vmcnt(24)\n"
"v_add_f32 v86, 1.0, v86\n"
"v_add_f32 v87, 1.0, v87\n"
"v_mfma_f32_16x16x32_bf16 v[28:31], v[168:171], a[160:163], v[28:31]\n"
"s_add_u32 s44, s84, 0\n"
"buffer_load_dwordx4 v[160:163], v1, s[36:39], s44 offen offset:0 sc1\n"
"v_mfma_f32_16x16x32_bf16 v[28:31], v[172:175], a[176:179], v[28:31]\n"
"v_rcp_f32 v86, v86\n"
"v_mfma_f32_16x16x32_bf16 v[32:35], v[168:171], a[164:167], v[32:35]\n"
"v_rcp_f32 v87, v87\n"
"v_mfma_f32_16x16x32_bf16 v[32:35], v[172:175], a[180:183], v[32:35]\n"
"v_mul_f32 v86, v52, v86\n"
"v_mul_f32 v87, v53, v87\n"
"v_mfma_f32_16x16x32_bf16 v[36:39], v[168:171], a[168:171], v[36:39]\n"
"buffer_load_dwordx4 v[164:167], v1, s[36:39], s44 offen offset:1024 sc1\n"
"v_mfma_f32_16x16x32_bf16 v[36:39], v[172:175], a[184:187], v[36:39]\n"
"v_exp_f32 v88, v54\n"
"v_mfma_f32_16x16x32_bf16 v[40:43], v[168:171], a[172:175], v[40:43]\n"
"v_exp_f32 v89, v55\n"
"v_mfma_f32_16x16x32_bf16 v[40:43], v[172:175], a[188:191], v[40:43]\n"
"s_waitcnt vmcnt(24)\n"
"v_add_f32 v88, 1.0, v88\n"
"v_add_f32 v89, 1.0, v89\n"
"v_mfma_f32_16x16x32_bf16 v[28:31], v[176:179], a[192:195], v[28:31]\n"
"s_add_u32 s44, s84, 2048\n"
"buffer_load_dwordx4 v[168:171], v1, s[36:39], s44 offen offset:0 sc1\n"
"v_mfma_f32_16x16x32_bf16 v[28:31], v[180:183], a[208:211], v[28:31]\n"
"v_rcp_f32 v88, v88\n"
"v_mfma_f32_16x16x32_bf16 v[32:35], v[176:179], a[196:199], v[32:35]\n"
"v_rcp_f32 v89, v89\n"
"v_mfma_f32_16x16x32_bf16 v[32:35], v[180:183], a[212:215], v[32:35]\n"
"v_mul_f32 v88, v54, v88\n"
"v_mul_f32 v89, v55, v89\n"
"v_mfma_f32_16x16x32_bf16 v[36:39], v[176:179], a[200:203], v[36:39]\n"
"buffer_load_dwordx4 v[172:175], v1, s[36:39], s44 offen offset:1024 sc1\n"
"v_mfma_f32_16x16x32_bf16 v[36:39], v[180:183], a[216:219], v[36:39]\n"
"v_cvt_pk_bf16_f32 v64, v86, v87\n"
"v_cvt_pk_bf16_f32 v65, v88, v89\n"
"v_mfma_f32_16x16x32_bf16 v[40:43], v[176:179], a[204:207], v[40:43]\n"
"v_exp_f32 v90, v56\n"
"v_mfma_f32_16x16x32_bf16 v[40:43], v[180:183], a[220:223], v[40:43]\n"
"s_waitcnt vmcnt(24)\n"
"v_exp_f32 v91, v57\n"
"v_mfma_f32_16x16x32_bf16 v[28:31], v[184:187], a[224:227], v[28:31]\n"
"s_add_u32 s44, s84, 4096\n"
"buffer_load_dwordx4 v[176:179], v1, s[36:39], s44 offen offset:0 sc1\n"
"v_mfma_f32_16x16x32_bf16 v[28:31], v[188:191], a[240:243], v[28:31]\n"
"v_add_f32 v90, 1.0, v90\n"
"v_add_f32 v91, 1.0, v91\n"
"v_mfma_f32_16x16x32_bf16 v[32:35], v[184:187], a[228:231], v[32:35]\n"
"ds_read_b128 a[0:3], v1 offset:0\n"
"v_rcp_f32 v90, v90\n"
"v_mfma_f32_16x16x32_bf16 v[32:35], v[188:191], a[244:247], v[32:35]\n"
"ds_read_b128 a[4:7], v1 offset:1024\n"
"v_rcp_f32 v91, v91\n"
"v_mfma_f32_16x16x32_bf16 v[36:39], v[184:187], a[232:235], v[36:39]\n"
"buffer_load_dwordx4 v[180:183], v1, s[36:39], s44 offen offset:1024 sc1\n"
"ds_read_b128 a[8:11], v1 offset:2048\n"
"v_mfma_f32_16x16x32_bf16 v[36:39], v[188:191], a[248:251], v[36:39]\n"
"ds_read_b128 a[12:15], v1 offset:3072\n"
"v_mul_f32 v90, v56, v90\n"
"v_mul_f32 v91, v57, v91\n"
"v_mfma_f32_16x16x32_bf16 v[40:43], v[184:187], a[236:239], v[40:43]\n"
"ds_read_b128 a[16:19], v1 offset:4096\n"
"v_exp_f32 v92, v58\n"
"v_mfma_f32_16x16x32_bf16 v[40:43], v[188:191], a[252:255], v[40:43]\n"
"s_waitcnt vmcnt(24)\n"
"ds_read_b128 a[20:23], v1 offset:5120\n"
"v_exp_f32 v93, v59\n"
"v_mfma_f32_16x16x32_bf16 v[28:31], v[192:195], v[224:227], v[28:31]\n"
"s_add_u32 s44, s84, 6144\n"
"buffer_load_dwordx4 v[184:187], v1, s[36:39], s44 offen offset:0 sc1\n"
"ds_read_b128 a[24:27], v1 offset:6144\n"
"v_mfma_f32_16x16x32_bf16 v[44:47], v[196:199], v[240:243], v[28:31]\n"
"ds_read_b128 a[28:31], v1 offset:7168\n"
"v_add_f32 v92, 1.0, v92\n"
"v_add_f32 v93, 1.0, v93\n"
"v_mfma_f32_16x16x32_bf16 v[32:35], v[192:195], v[228:231], v[32:35]\n"
"ds_read_b128 a[32:35], v1 offset:8192\n"
"v_rcp_f32 v92, v92\n"
"v_mfma_f32_16x16x32_bf16 v[48:51], v[196:199], v[244:247], v[32:35]\n"
"ds_read_b128 a[36:39], v1 offset:9216\n"
"v_rcp_f32 v93, v93\n"
"v_mfma_f32_16x16x32_bf16 v[36:39], v[192:195], v[232:235], v[36:39]\n"
"buffer_load_dwordx4 v[188:191], v1, s[36:39], s44 offen offset:1024 sc1\n"
"ds_read_b128 a[40:43], v1 offset:10240\n"
"v_mfma_f32_16x16x32_bf16 v[52:55], v[196:199], v[248:251], v[36:39]\n"
"ds_read_b128 a[44:47], v1 offset:11264\n"
"v_mul_f32 v92, v58, v92\n"
"v_mul_f32 v93, v59, v93\n"
"v_mfma_f32_16x16x32_bf16 v[40:43], v[192:195], v[236:239], v[40:43]\n"
"ds_write_b64 v16, v[64:65] offset:59392\n"
"v_cvt_pk_bf16_f32 v66, v90, v91\n"
"v_mfma_f32_16x16x32_bf16 v[56:59], v[196:199], v[252:255], v[40:43]\n"
"s_waitcnt vmcnt(24) lgkmcnt(5)\n"
"v_cvt_pk_bf16_f32 v67, v92, v93\n"
"ds_write_b64 v16, v[66:67] offset:60416\n"
"v_mfma_f32_16x16x32_bf16 v[28:31], v[200:203], a[0:3], 0\n"
"s_add_u32 s44, s84, 8192\n"
"buffer_load_dwordx4 v[192:195], v1, s[36:39], s44 offen offset:0 sc1\n"
"v_mfma_f32_16x16x32_bf16 v[28:31], v[204:207], a[16:19], v[28:31]\n"
"ds_read_b128 a[48:51], v1 offset:12288\n"
"ds_read_b128 a[52:55], v1 offset:13312\n"
"v_exp_f32 v86, v44\n"
"v_mfma_f32_16x16x32_bf16 v[32:35], v[200:203], a[4:7], 0\n"
"ds_read_b128 a[56:59], v1 offset:14336\n"
"ds_read_b128 a[60:63], v1 offset:15360\n"
"v_exp_f32 v87, v45\n"
"v_mfma_f32_16x16x32_bf16 v[32:35], v[204:207], a[20:23], v[32:35]\n"
"v_add_f32 v86, 1.0, v86\n"
"v_add_f32 v87, 1.0, v87\n"
"v_rcp_f32 v86, v86\n"
"v_mfma_f32_16x16x32_bf16 v[36:39], v[200:203], a[8:11], 0\n"
"buffer_load_dwordx4 v[196:199], v1, s[36:39], s44 offen offset:1024 sc1\n"
"v_mfma_f32_16x16x32_bf16 v[36:39], v[204:207], a[24:27], v[36:39]\n"
"ds_read_b128 a[64:67], v1 offset:16384\n"
"ds_read_b128 a[68:71], v1 offset:17408\n"
"v_rcp_f32 v87, v87\n"
"v_mfma_f32_16x16x32_bf16 v[40:43], v[200:203], a[12:15], 0\n"
"ds_read_b128 a[72:75], v1 offset:18432\n"
"ds_read_b128 a[76:79], v1 offset:19456\n"
"v_mul_f32 v86, v44, v86\n"
"v_mul_f32 v87, v45, v87\n"
"v_mfma_f32_16x16x32_bf16 v[40:43], v[204:207], a[28:31], v[40:43]\n"
"s_waitcnt vmcnt(24) lgkmcnt(4)\n"
"v_exp_f32 v88, v46\n"
"v_exp_f32 v89, v47\n"
"v_mfma_f32_16x16x32_bf16 v[28:31], v[208:211], a[32:35], v[28:31]\n"
"s_add_u32 s44, s84, 10240\n"
"buffer_load_dwordx4 v[200:203], v1, s[36:39], s44 offen offset:0 sc1\n"
"v_add_f32 v88, 1.0, v88\n"
"v_add_f32 v89, 1.0, v89\n"
"v_mfma_f32_16x16x32_bf16 v[28:31], v[212:215], a[48:51], v[28:31]\n"
"ds_read_b128 a[80:83], v1 offset:20480\n"
"ds_read_b128 a[84:87], v1 offset:21504\n"
"v_rcp_f32 v88, v88\n"
"v_mfma_f32_16x16x32_bf16 v[32:35], v[208:211], a[36:39], v[32:35]\n"
"ds_read_b128 a[88:91], v1 offset:22528\n"
"ds_read_b128 a[92:95], v1 offset:23552\n"
"v_rcp_f32 v89, v89\n"
"v_mfma_f32_16x16x32_bf16 v[32:35], v[212:215], a[52:55], v[32:35]\n"
"v_mul_f32 v88, v46, v88\n"
"v_mul_f32 v89, v47, v89\n"
"v_cvt_pk_bf16_f32 v60, v86, v87\n"
"v_cvt_pk_bf16_f32 v61, v88, v89\n"
"v_mfma_f32_16x16x32_bf16 v[36:39], v[208:211], a[40:43], v[36:39]\n"
"buffer_load_dwordx4 v[204:207], v1, s[36:39], s44 offen offset:1024 sc1\n"
"v_mfma_f32_16x16x32_bf16 v[36:39], v[212:215], a[56:59], v[36:39]\n"
"s_waitcnt lgkmcnt(12)\n"
"s_barrier\n"
"ds_read_b128 a[96:99], v1 offset:24576\n"
"ds_read_b128 a[100:103], v1 offset:25600\n"
"v_exp_f32 v90, v48\n"
"v_mfma_f32_16x16x32_bf16 v[40:43], v[208:211], a[44:47], v[40:43]\n"
"ds_read_b128 a[104:107], v1 offset:26624\n"
"ds_read_b128 a[108:111], v1 offset:27648\n"
"v_exp_f32 v91, v49\n"
"v_mfma_f32_16x16x32_bf16 v[40:43], v[212:215], a[60:63], v[40:43]\n"
"s_waitcnt vmcnt(24) lgkmcnt(4)\n"
"v_add_f32 v90, 1.0, v90\n"
"v_add_f32 v91, 1.0, v91\n"
"v_rcp_f32 v90, v90\n"
"v_mfma_f32_16x16x32_bf16 v[28:31], v[216:219], a[64:67], v[28:31]\n"
"s_add_u32 s44, s84, 12288\n"
"buffer_load_dwordx4 v[208:211], v1, s[36:39], s44 offen offset:0 sc1\n"
"v_rcp_f32 v91, v91\n"
"v_mfma_f32_16x16x32_bf16 v[28:31], v[220:223], a[80:83], v[28:31]\n"
"ds_read_b128 a[112:115], v1 offset:28672\n"
"ds_read_b128 a[116:119], v1 offset:29696\n"
"v_mul_f32 v90, v48, v90\n"
"v_mul_f32 v91, v49, v91\n"
"v_mfma_f32_16x16x32_bf16 v[32:35], v[216:219], a[68:71], v[32:35]\n"
"ds_read_b128 a[120:123], v1 offset:30720\n"
"ds_read_b128 a[124:127], v1 offset:31744\n"
"v_exp_f32 v92, v50\n"
"v_mfma_f32_16x16x32_bf16 v[32:35], v[220:223], a[84:87], v[32:35]\n"
"v_exp_f32 v93, v51\n"
"v_add_f32 v92, 1.0, v92\n"
"v_add_f32 v93, 1.0, v93\n"
"v_mfma_f32_16x16x32_bf16 v[36:39], v[216:219], a[72:75], v[36:39]\n"
"buffer_load_dwordx4 v[212:215], v1, s[36:39], s44 offen offset:1024 sc1\n"
"v_mfma_f32_16x16x32_bf16 v[36:39], v[220:223], a[88:91], v[36:39]\n"
"ds_read_b128 a[128:131], v1 offset:32768\n"
"ds_read_b128 a[132:135], v1 offset:33792\n"
"v_rcp_f32 v92, v92\n"
"v_mfma_f32_16x16x32_bf16 v[40:43], v[216:219], a[76:79], v[40:43]\n"
"ds_read_b128 a[136:139], v1 offset:34816\n"
"ds_read_b128 a[140:143], v1 offset:35840\n"
"v_rcp_f32 v93, v93\n"
"v_mfma_f32_16x16x32_bf16 v[40:43], v[220:223], a[92:95], v[40:43]\n"
"s_waitcnt vmcnt(24) lgkmcnt(4)\n"
"v_mul_f32 v92, v50, v92\n"
"v_mul_f32 v93, v51, v93\n"
"ds_write_b64 v17, v[60:61] offset:0\n"
"v_cvt_pk_bf16_f32 v62, v90, v91\n"
"v_mfma_f32_16x16x32_bf16 v[28:31], v[112:115], a[96:99], v[28:31]\n"
"s_add_u32 s44, s84, 14336\n"
"buffer_load_dwordx4 v[216:219], v1, s[36:39], s44 offen offset:0 sc1\n"
"v_cvt_pk_bf16_f32 v63, v92, v93\n"
"ds_write_b64 v17, v[62:63] offset:1024\n"
"v_mfma_f32_16x16x32_bf16 v[28:31], v[116:119], a[112:115], v[28:31]\n"
"ds_read_b128 a[144:147], v1 offset:36864\n"
"ds_read_b128 a[148:151], v1 offset:37888\n"
"v_exp_f32 v86, v52\n"
"v_mfma_f32_16x16x32_bf16 v[32:35], v[112:115], a[100:103], v[32:35]\n"
"ds_read_b128 a[152:155], v1 offset:38912\n"
"ds_read_b128 a[156:159], v1 offset:39936\n"
"v_exp_f32 v87, v53\n"
"v_mfma_f32_16x16x32_bf16 v[32:35], v[116:119], a[116:119], v[32:35]\n"
"v_add_f32 v86, 1.0, v86\n"
"v_add_f32 v87, 1.0, v87\n"
"v_rcp_f32 v86, v86\n"
"v_mfma_f32_16x16x32_bf16 v[36:39], v[112:115], a[104:107], v[36:39]\n"
"buffer_load_dwordx4 v[220:223], v1, s[36:39], s44 offen offset:1024 sc1\n"
"v_mfma_f32_16x16x32_bf16 v[36:39], v[116:119], a[120:123], v[36:39]\n"
"ds_read_b128 a[160:163], v1 offset:40960\n"
"ds_read_b128 a[164:167], v1 offset:41984\n"
"v_rcp_f32 v87, v87\n"
"v_mfma_f32_16x16x32_bf16 v[40:43], v[112:115], a[108:111], v[40:43]\n"
"ds_read_b128 a[168:171], v1 offset:43008\n"
"ds_read_b128 a[172:175], v1 offset:44032\n"
"v_mul_f32 v86, v52, v86\n"
"v_mul_f32 v87, v53, v87\n"
"v_mfma_f32_16x16x32_bf16 v[40:43], v[116:119], a[124:127], v[40:43]\n"
"s_waitcnt vmcnt(24) lgkmcnt(4)\n"
"v_exp_f32 v88, v54\n"
"v_exp_f32 v89, v55\n"
"v_mfma_f32_16x16x32_bf16 v[28:31], v[120:123], a[128:131], v[28:31]\n"
"s_add_u32 s44, s84, 16384\n"
"buffer_load_dwordx4 v[112:115], v1, s[36:39], s44 offen offset:0 sc1\n"
"v_add_f32 v88, 1.0, v88\n"
"v_add_f32 v89, 1.0, v89\n"
"v_mfma_f32_16x16x32_bf16 v[28:31], v[124:127], a[144:147], v[28:31]\n"
"ds_read_b128 a[176:179], v1 offset:45056\n"
"ds_read_b128 a[180:183], v1 offset:46080\n"
"v_rcp_f32 v88, v88\n"
"v_mfma_f32_16x16x32_bf16 v[32:35], v[120:123], a[132:135], v[32:35]\n"
"ds_read_b128 a[184:187], v1 offset:47104\n"
"ds_read_b128 a[188:191], v1 offset:48128\n"
"v_rcp_f32 v89, v89\n"
"v_mfma_f32_16x16x32_bf16 v[32:35], v[124:127], a[148:151], v[32:35]\n"
"v_mul_f32 v88, v54, v88\n"
"v_mul_f32 v89, v55, v89\n"
"v_cvt_pk_bf16_f32 v64, v86, v87\n"
"v_cvt_pk_bf16_f32 v65, v88, v89\n"
"v_mfma_f32_16x16x32_bf16 v[36:39], v[120:123], a[136:139], v[36:39]\n"
"buffer_load_dwordx4 v[116:119], v1, s[36:39], s44 offen offset:1024 sc1\n"
"v_mfma_f32_16x16x32_bf16 v[36:39], v[124:127], a[152:155], v[36:39]\n"
"ds_read_b128 a[192:195], v1 offset:49152\n"
"ds_read_b128 a[196:199], v1 offset:50176\n"
"v_exp_f32 v90, v56\n"
"v_mfma_f32_16x16x32_bf16 v[40:43], v[120:123], a[140:143], v[40:43]\n"
"ds_read_b128 a[200:203], v1 offset:51200\n"
"ds_read_b128 a[204:207], v1 offset:52224\n"
"v_exp_f32 v91, v57\n"
"v_mfma_f32_16x16x32_bf16 v[40:43], v[124:127], a[156:159], v[40:43]\n"
"s_waitcnt vmcnt(24) lgkmcnt(4)\n"
"v_add_f32 v90, 1.0, v90\n"
"v_add_f32 v91, 1.0, v91\n"
"v_rcp_f32 v90, v90\n"
"v_mfma_f32_16x16x32_bf16 v[28:31], v[128:131], a[160:163], v[28:31]\n"
"s_add_u32 s44, s85, 0\n"
"buffer_load_dwordx4 v[120:123], v1, s[36:39], s44 offen offset:0 sc1\n"
"v_rcp_f32 v91, v91\n"
"v_mfma_f32_16x16x32_bf16 v[28:31], v[132:135], a[176:179], v[28:31]\n"
"ds_read_b128 a[208:211], v1 offset:53248\n"
"ds_read_b128 a[212:215], v1 offset:54272\n"
"v_mul_f32 v90, v56, v90\n"
"v_mul_f32 v91, v57, v91\n"
"v_mfma_f32_16x16x32_bf16 v[32:35], v[128:131], a[164:167], v[32:35]\n"
"ds_read_b128 a[216:219], v1 offset:55296\n"
"ds_read_b128 a[220:223], v1 offset:56320\n"
"v_exp_f32 v92, v58\n"
"v_mfma_f32_16x16x32_bf16 v[32:35], v[132:135], a[180:183], v[32:35]\n"
"v_exp_f32 v93, v59\n"
"v_add_f32 v92, 1.0, v92\n"
"v_add_f32 v93, 1.0, v93\n"
"v_mfma_f32_16x16x32_bf16 v[36:39], v[128:131], a[168:171], v[36:39]\n"
"buffer_load_dwordx4 v[124:127], v1, s[36:39], s44 offen offset:1024 sc1\n"
"v_mfma_f32_16x16x32_bf16 v[36:39], v[132:135], a[184:187], v[36:39]\n"
"ds_read_b128 a[224:227], v1 offset:57344\n"
"ds_read_b128 a[228:231], v1 offset:58368\n"
"v_rcp_f32 v92, v92\n"
"v_mfma_f32_16x16x32_bf16 v[40:43], v[128:131], a[172:175], v[40:43]\n"
"ds_read_b128 a[232:235], v1 offset:59392\n"
"ds_read_b128 a[236:239], v1 offset:60416\n"
"v_rcp_f32 v93, v93\n"
"v_mfma_f32_16x16x32_bf16 v[40:43], v[132:135], a[188:191], v[40:43]\n"
"s_waitcnt vmcnt(24) lgkmcnt(4)\n"
"v_mul_f32 v92, v58, v92\n"
"v_mul_f32 v93, v59, v93\n"
"ds_write_b64 v17, v[64:65] offset:2048\n"
"v_cvt_pk_bf16_f32 v66, v90, v91\n"
"v_mfma_f32_16x16x32_bf16 v[28:31], v[136:139], a[192:195], v[28:31]\n"
"s_add_u32 s44, s85, 2048\n"
"buffer_load_dwordx4 v[128:131], v1, s[36:39], s44 offen offset:0 sc1\n"
"v_cvt_pk_bf16_f32 v67, v92, v93\n"
"ds_write_b64 v17, v[66:67] offset:3072\n"
"v_mfma_f32_16x16x32_bf16 v[28:31], v[140:143], a[208:211], v[28:31]\n"
"ds_read_b128 a[240:243], v1 offset:61440\n"
"ds_read_b128 a[244:247], v1 offset:62464\n"
"v_mfma_f32_16x16x32_bf16 v[32:35], v[136:139], a[196:199], v[32:35]\n"
"s_waitcnt lgkmcnt(2)\n"
"s_barrier\n"
"ds_read_b128 a[248:251], v1 offset:63488\n"
"ds_read_b128 a[252:255], v1 offset:64512\n"
"v_mfma_f32_16x16x32_bf16 v[32:35], v[140:143], a[212:215], v[32:35]\n"
"v_mfma_f32_16x16x32_bf16 v[36:39], v[136:139], a[200:203], v[36:39]\n"
"buffer_load_dwordx4 v[132:135], v1, s[36:39], s44 offen offset:1024 sc1\n"
"v_mfma_f32_16x16x32_bf16 v[36:39], v[140:143], a[216:219], v[36:39]\n"
"ds_read_b128 v[224:227], v18 offset:0\n"
"ds_read_b128 v[228:231], v18 offset:1024\n"
"v_mfma_f32_16x16x32_bf16 v[40:43], v[136:139], a[204:207], v[40:43]\n"
"ds_read_b128 v[232:235], v18 offset:2048\n"
"ds_read_b128 v[236:239], v18 offset:3072\n"
"v_mfma_f32_16x16x32_bf16 v[40:43], v[140:143], a[220:223], v[40:43]\n"
"s_waitcnt vmcnt(24) lgkmcnt(4)\n"
"v_mfma_f32_16x16x32_bf16 v[28:31], v[144:147], a[224:227], v[28:31]\n"
"s_add_u32 s44, s85, 4096\n"
"buffer_load_dwordx4 v[136:139], v1, s[36:39], s44 offen offset:0 sc1\n"
"v_mfma_f32_16x16x32_bf16 v[28:31], v[148:151], a[240:243], v[28:31]\n"
"ds_read_b128 v[240:243], v18 offset:4096\n"
"ds_read_b128 v[244:247], v18 offset:5120\n"
"v_mfma_f32_16x16x32_bf16 v[32:35], v[144:147], a[228:231], v[32:35]\n"
"ds_read_b128 v[248:251], v18 offset:6144\n"
"ds_read_b128 v[252:255], v18 offset:7168\n"
"v_mfma_f32_16x16x32_bf16 v[32:35], v[148:151], a[244:247], v[32:35]\n"
"v_mfma_f32_16x16x32_bf16 v[36:39], v[144:147], a[232:235], v[36:39]\n"
"buffer_load_dwordx4 v[140:143], v1, s[36:39], s44 offen offset:1024 sc1\n"
"v_mfma_f32_16x16x32_bf16 v[36:39], v[148:151], a[248:251], v[36:39]\n"
"v_mfma_f32_16x16x32_bf16 v[40:43], v[144:147], a[236:239], v[40:43]\n"
"v_mfma_f32_16x16x32_bf16 v[40:43], v[148:151], a[252:255], v[40:43]\n"
"s_waitcnt vmcnt(24) lgkmcnt(0)\n"
"v_mfma_f32_16x16x32_bf16 v[28:31], v[152:155], v[224:227], v[28:31]\n"
"s_add_u32 s44, s85, 6144\n"
"buffer_load_dwordx4 v[144:147], v1, s[36:39], s44 offen offset:0 sc1\n"
"v_mfma_f32_16x16x32_bf16 v[44:47], v[156:159], v[240:243], v[28:31]\n"
"v_mfma_f32_16x16x32_bf16 v[32:35], v[152:155], v[228:231], v[32:35]\n"
"v_mfma_f32_16x16x32_bf16 v[48:51], v[156:159], v[244:247], v[32:35]\n"
"v_mfma_f32_16x16x32_bf16 v[36:39], v[152:155], v[232:235], v[36:39]\n"
"buffer_load_dwordx4 v[148:151], v1, s[36:39], s44 offen offset:1024 sc1\n"
"v_mfma_f32_16x16x32_bf16 v[52:55], v[156:159], v[248:251], v[36:39]\n"
"v_mfma_f32_16x16x32_bf16 v[40:43], v[152:155], v[236:239], v[40:43]\n"
"v_mfma_f32_16x16x32_bf16 v[56:59], v[156:159], v[252:255], v[40:43]\n"
"s_waitcnt vmcnt(24)\n"
"v_mfma_f32_16x16x32_bf16 v[28:31], v[160:163], a[0:3], 0\n"
"s_add_u32 s44, s85, 8192\n"
"buffer_load_dwordx4 v[152:155], v1, s[36:39], s44 offen offset:0 sc1\n"
"v_mfma_f32_16x16x32_bf16 v[28:31], v[164:167], a[16:19], v[28:31]\n"
"v_mfma_f32_16x16x32_bf16 v[32:35], v[160:163], a[4:7], 0\n"
"v_cvt_pk_bf16_f32 v60, v44, v45\n"
"v_cvt_pk_bf16_f32 v61, v46, v47\n"
"v_mfma_f32_16x16x32_bf16 v[32:35], v[164:167], a[20:23], v[32:35]\n"
"v_cvt_pk_bf16_f32 v62, v48, v49\n"
"v_cvt_pk_bf16_f32 v63, v50, v51\n"
"v_mfma_f32_16x16x32_bf16 v[36:39], v[160:163], a[8:11], 0\n"
"buffer_load_dwordx4 v[156:159], v1, s[36:39], s44 offen offset:1024 sc1\n"
"v_mfma_f32_16x16x32_bf16 v[36:39], v[164:167], a[24:27], v[36:39]\n"
"v_cvt_pk_bf16_f32 v64, v52, v53\n"
"v_cvt_pk_bf16_f32 v65, v54, v55\n"
"v_mfma_f32_16x16x32_bf16 v[40:43], v[160:163], a[12:15], 0\n"
"v_cvt_pk_bf16_f32 v66, v56, v57\n"
"v_cvt_pk_bf16_f32 v67, v58, v59\n"
"v_mfma_f32_16x16x32_bf16 v[40:43], v[164:167], a[28:31], v[40:43]\n"
"s_waitcnt vmcnt(24)\n"
"v_add3_u32 v68, v6, v60, v61\n"
"v_add3_u32 v68, v68, v62, v63\n"
"v_mfma_f32_16x16x32_bf16 v[28:31], v[168:171], a[32:35], v[28:31]\n"
"s_add_u32 s44, s85, 10240\n"
"buffer_load_dwordx4 v[160:163], v1, s[36:39], s44 offen offset:0 sc1\n"
"v_mfma_f32_16x16x32_bf16 v[28:31], v[172:175], a[48:51], v[28:31]\n"
"v_add3_u32 v69, v6, v64, v65\n"
"v_add3_u32 v69, v69, v66, v67\n"
"v_mfma_f32_16x16x32_bf16 v[32:35], v[168:171], a[36:39], v[32:35]\n"
"buffer_store_dwordx4 v[60:63], v1, s[36:39], s64 offen sc1\n"
"v_mfma_f32_16x16x32_bf16 v[32:35], v[172:175], a[52:55], v[32:35]\n"
"buffer_store_dwordx4 v[64:67], v1, s[36:39], s65 offen sc1\n"
"v_mfma_f32_16x16x32_bf16 v[36:39], v[168:171], a[40:43], v[36:39]\n"
"buffer_load_dwordx4 v[164:167], v1, s[36:39], s44 offen offset:1024 sc1\n"
"v_mfma_f32_16x16x32_bf16 v[36:39], v[172:175], a[56:59], v[36:39]\n"
"buffer_store_dword v68, v8, s[36:39], s64 offen offset:1024 sc1\n"
"v_mfma_f32_16x16x32_bf16 v[40:43], v[168:171], a[44:47], v[40:43]\n"
"buffer_store_dword v69, v8, s[36:39], s65 offen offset:1024 sc1\n"
"v_mfma_f32_16x16x32_bf16 v[40:43], v[172:175], a[60:63], v[40:43]\n"
"s_waitcnt vmcnt(28)\n"
"v_mfma_f32_16x16x32_bf16 v[28:31], v[176:179], a[64:67], v[28:31]\n"
"s_add_u32 s44, s85, 12288\n"
"buffer_load_dwordx4 v[168:171], v1, s[36:39], s44 offen offset:0 sc1\n"
"v_mfma_f32_16x16x32_bf16 v[28:31], v[180:183], a[80:83], v[28:31]\n"
"v_mfma_f32_16x16x32_bf16 v[32:35], v[176:179], a[68:71], v[32:35]\n"
"v_mfma_f32_16x16x32_bf16 v[32:35], v[180:183], a[84:87], v[32:35]\n"
"v_mfma_f32_16x16x32_bf16 v[36:39], v[176:179], a[72:75], v[36:39]\n"
"buffer_load_dwordx4 v[172:175], v1, s[36:39], s44 offen offset:1024 sc1\n"
"v_mfma_f32_16x16x32_bf16 v[36:39], v[180:183], a[88:91], v[36:39]\n"
"v_mfma_f32_16x16x32_bf16 v[40:43], v[176:179], a[76:79], v[40:43]\n"
"v_mfma_f32_16x16x32_bf16 v[40:43], v[180:183], a[92:95], v[40:43]\n"
"s_waitcnt vmcnt(28)\n"
"v_mfma_f32_16x16x32_bf16 v[28:31], v[184:187], a[96:99], v[28:31]\n"
"s_add_u32 s44, s85, 14336\n"
"buffer_load_dwordx4 v[176:179], v1, s[36:39], s44 offen offset:0 sc1\n"
"v_mfma_f32_16x16x32_bf16 v[28:31], v[188:191], a[112:115], v[28:31]\n"
"v_mfma_f32_16x16x32_bf16 v[32:35], v[184:187], a[100:103], v[32:35]\n"
"v_mfma_f32_16x16x32_bf16 v[32:35], v[188:191], a[116:119], v[32:35]\n"
"v_mfma_f32_16x16x32_bf16 v[36:39], v[184:187], a[104:107], v[36:39]\n"
"buffer_load_dwordx4 v[180:183], v1, s[36:39], s44 offen offset:1024 sc1\n"
"v_mfma_f32_16x16x32_bf16 v[36:39], v[188:191], a[120:123], v[36:39]\n"
"v_mfma_f32_16x16x32_bf16 v[40:43], v[184:187], a[108:111], v[40:43]\n"
"v_mfma_f32_16x16x32_bf16 v[40:43], v[188:191], a[124:127], v[40:43]\n"
"s_waitcnt vmcnt(28)\n"
"v_mfma_f32_16x16x32_bf16 v[28:31], v[192:195], a[128:131], v[28:31]\n"
"s_add_u32 s44, s85, 16384\n"
"buffer_load_dwordx4 v[184:187], v1, s[36:39], s44 offen offset:0 sc1\n"
"v_mfma_f32_16x16x32_bf16 v[28:31], v[196:199], a[144:147], v[28:31]\n"
"v_mfma_f32_16x16x32_bf16 v[32:35], v[192:195], a[132:135], v[32:35]\n"
"v_mfma_f32_16x16x32_bf16 v[32:35], v[196:199], a[148:151], v[32:35]\n"
"v_mfma_f32_16x16x32_bf16 v[36:39], v[192:195], a[136:139], v[36:39]\n"
"buffer_load_dwordx4 v[188:191], v1, s[36:39], s44 offen offset:1024 sc1\n"
"v_mfma_f32_16x16x32_bf16 v[36:39], v[196:199], a[152:155], v[36:39]\n"
"v_mfma_f32_16x16x32_bf16 v[40:43], v[192:195], a[140:143], v[40:43]\n"
"v_mfma_f32_16x16x32_bf16 v[40:43], v[196:199], a[156:159], v[40:43]\n"
"s_waitcnt vmcnt(28)\n"
"v_mfma_f32_16x16x32_bf16 v[28:31], v[200:203], a[160:163], v[28:31]\n"
"s_add_u32 s44, s86, 0\n"
"buffer_load_dwordx4 v[192:195], v1, s[36:39], s44 offen offset:0 sc1\n"
"v_mfma_f32_16x16x32_bf16 v[28:31], v[204:207], a[176:179], v[28:31]\n"
"v_mfma_f32_16x16x32_bf16 v[32:35], v[200:203], a[164:167], v[32:35]\n"
"v_mfma_f32_16x16x32_bf16 v[32:35], v[204:207], a[180:183], v[32:35]\n"
"v_mfma_f32_16x16x32_bf16 v[36:39], v[200:203], a[168:171], v[36:39]\n"
"buffer_load_dwordx4 v[196:199], v1, s[36:39], s44 offen offset:1024 sc1\n"
"v_mfma_f32_16x16x32_bf16 v[36:39], v[204:207], a[184:187], v[36:39]\n"
"v_mfma_f32_16x16x32_bf16 v[40:43], v[200:203], a[172:175], v[40:43]\n"
"v_mfma_f32_16x16x32_bf16 v[40:43], v[204:207], a[188:191], v[40:43]\n"
"s_waitcnt vmcnt(28)\n"
"v_mfma_f32_16x16x32_bf16 v[28:31], v[208:211], a[192:195], v[28:31]\n"
"s_add_u32 s44, s86, 2048\n"
"buffer_load_dwordx4 v[200:203], v1, s[36:39], s44 offen offset:0 sc1\n"
"v_mfma_f32_16x16x32_bf16 v[28:31], v[212:215], a[208:211], v[28:31]\n"
"v_mfma_f32_16x16x32_bf16 v[32:35], v[208:211], a[196:199], v[32:35]\n"
"v_mfma_f32_16x16x32_bf16 v[32:35], v[212:215], a[212:215], v[32:35]\n"
"v_mfma_f32_16x16x32_bf16 v[36:39], v[208:211], a[200:203], v[36:39]\n"
"buffer_load_dwordx4 v[204:207], v1, s[36:39], s44 offen offset:1024 sc1\n"
"v_mfma_f32_16x16x32_bf16 v[36:39], v[212:215], a[216:219], v[36:39]\n"
"v_mfma_f32_16x16x32_bf16 v[40:43], v[208:211], a[204:207], v[40:43]\n"
"v_mfma_f32_16x16x32_bf16 v[40:43], v[212:215], a[220:223], v[40:43]\n"
"s_waitcnt vmcnt(28)\n"
"v_mfma_f32_16x16x32_bf16 v[28:31], v[216:219], a[224:227], v[28:31]\n"
"s_add_u32 s44, s86, 4096\n"
"buffer_load_dwordx4 v[208:211], v1, s[36:39], s44 offen offset:0 sc1\n"
"v_mfma_f32_16x16x32_bf16 v[28:31], v[220:223], a[240:243], v[28:31]\n"
"v_mfma_f32_16x16x32_bf16 v[32:35], v[216:219], a[228:231], v[32:35]\n"
"v_mfma_f32_16x16x32_bf16 v[32:35], v[220:223], a[244:247], v[32:35]\n"
"v_mfma_f32_16x16x32_bf16 v[36:39], v[216:219], a[232:235], v[36:39]\n"
"buffer_load_dwordx4 v[212:215], v1, s[36:39], s44 offen offset:1024 sc1\n"
"v_mfma_f32_16x16x32_bf16 v[36:39], v[220:223], a[248:251], v[36:39]\n"
"v_mfma_f32_16x16x32_bf16 v[40:43], v[216:219], a[236:239], v[40:43]\n"
"v_mfma_f32_16x16x32_bf16 v[40:43], v[220:223], a[252:255], v[40:43]\n"
"s_waitcnt vmcnt(28)\n"
"v_mfma_f32_16x16x32_bf16 v[28:31], v[112:115], v[224:227], v[28:31]\n"
"s_add_u32 s44, s86, 6144\n"
"buffer_load_dwordx4 v[216:219], v1, s[36:39], s44 offen offset:0 sc1\n"
"v_mfma_f32_16x16x32_bf16 v[44:47], v[116:119], v[240:243], v[28:31]\n"
"v_mfma_f32_16x16x32_bf16 v[32:35], v[112:115], v[228:231], v[32:35]\n"
"v_mfma_f32_16x16x32_bf16 v[48:51], v[116:119], v[244:247], v[32:35]\n"
"v_mfma_f32_16x16x32_bf16 v[36:39], v[112:115], v[232:235], v[36:39]\n"
"buffer_load_dwordx4 v[220:223], v1, s[36:39], s44 offen offset:1024 sc1\n"
"v_mfma_f32_16x16x32_bf16 v[52:55], v[116:119], v[248:251], v[36:39]\n"
"v_mfma_f32_16x16x32_bf16 v[40:43], v[112:115], v[236:239], v[40:43]\n"
"v_mfma_f32_16x16x32_bf16 v[56:59], v[116:119], v[252:255], v[40:43]\n"
"s_waitcnt vmcnt(28)\n"
"v_mfma_f32_16x16x32_bf16 v[28:31], v[120:123], a[0:3], 0\n"
"s_add_u32 s44, s86, 8192\n"
"buffer_load_dwordx4 v[112:115], v1, s[36:39], s44 offen offset:0 sc1\n"
"v_mfma_f32_16x16x32_bf16 v[28:31], v[124:127], a[16:19], v[28:31]\n"
"ds_read_b32 v108, v26\n"
"v_mfma_f32_16x16x32_bf16 v[32:35], v[120:123], a[4:7], 0\n"
"v_cvt_pk_bf16_f32 v60, v44, v45\n"
"v_cvt_pk_bf16_f32 v61, v46, v47\n"
"v_mfma_f32_16x16x32_bf16 v[32:35], v[124:127], a[20:23], v[32:35]\n"
"v_cvt_pk_bf16_f32 v62, v48, v49\n"
"v_cvt_pk_bf16_f32 v63, v50, v51\n"
"v_mfma_f32_16x16x32_bf16 v[36:39], v[120:123], a[8:11], 0\n"
"buffer_load_dwordx4 v[116:119], v1, s[36:39], s44 offen offset:1024 sc1\n"
"v_mfma_f32_16x16x32_bf16 v[36:39], v[124:127], a[24:27], v[36:39]\n"
"v_cvt_pk_bf16_f32 v64, v52, v53\n"
"v_cvt_pk_bf16_f32 v65, v54, v55\n"
"v_mfma_f32_16x16x32_bf16 v[40:43], v[120:123], a[12:15], 0\n"
"v_cvt_pk_bf16_f32 v66, v56, v57\n"
"v_cvt_pk_bf16_f32 v67, v58, v59\n"
"v_mfma_f32_16x16x32_bf16 v[40:43], v[124:127], a[28:31], v[40:43]\n"
"s_waitcnt vmcnt(28)\n"
"v_add3_u32 v68, v6, v60, v61\n"
"v_add3_u32 v68, v68, v62, v63\n"
"v_mfma_f32_16x16x32_bf16 v[28:31], v[128:131], a[32:35], v[28:31]\n"
"s_add_u32 s44, s86, 10240\n"
"buffer_load_dwordx4 v[120:123], v1, s[36:39], s44 offen offset:0 sc1\n"
"v_mfma_f32_16x16x32_bf16 v[28:31], v[132:135], a[48:51], v[28:31]\n"
"v_add3_u32 v68, v68, v64, v65\n"
"v_add3_u32 v68, v68, v66, v67\n"
"v_mfma_f32_16x16x32_bf16 v[32:35], v[128:131], a[36:39], v[32:35]\n"
"buffer_store_dwordx4 v[60:63], v1, s[36:39], s25 offen offset:0 sc1\n"
"v_mfma_f32_16x16x32_bf16 v[32:35], v[132:135], a[52:55], v[32:35]\n"
"buffer_store_dwordx4 v[64:67], v1, s[36:39], s25 offen offset:1024 sc1\n"
"v_mfma_f32_16x16x32_bf16 v[36:39], v[128:131], a[40:43], v[36:39]\n"
"buffer_load_dwordx4 v[124:127], v1, s[36:39], s44 offen offset:1024 sc1\n"
"v_mfma_f32_16x16x32_bf16 v[36:39], v[132:135], a[56:59], v[36:39]\n"
"buffer_store_dword v68, v8, s[36:39], s25 offen offset:2048 sc1\n"
"v_mfma_f32_16x16x32_bf16 v[40:43], v[128:131], a[44:47], v[40:43]\n"
"v_mfma_f32_16x16x32_bf16 v[40:43], v[132:135], a[60:63], v[40:43]\n"
"s_waitcnt vmcnt(31)\n"
"v_mfma_f32_16x16x32_bf16 v[28:31], v[136:139], a[64:67], v[28:31]\n"
"s_add_u32 s44, s86, 12288\n"
"buffer_load_dwordx4 v[128:131], v1, s[36:39], s44 offen offset:0 sc1\n"
"v_mfma_f32_16x16x32_bf16 v[28:31], v[140:143], a[80:83], v[28:31]\n"
"v_mfma_f32_16x16x32_bf16 v[32:35], v[136:139], a[68:71], v[32:35]\n"
"v_mfma_f32_16x16x32_bf16 v[32:35], v[140:143], a[84:87], v[32:35]\n"
"v_mfma_f32_16x16x32_bf16 v[36:39], v[136:139], a[72:75], v[36:39]\n"
"buffer_load_dwordx4 v[132:135], v1, s[36:39], s44 offen offset:1024 sc1\n"
"v_mfma_f32_16x16x32_bf16 v[36:39], v[140:143], a[88:91], v[36:39]\n"
"v_mfma_f32_16x16x32_bf16 v[40:43], v[136:139], a[76:79], v[40:43]\n"
"v_mfma_f32_16x16x32_bf16 v[40:43], v[140:143], a[92:95], v[40:43]\n"
"s_waitcnt vmcnt(31)\n"
"v_mfma_f32_16x16x32_bf16 v[28:31], v[144:147], a[96:99], v[28:31]\n"
"s_add_u32 s44, s86, 14336\n"
"buffer_load_dwordx4 v[136:139], v1, s[36:39], s44 offen offset:0 sc1\n"
"v_mfma_f32_16x16x32_bf16 v[28:31], v[148:151], a[112:115], v[28:31]\n"
"v_mfma_f32_16x16x32_bf16 v[32:35], v[144:147], a[100:103], v[32:35]\n"
"v_mfma_f32_16x16x32_bf16 v[32:35], v[148:151], a[116:119], v[32:35]\n"
"v_mfma_f32_16x16x32_bf16 v[36:39], v[144:147], a[104:107], v[36:39]\n"
"buffer_load_dwordx4 v[140:143], v1, s[36:39], s44 offen offset:1024 sc1\n"
"v_mfma_f32_16x16x32_bf16 v[36:39], v[148:151], a[120:123], v[36:39]\n"
"v_mfma_f32_16x16x32_bf16 v[40:43], v[144:147], a[108:111], v[40:43]\n"
"v_mfma_f32_16x16x32_bf16 v[40:43], v[148:151], a[124:127], v[40:43]\n"
"s_waitcnt vmcnt(31)\n"
"v_mfma_f32_16x16x32_bf16 v[28:31], v[152:155], a[128:131], v[28:31]\n"
"s_add_u32 s44, s86, 16384\n"
"buffer_load_dwordx4 v[144:147], v1, s[36:39], s44 offen offset:0 sc1\n"
"v_mfma_f32_16x16x32_bf16 v[28:31], v[156:159], a[144:147], v[28:31]\n"
"v_mfma_f32_16x16x32_bf16 v[32:35], v[152:155], a[132:135], v[32:35]\n"
"v_mfma_f32_16x16x32_bf16 v[32:35], v[156:159], a[148:151], v[32:35]\n"
"v_mfma_f32_16x16x32_bf16 v[36:39], v[152:155], a[136:139], v[36:39]\n"
"buffer_load_dwordx4 v[148:151], v1, s[36:39], s44 offen offset:1024 sc1\n"
"v_mfma_f32_16x16x32_bf16 v[36:39], v[156:159], a[152:155], v[36:39]\n"
"v_mfma_f32_16x16x32_bf16 v[40:43], v[152:155], a[140:143], v[40:43]\n"
"v_mfma_f32_16x16x32_bf16 v[40:43], v[156:159], a[156:159], v[40:43]\n"
"s_waitcnt vmcnt(29)\n"
"v_mfma_f32_16x16x32_bf16 v[28:31], v[160:163], a[160:163], v[28:31]\n"
"s_add_u32 s44, s87, 0\n"
"buffer_load_dwordx4 v[152:155], v1, s[36:39], s44 offen offset:0 sc1\n"
"v_mfma_f32_16x16x32_bf16 v[28:31], v[164:167], a[176:179], v[28:31]\n"
"v_mfma_f32_16x16x32_bf16 v[32:35], v[160:163], a[164:167], v[32:35]\n"
"v_mfma_f32_16x16x32_bf16 v[32:35], v[164:167], a[180:183], v[32:35]\n"
"v_mfma_f32_16x16x32_bf16 v[36:39], v[160:163], a[168:171], v[36:39]\n"
"buffer_load_dwordx4 v[156:159], v1, s[36:39], s44 offen offset:1024 sc1\n"
"v_mfma_f32_16x16x32_bf16 v[36:39], v[164:167], a[184:187], v[36:39]\n"
"v_mfma_f32_16x16x32_bf16 v[40:43], v[160:163], a[172:175], v[40:43]\n"
"v_mfma_f32_16x16x32_bf16 v[40:43], v[164:167], a[188:191], v[40:43]\n"
"s_waitcnt vmcnt(27)\n"
"v_mfma_f32_16x16x32_bf16 v[28:31], v[168:171], a[192:195], v[28:31]\n"
"s_add_u32 s44, s87, 2048\n"
"buffer_load_dwordx4 v[160:163], v1, s[36:39], s44 offen offset:0 sc1\n"
"v_mfma_f32_16x16x32_bf16 v[28:31], v[172:175], a[208:211], v[28:31]\n"
"v_mfma_f32_16x16x32_bf16 v[32:35], v[168:171], a[196:199], v[32:35]\n"
"v_mfma_f32_16x16x32_bf16 v[32:35], v[172:175], a[212:215], v[32:35]\n"
"v_mfma_f32_16x16x32_bf16 v[36:39], v[168:171], a[200:203], v[36:39]\n"
"buffer_load_dwordx4 v[164:167], v1, s[36:39], s44 offen offset:1024 sc1\n"
"v_mfma_f32_16x16x32_bf16 v[36:39], v[172:175], a[216:219], v[36:39]\n"
"v_mfma_f32_16x16x32_bf16 v[40:43], v[168:171], a[204:207], v[40:43]\n"
"v_mfma_f32_16x16x32_bf16 v[40:43], v[172:175], a[220:223], v[40:43]\n"
"s_waitcnt vmcnt(27)\n"
"v_mfma_f32_16x16x32_bf16 v[28:31], v[176:179], a[224:227], v[28:31]\n"
"s_add_u32 s44, s87, 4096\n"
"buffer_load_dwordx4 v[168:171], v1, s[36:39], s44 offen offset:0 sc1\n"
"v_mfma_f32_16x16x32_bf16 v[28:31], v[180:183], a[240:243], v[28:31]\n"
"v_mfma_f32_16x16x32_bf16 v[32:35], v[176:179], a[228:231], v[32:35]\n"
"v_mfma_f32_16x16x32_bf16 v[32:35], v[180:183], a[244:247], v[32:35]\n"
"v_mfma_f32_16x16x32_bf16 v[36:39], v[176:179], a[232:235], v[36:39]\n"
"buffer_load_dwordx4 v[172:175], v1, s[36:39], s44 offen offset:1024 sc1\n"
"v_mfma_f32_16x16x32_bf16 v[36:39], v[180:183], a[248:251], v[36:39]\n"
"v_mfma_f32_16x16x32_bf16 v[40:43], v[176:179], a[236:239], v[40:43]\n"
"v_mfma_f32_16x16x32_bf16 v[40:43], v[180:183], a[252:255], v[40:43]\n"
"s_waitcnt vmcnt(27)\n"
"v_mfma_f32_16x16x32_bf16 v[28:31], v[184:187], v[224:227], v[28:31]\n"
"s_add_u32 s44, s87, 6144\n"
"buffer_load_dwordx4 v[176:179], v1, s[36:39], s44 offen offset:0 sc1\n"
"v_mfma_f32_16x16x32_bf16 v[44:47], v[188:191], v[240:243], v[28:31]\n"
"v_mfma_f32_16x16x32_bf16 v[32:35], v[184:187], v[228:231], v[32:35]\n"
"v_mfma_f32_16x16x32_bf16 v[48:51], v[188:191], v[244:247], v[32:35]\n"
"v_mfma_f32_16x16x32_bf16 v[36:39], v[184:187], v[232:235], v[36:39]\n"
"buffer_load_dwordx4 v[180:183], v1, s[36:39], s44 offen offset:1024 sc1\n"
"v_mfma_f32_16x16x32_bf16 v[52:55], v[188:191], v[248:251], v[36:39]\n"
"v_mfma_f32_16x16x32_bf16 v[40:43], v[184:187], v[236:239], v[40:43]\n"
"v_mfma_f32_16x16x32_bf16 v[56:59], v[188:191], v[252:255], v[40:43]\n"
"s_waitcnt vmcnt(27)\n"
"v_mfma_f32_16x16x32_bf16 v[28:31], v[192:195], a[0:3], 0\n"
"s_add_u32 s44, s87, 8192\n"
"buffer_load_dwordx4 v[184:187], v1, s[36:39], s44 offen offset:0 sc1\n"
"v_mfma_f32_16x16x32_bf16 v[28:31], v[196:199], a[16:19], v[28:31]\n"
"v_mfma_f32_16x16x32_bf16 v[32:35], v[192:195], a[4:7], 0\n"
"v_cvt_pk_bf16_f32 v60, v44, v45\n"
"v_cvt_pk_bf16_f32 v61, v46, v47\n"
"v_mfma_f32_16x16x32_bf16 v[32:35], v[196:199], a[20:23], v[32:35]\n"
"v_cvt_pk_bf16_f32 v62, v48, v49\n"
"v_cvt_pk_bf16_f32 v63, v50, v51\n"
"v_mfma_f32_16x16x32_bf16 v[36:39], v[192:195], a[8:11], 0\n"
"buffer_load_dwordx4 v[188:191], v1, s[36:39], s44 offen offset:1024 sc1\n"
"v_mfma_f32_16x16x32_bf16 v[36:39], v[196:199], a[24:27], v[36:39]\n"
"v_cvt_pk_bf16_f32 v64, v52, v53\n"
"v_cvt_pk_bf16_f32 v65, v54, v55\n"
"v_mfma_f32_16x16x32_bf16 v[40:43], v[192:195], a[12:15], 0\n"
"v_cvt_pk_bf16_f32 v66, v56, v57\n"
"v_cvt_pk_bf16_f32 v67, v58, v59\n"
"v_mfma_f32_16x16x32_bf16 v[40:43], v[196:199], a[28:31], v[40:43]\n"
"s_waitcnt vmcnt(27)\n"
"v_add3_u32 v68, v6, v60, v61\n"
"v_add3_u32 v68, v68, v62, v63\n"
"v_mfma_f32_16x16x32_bf16 v[28:31], v[200:203], a[32:35], v[28:31]\n"
"s_add_u32 s44, s87, 10240\n"
"buffer_load_dwordx4 v[192:195], v1, s[36:39], s44 offen offset:0 sc1\n"
"v_mfma_f32_16x16x32_bf16 v[28:31], v[204:207], a[48:51], v[28:31]\n"
"v_add3_u32 v68, v68, v64, v65\n"
"v_add3_u32 v68, v68, v66, v67\n"
"v_mfma_f32_16x16x32_bf16 v[32:35], v[200:203], a[36:39], v[32:35]\n"
"buffer_store_dwordx4 v[60:63], v1, s[36:39], s26 offen offset:0 sc1\n"
"v_mfma_f32_16x16x32_bf16 v[32:35], v[204:207], a[52:55], v[32:35]\n"
"buffer_store_dwordx4 v[64:67], v1, s[36:39], s26 offen offset:1024 sc1\n"
"v_mfma_f32_16x16x32_bf16 v[36:39], v[200:203], a[40:43], v[36:39]\n"
"buffer_load_dwordx4 v[196:199], v1, s[36:39], s44 offen offset:1024 sc1\n"
"v_mfma_f32_16x16x32_bf16 v[36:39], v[204:207], a[56:59], v[36:39]\n"
"buffer_store_dword v68, v8, s[36:39], s26 offen offset:2048 sc1\n"
"v_mfma_f32_16x16x32_bf16 v[40:43], v[200:203], a[44:47], v[40:43]\n"
"buffer_load_dwordx4 v[86:89], v21, s[36:39], s66 offen sc1\n"
"v_mfma_f32_16x16x32_bf16 v[40:43], v[204:207], a[60:63], v[40:43]\n"
"s_waitcnt vmcnt(31)\n"
"buffer_load_dwordx4 v[90:93], v21, s[36:39], s67 offen sc1\n"
"v_mfma_f32_16x16x32_bf16 v[28:31], v[208:211], a[64:67], v[28:31]\n"
"s_add_u32 s44, s87, 12288\n"
"buffer_load_dwordx4 v[200:203], v1, s[36:39], s44 offen offset:0 sc1\n"
"v_mfma_f32_16x16x32_bf16 v[28:31], v[212:215], a[80:83], v[28:31]\n"
"buffer_load_dword v94, v22, s[36:39], s66 offen offset:1024 sc1\n"
"v_mfma_f32_16x16x32_bf16 v[32:35], v[208:211], a[68:71], v[32:35]\n"
"buffer_load_dword v95, v22, s[36:39], s67 offen offset:1024 sc1\n"
"v_mfma_f32_16x16x32_bf16 v[32:35], v[212:215], a[84:87], v[32:35]\n"
"v_mfma_f32_16x16x32_bf16 v[36:39], v[208:211], a[72:75], v[36:39]\n"
"buffer_load_dwordx4 v[204:207], v1, s[36:39], s44 offen offset:1024 sc1\n"
"v_mfma_f32_16x16x32_bf16 v[36:39], v[212:215], a[88:91], v[36:39]\n"
"v_mfma_f32_16x16x32_bf16 v[40:43], v[208:211], a[76:79], v[40:43]\n"
"v_mfma_f32_16x16x32_bf16 v[40:43], v[212:215], a[92:95], v[40:43]\n"
"s_waitcnt vmcnt(34)\n"
"v_mfma_f32_16x16x32_bf16 v[28:31], v[216:219], a[96:99], v[28:31]\n"
"s_add_u32 s44, s87, 14336\n"
"buffer_load_dwordx4 v[208:211], v1, s[36:39], s44 offen offset:0 sc1\n"
"v_mfma_f32_16x16x32_bf16 v[28:31], v[220:223], a[112:115], v[28:31]\n"
"v_mfma_f32_16x16x32_bf16 v[32:35], v[216:219], a[100:103], v[32:35]\n"
"v_mfma_f32_16x16x32_bf16 v[32:35], v[220:223], a[116:119], v[32:35]\n"
"v_mfma_f32_16x16x32_bf16 v[36:39], v[216:219], a[104:107], v[36:39]\n"
"buffer_load_dwordx4 v[212:215], v1, s[36:39], s44 offen offset:1024 sc1\n"
"v_mfma_f32_16x16x32_bf16 v[36:39], v[220:223], a[120:123], v[36:39]\n"
"v_mfma_f32_16x16x32_bf16 v[40:43], v[216:219], a[108:111], v[40:43]\n"
"v_mfma_f32_16x16x32_bf16 v[40:43], v[220:223], a[124:127], v[40:43]\n"
"s_waitcnt vmcnt(34)\n"
"v_mfma_f32_16x16x32_bf16 v[28:31], v[112:115], a[128:131], v[28:31]\n"
"s_add_u32 s44, s87, 16384\n"
"buffer_load_dwordx4 v[216:219], v1, s[36:39], s44 offen offset:0 sc1\n"
"v_mfma_f32_16x16x32_bf16 v[28:31], v[116:119], a[144:147], v[28:31]\n"
"v_mfma_f32_16x16x32_bf16 v[32:35], v[112:115], a[132:135], v[32:35]\n"
"v_mfma_f32_16x16x32_bf16 v[32:35], v[116:119], a[148:151], v[32:35]\n"
"v_mfma_f32_16x16x32_bf16 v[36:39], v[112:115], a[136:139], v[36:39]\n"
"buffer_load_dwordx4 v[220:223], v1, s[36:39], s44 offen offset:1024 sc1\n"
"v_mfma_f32_16x16x32_bf16 v[36:39], v[116:119], a[152:155], v[36:39]\n"
"v_mfma_f32_16x16x32_bf16 v[40:43], v[112:115], a[140:143], v[40:43]\n"
"v_mfma_f32_16x16x32_bf16 v[40:43], v[116:119], a[156:159], v[40:43]\n"
"s_waitcnt vmcnt(32)\n"
"v_mfma_f32_16x16x32_bf16 v[28:31], v[120:123], a[160:163], v[28:31]\n"
"s_add_u32 s44, s88, 0\n"
"buffer_load_dwordx4 v[112:115], v1, s[36:39], s44 offen offset:0 sc1\n"
"v_mfma_f32_16x16x32_bf16 v[28:31], v[124:127], a[176:179], v[28:31]\n"
"v_mfma_f32_16x16x32_bf16 v[32:35], v[120:123], a[164:167], v[32:35]\n"
"v_mfma_f32_16x16x32_bf16 v[32:35], v[124:127], a[180:183], v[32:35]\n"
"v_mfma_f32_16x16x32_bf16 v[36:39], v[120:123], a[168:171], v[36:39]\n"
"buffer_load_dwordx4 v[116:119], v1, s[36:39], s44 offen offset:1024 sc1\n"
"v_mfma_f32_16x16x32_bf16 v[36:39], v[124:127], a[184:187], v[36:39]\n"
"v_mfma_f32_16x16x32_bf16 v[40:43], v[120:123], a[172:175], v[40:43]\n"
"v_mfma_f32_16x16x32_bf16 v[40:43], v[124:127], a[188:191], v[40:43]\n"
"s_waitcnt vmcnt(31)\n"
"v_mfma_f32_16x16x32_bf16 v[28:31], v[128:131], a[192:195], v[28:31]\n"
"s_add_u32 s44, s88, 2048\n"
"buffer_load_dwordx4 v[120:123], v1, s[36:39], s44 offen offset:0 sc1\n"
"v_mfma_f32_16x16x32_bf16 v[28:31], v[132:135], a[208:211], v[28:31]\n"
"v_mfma_f32_16x16x32_bf16 v[32:35], v[128:131], a[196:199], v[32:35]\n"
"v_mfma_f32_16x16x32_bf16 v[32:35], v[132:135], a[212:215], v[32:35]\n"
"v_mfma_f32_16x16x32_bf16 v[36:39], v[128:131], a[200:203], v[36:39]\n"
"buffer_load_dwordx4 v[124:127], v1, s[36:39], s44 offen offset:1024 sc1\n"
"v_mfma_f32_16x16x32_bf16 v[36:39], v[132:135], a[216:219], v[36:39]\n"
"v_mfma_f32_16x16x32_bf16 v[40:43], v[128:131], a[204:207], v[40:43]\n"
"v_mfma_f32_16x16x32_bf16 v[40:43], v[132:135], a[220:223], v[40:43]\n"
"s_waitcnt vmcnt(31)\n"
"v_mfma_f32_16x16x32_bf16 v[28:31], v[136:139], a[224:227], v[28:31]\n"
"s_add_u32 s44, s88, 4096\n"
"buffer_load_dwordx4 v[128:131], v1, s[36:39], s44 offen offset:0 sc1\n"
"v_mfma_f32_16x16x32_bf16 v[28:31], v[140:143], a[240:243], v[28:31]\n"
"v_mfma_f32_16x16x32_bf16 v[32:35], v[136:139], a[228:231], v[32:35]\n"
"v_mfma_f32_16x16x32_bf16 v[32:35], v[140:143], a[244:247], v[32:35]\n"
"v_mfma_f32_16x16x32_bf16 v[36:39], v[136:139], a[232:235], v[36:39]\n"
"buffer_load_dwordx4 v[132:135], v1, s[36:39], s44 offen offset:1024 sc1\n"
"v_mfma_f32_16x16x32_bf16 v[36:39], v[140:143], a[248:251], v[36:39]\n"
"v_mfma_f32_16x16x32_bf16 v[40:43], v[136:139], a[236:239], v[40:43]\n"
"v_mfma_f32_16x16x32_bf16 v[40:43], v[140:143], a[252:255], v[40:43]\n"
"s_waitcnt vmcnt(31)\n"
"v_mfma_f32_16x16x32_bf16 v[28:31], v[144:147], v[224:227], v[28:31]\n"
"s_add_u32 s44, s88, 6144\n"
"buffer_load_dwordx4 v[136:139], v1, s[36:39], s44 offen offset:0 sc1\n"
"v_mfma_f32_16x16x32_bf16 v[44:47], v[148:151], v[240:243], v[28:31]\n"
"v_mfma_f32_16x16x32_bf16 v[32:35], v[144:147], v[228:231], v[32:35]\n"
"v_mfma_f32_16x16x32_bf16 v[48:51], v[148:151], v[244:247], v[32:35]\n"
"v_mfma_f32_16x16x32_bf16 v[36:39], v[144:147], v[232:235], v[36:39]\n"
"buffer_load_dwordx4 v[140:143], v1, s[36:39], s44 offen offset:1024 sc1\n"
"v_mfma_f32_16x16x32_bf16 v[52:55], v[148:151], v[248:251], v[36:39]\n"
"v_mfma_f32_16x16x32_bf16 v[40:43], v[144:147], v[236:239], v[40:43]\n"
"v_mfma_f32_16x16x32_bf16 v[56:59], v[148:151], v[252:255], v[40:43]\n"
"s_waitcnt vmcnt(31)\n"
"v_mfma_f32_16x16x32_bf16 v[28:31], v[152:155], a[0:3], 0\n"
"s_add_u32 s44, s88, 8192\n"
"buffer_load_dwordx4 v[144:147], v1, s[36:39], s44 offen offset:0 sc1\n"
"v_mfma_f32_16x16x32_bf16 v[28:31], v[156:159], a[16:19], v[28:31]\n"
"v_mfma_f32_16x16x32_bf16 v[32:35], v[152:155], a[4:7], 0\n"
"v_cvt_pk_bf16_f32 v60, v44, v45\n"
"v_cvt_pk_bf16_f32 v61, v46, v47\n"
"v_mfma_f32_16x16x32_bf16 v[32:35], v[156:159], a[20:23], v[32:35]\n"
"v_cvt_pk_bf16_f32 v62, v48, v49\n"
"v_cvt_pk_bf16_f32 v63, v50, v51\n"
"v_mfma_f32_16x16x32_bf16 v[36:39], v[152:155], a[8:11], 0\n"
"buffer_load_dwordx4 v[148:151], v1, s[36:39], s44 offen offset:1024 sc1\n"
"v_mfma_f32_16x16x32_bf16 v[36:39], v[156:159], a[24:27], v[36:39]\n"
"v_cvt_pk_bf16_f32 v64, v52, v53\n"
"v_cvt_pk_bf16_f32 v65, v54, v55\n"
"v_mfma_f32_16x16x32_bf16 v[40:43], v[152:155], a[12:15], 0\n"
"v_cvt_pk_bf16_f32 v66, v56, v57\n"
"v_cvt_pk_bf16_f32 v67, v58, v59\n"
"v_mfma_f32_16x16x32_bf16 v[40:43], v[156:159], a[28:31], v[40:43]\n"
"s_waitcnt vmcnt(31)\n"
"v_add3_u32 v68, v6, v60, v61\n"
"v_add3_u32 v68, v68, v62, v63\n"
"v_mfma_f32_16x16x32_bf16 v[28:31], v[160:163], a[32:35], v[28:31]\n"
"s_add_u32 s44, s88, 10240\n"
"buffer_load_dwordx4 v[152:155], v1, s[36:39], s44 offen offset:0 sc1\n"
"v_mfma_f32_16x16x32_bf16 v[28:31], v[164:167], a[48:51], v[28:31]\n"
"v_add3_u32 v68, v68, v64, v65\n"
"v_add3_u32 v68, v68, v66, v67\n"
"v_mfma_f32_16x16x32_bf16 v[32:35], v[160:163], a[36:39], v[32:35]\n"
"buffer_store_dwordx4 v[60:63], v1, s[36:39], s27 offen offset:0 sc1\n"
"v_mfma_f32_16x16x32_bf16 v[32:35], v[164:167], a[52:55], v[32:35]\n"
"buffer_store_dwordx4 v[64:67], v1, s[36:39], s27 offen offset:1024 sc1\n"
"v_mfma_f32_16x16x32_bf16 v[36:39], v[160:163], a[40:43], v[36:39]\n"
"buffer_load_dwordx4 v[156:159], v1, s[36:39], s44 offen offset:1024 sc1\n"
"v_mfma_f32_16x16x32_bf16 v[36:39], v[164:167], a[56:59], v[36:39]\n"
"buffer_store_dword v68, v8, s[36:39], s27 offen offset:2048 sc1\n"
"v_mfma_f32_16x16x32_bf16 v[40:43], v[160:163], a[44:47], v[40:43]\n"
"buffer_load_dwordx4 v[96:99], v1, s[36:39], s56 offen offset:0 sc1\n"
"v_mfma_f32_16x16x32_bf16 v[40:43], v[164:167], a[60:63], v[40:43]\n"
"s_waitcnt vmcnt(35)\n"
"buffer_load_dwordx4 v[100:103], v1, s[36:39], s56 offen offset:1024 sc1\n"
"v_mfma_f32_16x16x32_bf16 v[28:31], v[168:171], a[64:67], v[28:31]\n"
"s_add_u32 s44, s88, 12288\n"
"buffer_load_dwordx4 v[160:163], v1, s[36:39], s44 offen offset:0 sc1\n"
"v_mfma_f32_16x16x32_bf16 v[28:31], v[172:175], a[80:83], v[28:31]\n"
"buffer_load_dword v104, v8, s[36:39], s56 offen offset:2048 sc1\n"
"v_mfma_f32_16x16x32_bf16 v[32:35], v[168:171], a[68:71], v[32:35]\n"
"v_mfma_f32_16x16x32_bf16 v[32:35], v[172:175], a[84:87], v[32:35]\n"
"v_mfma_f32_16x16x32_bf16 v[36:39], v[168:171], a[72:75], v[36:39]\n"
"buffer_load_dwordx4 v[164:167], v1, s[36:39], s44 offen offset:1024 sc1\n"
"v_mfma_f32_16x16x32_bf16 v[36:39], v[172:175], a[88:91], v[36:39]\n"
"v_mfma_f32_16x16x32_bf16 v[40:43], v[168:171], a[76:79], v[40:43]\n"
"v_mfma_f32_16x16x32_bf16 v[40:43], v[172:175], a[92:95], v[40:43]\n"
"s_waitcnt vmcnt(37)\n"
"v_mfma_f32_16x16x32_bf16 v[28:31], v[176:179], a[96:99], v[28:31]\n"
"s_add_u32 s44, s88, 14336\n"
"buffer_load_dwordx4 v[168:171], v1, s[36:39], s44 offen offset:0 sc1\n"
"v_mfma_f32_16x16x32_bf16 v[28:31], v[180:183], a[112:115], v[28:31]\n"
"s_waitcnt vmcnt(26)\n"
"v_add3_u32 v106, v6, v86, v87\n"
"v_add3_u32 v106, v106, v88, v89\n"
"v_mfma_f32_16x16x32_bf16 v[32:35], v[176:179], a[100:103], v[32:35]\n"
"v_add3_u32 v107, v6, v90, v91\n"
"v_add3_u32 v107, v107, v92, v93\n"
"v_mfma_f32_16x16x32_bf16 v[32:35], v[180:183], a[116:119], v[32:35]\n"
"v_cmp_ne_u64 vcc, v[106:107], v[94:95]\n"
"v_mfma_f32_16x16x32_bf16 v[36:39], v[176:179], a[104:107], v[36:39]\n"
"buffer_load_dwordx4 v[172:175], v1, s[36:39], s44 offen offset:1024 sc1\n"
"v_mfma_f32_16x16x32_bf16 v[36:39], v[180:183], a[120:123], v[36:39]\n"
"s_cbranch_vccnz LPX0_block_kernel_slow\n"
"LPX0_block_kernel_back:\n"
"v_lshlrev_b32 v70, 16, v86\n"
"v_and_b32 v71, s45, v86\n"
"v_mfma_f32_16x16x32_bf16 v[40:43], v[176:179], a[108:111], v[40:43]\n"
"v_lshlrev_b32 v106, 16, v90\n"
"v_and_b32 v107, s45, v90\n"
"v_mfma_f32_16x16x32_bf16 v[40:43], v[180:183], a[124:127], v[40:43]\n"
"s_waitcnt vmcnt(37)\n"
"v_add_f32 v70, v70, v106\n"
"v_add_f32 v71, v71, v107\n"
"v_mfma_f32_16x16x32_bf16 v[28:31], v[184:187], a[128:131], v[28:31]\n"
"s_add_u32 s44, s88, 16384\n"
"buffer_load_dwordx4 v[176:179], v1, s[36:39], s44 offen offset:0 sc1\n"
"v_mfma_f32_16x16x32_bf16 v[28:31], v[188:191], a[144:147], v[28:31]\n"
"v_lshlrev_b32 v72, 16, v87\n"
"v_and_b32 v73, s45, v87\n"
"v_mfma_f32_16x16x32_bf16 v[32:35], v[184:187], a[132:135], v[32:35]\n"
"v_lshlrev_b32 v106, 16, v91\n"
"v_and_b32 v107, s45, v91\n"
"v_mfma_f32_16x16x32_bf16 v[32:35], v[188:191], a[148:151], v[32:35]\n"
"v_add_f32 v72, v72, v106\n"
"v_add_f32 v73, v73, v107\n"
"v_mfma_f32_16x16x32_bf16 v[36:39], v[184:187], a[136:139], v[36:39]\n"
"buffer_load_dwordx4 v[180:183], v1, s[36:39], s44 offen offset:1024 sc1\n"
"v_mfma_f32_16x16x32_bf16 v[36:39], v[188:191], a[152:155], v[36:39]\n"
"v_lshlrev_b32 v74, 16, v88\n"
"v_and_b32 v75, s45, v88\n"
"v_mfma_f32_16x16x32_bf16 v[40:43], v[184:187], a[140:143], v[40:43]\n"
"v_lshlrev_b32 v106, 16, v92\n"
"v_and_b32 v107, s45, v92\n"
"v_mfma_f32_16x16x32_bf16 v[40:43], v[188:191], a[156:159], v[40:43]\n"
"s_waitcnt vmcnt(35)\n"
"v_add_f32 v74, v74, v106\n"
"v_add_f32 v75, v75, v107\n"
"v_mfma_f32_16x16x32_bf16 v[28:31], v[192:195], a[160:163], v[28:31]\n"
"s_add_u32 s44, s89, 0\n"
"buffer_load_dwordx4 v[184:187], v1, s[36:39], s44 offen offset:0 sc1\n"
"v_mfma_f32_16x16x32_bf16 v[28:31], v[196:199], a[176:179], v[28:31]\n"
"v_lshlrev_b32 v76, 16, v89\n"
"v_and_b32 v77, s45, v89\n"
"v_mfma_f32_16x16x32_bf16 v[32:35], v[192:195], a[164:167], v[32:35]\n"
"v_lshlrev_b32 v106, 16, v93\n"
"v_and_b32 v107, s45, v93\n"
"v_mfma_f32_16x16x32_bf16 v[32:35], v[196:199], a[180:183], v[32:35]\n"
"v_add_f32 v76, v76, v106\n"
"v_add_f32 v77, v77, v107\n"
"v_mfma_f32_16x16x32_bf16 v[36:39], v[192:195], a[168:171], v[36:39]\n"
"buffer_load_dwordx4 v[188:191], v1, s[36:39], s44 offen offset:1024 sc1\n"
"v_mfma_f32_16x16x32_bf16 v[36:39], v[196:199], a[184:187], v[36:39]\n"
"s_waitcnt lgkmcnt(0)\n"
"v_lshlrev_b32 v78, 16, v108\n"
"v_and_b32 v79, s45, v108\n"
"v_mfma_f32_16x16x32_bf16 v[40:43], v[192:195], a[172:175], v[40:43]\n"
"v_permlane32_swap_b32 v70, v74\n"
"v_mfma_f32_16x16x32_bf16 v[40:43], v[196:199], a[188:191], v[40:43]\n"
"s_waitcnt vmcnt(30)\n"
"v_permlane32_swap_b32 v71, v75\n"
"v_mfma_f32_16x16x32_bf16 v[28:31], v[200:203], a[192:195], v[28:31]\n"
"s_add_u32 s44, s89, 2048\n"
"buffer_load_dwordx4 v[192:195], v1, s[36:39], s44 offen offset:0 sc1\n"
"v_mfma_f32_16x16x32_bf16 v[28:31], v[204:207], a[208:211], v[28:31]\n"
"v_permlane32_swap_b32 v72, v76\n"
"v_mfma_f32_16x16x32_bf16 v[32:35], v[200:203], a[196:199], v[32:35]\n"
"v_permlane32_swap_b32 v73, v77\n"
"v_mfma_f32_16x16x32_bf16 v[32:35], v[204:207], a[212:215], v[32:35]\n"
"v_add_f32 v70, v70, v74\n"
"v_add_f32 v71, v71, v75\n"
"v_mfma_f32_16x16x32_bf16 v[36:39], v[200:203], a[200:203], v[36:39]\n"
"buffer_load_dwordx4 v[196:199], v1, s[36:39], s44 offen offset:1024 sc1\n"
"v_mfma_f32_16x16x32_bf16 v[36:39], v[204:207], a[216:219], v[36:39]\n"
"v_add_f32 v72, v72, v76\n"
"v_add_f32 v73, v73, v77\n"
"v_mfma_f32_16x16x32_bf16 v[40:43], v[200:203], a[204:207], v[40:43]\n"
"v_permlane16_swap_b32 v70, v72\n"
"v_mfma_f32_16x16x32_bf16 v[40:43], v[204:207], a[220:223], v[40:43]\n"
"s_waitcnt vmcnt(30)\n"
"v_permlane16_swap_b32 v71, v73\n"
"v_mfma_f32_16x16x32_bf16 v[28:31], v[208:211], a[224:227], v[28:31]\n"
"s_add_u32 s44, s89, 4096\n"
"buffer_load_dwordx4 v[200:203], v1, s[36:39], s44 offen offset:0 sc1\n"
"v_mfma_f32_16x16x32_bf16 v[28:31], v[212:215], a[240:243], v[28:31]\n"
"v_add_f32 v70, v70, v72\n"
"v_add_f32 v71, v71, v73\n"
"v_mfma_f32_16x16x32_bf16 v[32:35], v[208:211], a[228:231], v[32:35]\n"
"v_fmac_f32 v78, s47, v70\n"
"v_fmac_f32 v79, s47, v71\n"
"v_mfma_f32_16x16x32_bf16 v[32:35], v[212:215], a[244:247], v[32:35]\n"
"v_cvt_pk_bf16_f32 v78, v78, v79\n"
"ds_write_b32 v20, v78\n"
"v_mfma_f32_16x16x32_bf16 v[36:39], v[208:211], a[232:235], v[36:39]\n"
"buffer_load_dwordx4 v[204:207], v1, s[36:39], s44 offen offset:1024 sc1\n"
"v_mfma_f32_16x16x32_bf16 v[36:39], v[212:215], a[248:251], v[36:39]\n"
"v_mfma_f32_16x16x32_bf16 v[40:43], v[208:211], a[236:239], v[40:43]\n"
"v_mfma_f32_16x16x32_bf16 v[40:43], v[212:215], a[252:255], v[40:43]\n"
"s_waitcnt vmcnt(30)\n"
"v_mfma_f32_16x16x32_bf16 v[28:31], v[216:219], v[224:227], v[28:31]\n"
"s_add_u32 s44, s89, 6144\n"
"buffer_load_dwordx4 v[208:211], v1, s[36:39], s44 offen offset:0 sc1\n"
"v_mfma_f32_16x16x32_bf16 v[44:47], v[220:223], v[240:243], v[28:31]\n"
"v_mfma_f32_16x16x32_bf16 v[32:35], v[216:219], v[228:231], v[32:35]\n"
"v_mfma_f32_16x16x32_bf16 v[48:51], v[220:223], v[244:247], v[32:35]\n"
"v_mfma_f32_16x16x32_bf16 v[36:39], v[216:219], v[232:235], v[36:39]\n"
"buffer_load_dwordx4 v[212:215], v1, s[36:39], s44 offen offset:1024 sc1\n"
"v_mfma_f32_16x16x32_bf16 v[52:55], v[220:223], v[248:251], v[36:39]\n"
"v_mfma_f32_16x16x32_bf16 v[40:43], v[216:219], v[236:239], v[40:43]\n"
"v_mfma_f32_16x16x32_bf16 v[56:59], v[220:223], v[252:255], v[40:43]\n"
"s_waitcnt vmcnt(30)\n"
"v_mfma_f32_16x16x32_bf16 v[28:31], v[112:115], a[0:3], 0\n"
"s_add_u32 s44, s89, 8192\n"
"buffer_load_dwordx4 v[216:219], v1, s[36:39], s44 offen offset:0 sc1\n"
"v_mfma_f32_16x16x32_bf16 v[28:31], v[116:119], a[16:19], v[28:31]\n"
"v_mfma_f32_16x16x32_bf16 v[32:35], v[112:115], a[4:7], 0\n"
"v_cvt_pk_bf16_f32 v60, v44, v45\n"
"v_cvt_pk_bf16_f32 v61, v46, v47\n"
"v_mfma_f32_16x16x32_bf16 v[32:35], v[116:119], a[20:23], v[32:35]\n"
"v_cvt_pk_bf16_f32 v62, v48, v49\n"
"v_cvt_pk_bf16_f32 v63, v50, v51\n"
"v_mfma_f32_16x16x32_bf16 v[36:39], v[112:115], a[8:11], 0\n"
"buffer_load_dwordx4 v[220:223], v1, s[36:39], s44 offen offset:1024 sc1\n"
"v_mfma_f32_16x16x32_bf16 v[36:39], v[116:119], a[24:27], v[36:39]\n"
"v_cvt_pk_bf16_f32 v64, v52, v53\n"
"v_cvt_pk_bf16_f32 v65, v54, v55\n"
"v_mfma_f32_16x16x32_bf16 v[40:43], v[112:115], a[12:15], 0\n"
"v_cvt_pk_bf16_f32 v66, v56, v57\n"
"v_cvt_pk_bf16_f32 v67, v58, v59\n"
"v_mfma_f32_16x16x32_bf16 v[40:43], v[116:119], a[28:31], v[40:43]\n"
"s_waitcnt vmcnt(30)\n"
"v_add3_u32 v68, v6, v60, v61\n"
"v_add3_u32 v68, v68, v62, v63\n"
"v_mfma_f32_16x16x32_bf16 v[28:31], v[120:123], a[32:35], v[28:31]\n"
"s_add_u32 s44, s89, 10240\n"
"buffer_load_dwordx4 v[112:115], v1, s[36:39], s44 offen offset:0 sc1\n"
"v_mfma_f32_16x16x32_bf16 v[28:31], v[124:127], a[48:51], v[28:31]\n"
"v_add3_u32 v68, v68, v64, v65\n"
"v_add3_u32 v68, v68, v66, v67\n"
"v_mfma_f32_16x16x32_bf16 v[32:35], v[120:123], a[36:39], v[32:35]\n"
"buffer_store_dwordx4 v[60:63], v1, s[36:39], s28 offen offset:0 sc1\n"
"v_mfma_f32_16x16x32_bf16 v[32:35], v[124:127], a[52:55], v[32:35]\n"
"buffer_store_dwordx4 v[64:67], v1, s[36:39], s28 offen offset:1024 sc1\n"
"v_mfma_f32_16x16x32_bf16 v[36:39], v[120:123], a[40:43], v[36:39]\n"
"buffer_load_dwordx4 v[116:119], v1, s[36:39], s44 offen offset:1024 sc1\n"
"v_mfma_f32_16x16x32_bf16 v[36:39], v[124:127], a[56:59], v[36:39]\n"
"buffer_store_dword v68, v8, s[36:39], s28 offen offset:2048 sc1\n"
"v_mfma_f32_16x16x32_bf16 v[40:43], v[120:123], a[44:47], v[40:43]\n"
"buffer_load_dwordx4 v[86:89], v1, s[36:39], s57 offen offset:0 sc1\n"
"v_mfma_f32_16x16x32_bf16 v[40:43], v[124:127], a[60:63], v[40:43]\n"
"s_waitcnt vmcnt(34)\n"
"buffer_load_dwordx4 v[90:93], v1, s[36:39], s57 offen offset:1024 sc1\n"
"v_mfma_f32_16x16x32_bf16 v[28:31], v[128:131], a[64:67], v[28:31]\n"
"s_add_u32 s44, s89, 12288\n"
"buffer_load_dwordx4 v[120:123], v1, s[36:39], s44 offen offset:0 sc1\n"
"v_mfma_f32_16x16x32_bf16 v[28:31], v[132:135], a[80:83], v[28:31]\n"
"buffer_load_dword v94, v8, s[36:39], s57 offen offset:2048 sc1\n"
"v_mfma_f32_16x16x32_bf16 v[32:35], v[128:131], a[68:71], v[32:35]\n"
"v_mfma_f32_16x16x32_bf16 v[32:35], v[132:135], a[84:87], v[32:35]\n"
"v_mfma_f32_16x16x32_bf16 v[36:39], v[128:131], a[72:75], v[36:39]\n"
"buffer_load_dwordx4 v[124:127], v1, s[36:39], s44 offen offset:1024 sc1\n"
"v_mfma_f32_16x16x32_bf16 v[36:39], v[132:135], a[88:91], v[36:39]\n"
"v_mfma_f32_16x16x32_bf16 v[40:43], v[128:131], a[76:79], v[40:43]\n"
"v_mfma_f32_16x16x32_bf16 v[40:43], v[132:135], a[92:95], v[40:43]\n"
"s_waitcnt vmcnt(36)\n"
"v_mfma_f32_16x16x32_bf16 v[28:31], v[136:139], a[96:99], v[28:31]\n"
"s_add_u32 s44, s89, 14336\n"
"buffer_load_dwordx4 v[128:131], v1, s[36:39], s44 offen offset:0 sc1\n"
"v_mfma_f32_16x16x32_bf16 v[28:31], v[140:143], a[112:115], v[28:31]\n"
"s_waitcnt vmcnt(26)\n"
"v_add3_u32 v4, v6, v96, v97\n"
"v_add3_u32 v4, v4, v98, v99\n"
"v_mfma_f32_16x16x32_bf16 v[32:35], v[136:139], a[100:103], v[32:35]\n"
"v_add3_u32 v4, v4, v100, v101\n"
"v_add3_u32 v4, v4, v102, v103\n"
"v_mfma_f32_16x16x32_bf16 v[32:35], v[140:143], a[116:119], v[32:35]\n"
"v_cmp_ne_u32 vcc, v4, v104\n"
"v_mfma_f32_16x16x32_bf16 v[36:39], v[136:139], a[104:107], v[36:39]\n"
"buffer_load_dwordx4 v[132:135], v1, s[36:39], s44 offen offset:1024 sc1\n"
"v_mfma_f32_16x16x32_bf16 v[36:39], v[140:143], a[120:123], v[36:39]\n"
"s_cbranch_vccnz LPX1_block_kernel_slow\n"
"LPX1_block_kernel_back:\n"
"v_lshlrev_b32 v70, 16, v96\n"
"v_and_b32 v71, s45, v96\n"
"v_mfma_f32_16x16x32_bf16 v[40:43], v[136:139], a[108:111], v[40:43]\n"
"v_lshlrev_b32 v72, 16, v97\n"
"v_and_b32 v73, s45, v97\n"
"v_mfma_f32_16x16x32_bf16 v[40:43], v[140:143], a[124:127], v[40:43]\n"
"s_waitcnt vmcnt(36)\n"
"v_lshlrev_b32 v74, 16, v98\n"
"v_and_b32 v75, s45, v98\n"
"v_mfma_f32_16x16x32_bf16 v[28:31], v[144:147], a[128:131], v[28:31]\n"
"s_add_u32 s44, s89, 16384\n"
"buffer_load_dwordx4 v[136:139], v1, s[36:39], s44 offen offset:0 sc1\n"
"v_mfma_f32_16x16x32_bf16 v[28:31], v[148:151], a[144:147], v[28:31]\n"
"v_lshlrev_b32 v76, 16, v99\n"
"v_and_b32 v77, s45, v99\n"
"v_mfma_f32_16x16x32_bf16 v[32:35], v[144:147], a[132:135], v[32:35]\n"
"v_lshlrev_b32 v78, 16, v100\n"
"v_and_b32 v79, s45, v100\n"
"v_mfma_f32_16x16x32_bf16 v[32:35], v[148:151], a[148:151], v[32:35]\n"
"v_lshlrev_b32 v80, 16, v101\n"
"v_and_b32 v81, s45, v101\n"
"v_mfma_f32_16x16x32_bf16 v[36:39], v[144:147], a[136:139], v[36:39]\n"
"buffer_load_dwordx4 v[140:143], v1, s[36:39], s44 offen offset:1024 sc1\n"
"v_mfma_f32_16x16x32_bf16 v[36:39], v[148:151], a[152:155], v[36:39]\n"
"v_lshlrev_b32 v82, 16, v102\n"
"v_and_b32 v83, s45, v102\n"
"v_mfma_f32_16x16x32_bf16 v[40:43], v[144:147], a[140:143], v[40:43]\n"
"v_lshlrev_b32 v84, 16, v103\n"
"v_and_b32 v85, s45, v103\n"
"v_mfma_f32_16x16x32_bf16 v[40:43], v[148:151], a[156:159], v[40:43]\n"
"s_waitcnt vmcnt(34)\n"
"v_mfma_f32_16x16x32_bf16 v[28:31], v[152:155], a[160:163], v[28:31]\n"
"s_add_u32 s44, s90, 0\n"
"buffer_load_dwordx4 v[144:147], v1, s[36:39], s44 offen offset:0 sc1\n"
"v_mfma_f32_16x16x32_bf16 v[28:31], v[156:159], a[176:179], v[28:31]\n"
"v_mfma_f32_16x16x32_bf16 v[32:35], v[152:155], a[164:167], v[32:35]\n"
"v_mfma_f32_16x16x32_bf16 v[32:35], v[156:159], a[180:183], v[32:35]\n"
"v_mfma_f32_16x16x32_bf16 v[36:39], v[152:155], a[168:171], v[36:39]\n"
"buffer_load_dwordx4 v[148:151], v1, s[36:39], s44 offen offset:1024 sc1\n"
"v_mfma_f32_16x16x32_bf16 v[36:39], v[156:159], a[184:187], v[36:39]\n"
"v_mfma_f32_16x16x32_bf16 v[40:43], v[152:155], a[172:175], v[40:43]\n"
"v_mfma_f32_16x16x32_bf16 v[40:43], v[156:159], a[188:191], v[40:43]\n"
"s_waitcnt vmcnt(30)\n"
"v_mfma_f32_16x16x32_bf16 v[28:31], v[160:163], a[192:195], v[28:31]\n"
"s_add_u32 s44, s90, 2048\n"
"buffer_load_dwordx4 v[152:155], v1, s[36:39], s44 offen offset:0 sc1\n"
"v_mfma_f32_16x16x32_bf16 v[28:31], v[164:167], a[208:211], v[28:31]\n"
"v_mfma_f32_16x16x32_bf16 v[32:35], v[160:163], a[196:199], v[32:35]\n"
"v_mfma_f32_16x16x32_bf16 v[32:35], v[164:167], a[212:215], v[32:35]\n"
"v_mfma_f32_16x16x32_bf16 v[36:39], v[160:163], a[200:203], v[36:39]\n"
"buffer_load_dwordx4 v[156:159], v1, s[36:39], s44 offen offset:1024 sc1\n"
"v_mfma_f32_16x16x32_bf16 v[36:39], v[164:167], a[216:219], v[36:39]\n"
"v_mfma_f32_16x16x32_bf16 v[40:43], v[160:163], a[204:207], v[40:43]\n"
"v_mfma_f32_16x16x32_bf16 v[40:43], v[164:167], a[220:223], v[40:43]\n"
"s_waitcnt vmcnt(30)\n"
"v_mfma_f32_16x16x32_bf16 v[28:31], v[168:171], a[224:227], v[28:31]\n"
"s_add_u32 s44, s90, 4096\n"
"buffer_load_dwordx4 v[160:163], v1, s[36:39], s44 offen offset:0 sc1\n"
"v_mfma_f32_16x16x32_bf16 v[28:31], v[172:175], a[240:243], v[28:31]\n"
"v_mfma_f32_16x16x32_bf16 v[32:35], v[168:171], a[228:231], v[32:35]\n"
"v_mfma_f32_16x16x32_bf16 v[32:35], v[172:175], a[244:247], v[32:35]\n"
"v_mfma_f32_16x16x32_bf16 v[36:39], v[168:171], a[232:235], v[36:39]\n"
"buffer_load_dwordx4 v[164:167], v1, s[36:39], s44 offen offset:1024 sc1\n"
"v_mfma_f32_16x16x32_bf16 v[36:39], v[172:175], a[248:251], v[36:39]\n"
"v_mfma_f32_16x16x32_bf16 v[40:43], v[168:171], a[236:239], v[40:43]\n"
"v_mfma_f32_16x16x32_bf16 v[40:43], v[172:175], a[252:255], v[40:43]\n"
"s_waitcnt vmcnt(30)\n"
"v_mfma_f32_16x16x32_bf16 v[28:31], v[176:179], v[224:227], v[28:31]\n"
"s_add_u32 s44, s90, 6144\n"
"buffer_load_dwordx4 v[168:171], v1, s[36:39], s44 offen offset:0 sc1\n"
"v_mfma_f32_16x16x32_bf16 v[44:47], v[180:183], v[240:243], v[28:31]\n"
"v_mfma_f32_16x16x32_bf16 v[32:35], v[176:179], v[228:231], v[32:35]\n"
"v_mfma_f32_16x16x32_bf16 v[48:51], v[180:183], v[244:247], v[32:35]\n"
"v_mfma_f32_16x16x32_bf16 v[36:39], v[176:179], v[232:235], v[36:39]\n"
"buffer_load_dwordx4 v[172:175], v1, s[36:39], s44 offen offset:1024 sc1\n"
"v_mfma_f32_16x16x32_bf16 v[52:55], v[180:183], v[248:251], v[36:39]\n"
"v_mfma_f32_16x16x32_bf16 v[40:43], v[176:179], v[236:239], v[40:43]\n"
"v_mfma_f32_16x16x32_bf16 v[56:59], v[180:183], v[252:255], v[40:43]\n"
"s_waitcnt vmcnt(30)\n"
"v_mfma_f32_16x16x32_bf16 v[28:31], v[184:187], a[0:3], 0\n"
"s_add_u32 s44, s90, 8192\n"
"buffer_load_dwordx4 v[176:179], v1, s[36:39], s44 offen offset:0 sc1\n"
"v_mfma_f32_16x16x32_bf16 v[28:31], v[188:191], a[16:19], v[28:31]\n"
"v_mfma_f32_16x16x32_bf16 v[32:35], v[184:187], a[4:7], 0\n"
"v_cvt_pk_bf16_f32 v60, v44, v45\n"
"v_cvt_pk_bf16_f32 v61, v46, v47\n"
"v_mfma_f32_16x16x32_bf16 v[32:35], v[188:191], a[20:23], v[32:35]\n"
"v_cvt_pk_bf16_f32 v62, v48, v49\n"
"v_cvt_pk_bf16_f32 v63, v50, v51\n"
"v_mfma_f32_16x16x32_bf16 v[36:39], v[184:187], a[8:11], 0\n"
"buffer_load_dwordx4 v[180:183], v1, s[36:39], s44 offen offset:1024 sc1\n"
"v_mfma_f32_16x16x32_bf16 v[36:39], v[188:191], a[24:27], v[36:39]\n"
"v_cvt_pk_bf16_f32 v64, v52, v53\n"
"v_cvt_pk_bf16_f32 v65, v54, v55\n"
"v_mfma_f32_16x16x32_bf16 v[40:43], v[184:187], a[12:15], 0\n"
"v_cvt_pk_bf16_f32 v66, v56, v57\n"
"v_cvt_pk_bf16_f32 v67, v58, v59\n"
"v_mfma_f32_16x16x32_bf16 v[40:43], v[188:191], a[28:31], v[40:43]\n"
"s_waitcnt vmcnt(30)\n"
"v_add3_u32 v68, v6, v60, v61\n"
"v_add3_u32 v68, v68, v62, v63\n"
"v_mfma_f32_16x16x32_bf16 v[28:31], v[192:195], a[32:35], v[28:31]\n"
"s_add_u32 s44, s90, 10240\n"
"buffer_load_dwordx4 v[184:187], v1, s[36:39], s44 offen offset:0 sc1\n"
"v_mfma_f32_16x16x32_bf16 v[28:31], v[196:199], a[48:51], v[28:31]\n"
"v_add3_u32 v68, v68, v64, v65\n"
"v_add3_u32 v68, v68, v66, v67\n"
"v_mfma_f32_16x16x32_bf16 v[32:35], v[192:195], a[36:39], v[32:35]\n"
"buffer_store_dwordx4 v[60:63], v1, s[36:39], s29 offen offset:0 sc1\n"
"v_mfma_f32_16x16x32_bf16 v[32:35], v[196:199], a[52:55], v[32:35]\n"
"buffer_store_dwordx4 v[64:67], v1, s[36:39], s29 offen offset:1024 sc1\n"
"v_mfma_f32_16x16x32_bf16 v[36:39], v[192:195], a[40:43], v[36:39]\n"
"buffer_load_dwordx4 v[188:191], v1, s[36:39], s44 offen offset:1024 sc1\n"
"v_mfma_f32_16x16x32_bf16 v[36:39], v[196:199], a[56:59], v[36:39]\n"
"buffer_store_dword v68, v8, s[36:39], s29 offen offset:2048 sc1\n"
"v_mfma_f32_16x16x32_bf16 v[40:43], v[192:195], a[44:47], v[40:43]\n"
"buffer_load_dwordx4 v[96:99], v1, s[36:39], s58 offen offset:0 sc1\n"
"v_mfma_f32_16x16x32_bf16 v[40:43], v[196:199], a[60:63], v[40:43]\n"
"s_waitcnt vmcnt(34)\n"
"buffer_load_dwordx4 v[100:103], v1, s[36:39], s58 offen offset:1024 sc1\n"
"v_mfma_f32_16x16x32_bf16 v[28:31], v[200:203], a[64:67], v[28:31]\n"
"s_add_u32 s44, s90, 12288\n"
"buffer_load_dwordx4 v[192:195], v1, s[36:39], s44 offen offset:0 sc1\n"
"v_mfma_f32_16x16x32_bf16 v[28:31], v[204:207], a[80:83], v[28:31]\n"
"buffer_load_dword v104, v8, s[36:39], s58 offen offset:2048 sc1\n"
"v_mfma_f32_16x16x32_bf16 v[32:35], v[200:203], a[68:71], v[32:35]\n"
"v_mfma_f32_16x16x32_bf16 v[32:35], v[204:207], a[84:87], v[32:35]\n"
"v_mfma_f32_16x16x32_bf16 v[36:39], v[200:203], a[72:75], v[36:39]\n"
"buffer_load_dwordx4 v[196:199], v1, s[36:39], s44 offen offset:1024 sc1\n"
"v_mfma_f32_16x16x32_bf16 v[36:39], v[204:207], a[88:91], v[36:39]\n"
"v_mfma_f32_16x16x32_bf16 v[40:43], v[200:203], a[76:79], v[40:43]\n"
"v_mfma_f32_16x16x32_bf16 v[40:43], v[204:207], a[92:95], v[40:43]\n"
"s_waitcnt vmcnt(36)\n"
"v_mfma_f32_16x16x32_bf16 v[28:31], v[208:211], a[96:99], v[28:31]\n"
"s_add_u32 s44, s90, 14336\n"
"buffer_load_dwordx4 v[200:203], v1, s[36:39], s44 offen offset:0 sc1\n"
"v_mfma_f32_16x16x32_bf16 v[28:31], v[212:215], a[112:115], v[28:31]\n"
"s_waitcnt vmcnt(26)\n"
"v_add3_u32 v4, v6, v86, v87\n"
"v_add3_u32 v4, v4, v88, v89\n"
"v_mfma_f32_16x16x32_bf16 v[32:35], v[208:211], a[100:103], v[32:35]\n"
"v_add3_u32 v4, v4, v90, v91\n"
"v_add3_u32 v4, v4, v92, v93\n"
"v_mfma_f32_16x16x32_bf16 v[32:35], v[212:215], a[116:119], v[32:35]\n"
"v_cmp_ne_u32 vcc, v4, v94\n"
"v_mfma_f32_16x16x32_bf16 v[36:39], v[208:211], a[104:107], v[36:39]\n"
"buffer_load_dwordx4 v[204:207], v1, s[36:39], s44 offen offset:1024 sc1\n"
"v_mfma_f32_16x16x32_bf16 v[36:39], v[212:215], a[120:123], v[36:39]\n"
"s_cbranch_vccnz LPX2_block_kernel_slow\n"
"LPX2_block_kernel_back:\n"
"v_lshlrev_b32 v106, 16, v86\n"
"v_and_b32 v107, s45, v86\n"
"v_mfma_f32_16x16x32_bf16 v[40:43], v[208:211], a[108:111], v[40:43]\n"
"v_add_f32 v70, v70, v106\n"
"v_add_f32 v71, v71, v107\n"
"v_mfma_f32_16x16x32_bf16 v[40:43], v[212:215], a[124:127], v[40:43]\n"
"s_waitcnt vmcnt(36)\n"
"v_lshlrev_b32 v106, 16, v87\n"
"v_and_b32 v107, s45, v87\n"
"v_mfma_f32_16x16x32_bf16 v[28:31], v[216:219], a[128:131], v[28:31]\n"
"s_add_u32 s44, s90, 16384\n"
"buffer_load_dwordx4 v[208:211], v1, s[36:39], s44 offen offset:0 sc1\n"
"v_mfma_f32_16x16x32_bf16 v[28:31], v[220:223], a[144:147], v[28:31]\n"
"v_add_f32 v72, v72, v106\n"
"v_add_f32 v73, v73, v107\n"
"v_mfma_f32_16x16x32_bf16 v[32:35], v[216:219], a[132:135], v[32:35]\n"
"v_lshlrev_b32 v106, 16, v88\n"
"v_and_b32 v107, s45, v88\n"
"v_mfma_f32_16x16x32_bf16 v[32:35], v[220:223], a[148:151], v[32:35]\n"
"v_add_f32 v74, v74, v106\n"
"v_add_f32 v75, v75, v107\n"
"v_mfma_f32_16x16x32_bf16 v[36:39], v[216:219], a[136:139], v[36:39]\n"
"buffer_load_dwordx4 v[212:215], v1, s[36:39], s44 offen offset:1024 sc1\n"
"v_mfma_f32_16x16x32_bf16 v[36:39], v[220:223], a[152:155], v[36:39]\n"
"v_lshlrev_b32 v106, 16, v89\n"
"v_and_b32 v107, s45, v89\n"
"v_mfma_f32_16x16x32_bf16 v[40:43], v[216:219], a[140:143], v[40:43]\n"
"v_add_f32 v76, v76, v106\n"
"v_add_f32 v77, v77, v107\n"
"v_mfma_f32_16x16x32_bf16 v[40:43], v[220:223], a[156:159], v[40:43]\n"
"s_waitcnt vmcnt(34)\n"
"v_lshlrev_b32 v106, 16, v90\n"
"v_and_b32 v107, s45, v90\n"
"v_mfma_f32_16x16x32_bf16 v[28:31], v[112:115], a[160:163], v[28:31]\n"
"s_add_u32 s44, s91, 0\n"
"buffer_load_dwordx4 v[216:219], v1, s[36:39], s44 offen offset:0 sc1\n"
"v_mfma_f32_16x16x32_bf16 v[28:31], v[116:119], a[176:179], v[28:31]\n"
"v_add_f32 v78, v78, v106\n"
"v_add_f32 v79, v79, v107\n"
"v_mfma_f32_16x16x32_bf16 v[32:35], v[112:115], a[164:167], v[32:35]\n"
"v_lshlrev_b32 v106, 16, v91\n"
"v_and_b32 v107, s45, v91\n"
"v_mfma_f32_16x16x32_bf16 v[32:35], v[116:119], a[180:183], v[32:35]\n"
"v_add_f32 v80, v80, v106\n"
"v_add_f32 v81, v81, v107\n"
"v_mfma_f32_16x16x32_bf16 v[36:39], v[112:115], a[168:171], v[36:39]\n"
"buffer_load_dwordx4 v[220:223], v1, s[36:39], s44 offen offset:1024 sc1\n"
"v_mfma_f32_16x16x32_bf16 v[36:39], v[116:119], a[184:187], v[36:39]\n"
"v_lshlrev_b32 v106, 16, v92\n"
"v_and_b32 v107, s45, v92\n"
"v_mfma_f32_16x16x32_bf16 v[40:43], v[112:115], a[172:175], v[40:43]\n"
"v_add_f32 v82, v82, v106\n"
"v_add_f32 v83, v83, v107\n"
"v_mfma_f32_16x16x32_bf16 v[40:43], v[116:119], a[188:191], v[40:43]\n"
"s_waitcnt vmcnt(30)\n"
"v_lshlrev_b32 v106, 16, v93\n"
"v_and_b32 v107, s45, v93\n"
"v_mfma_f32_16x16x32_bf16 v[28:31], v[120:123], a[192:195], v[28:31]\n"
"s_add_u32 s44, s91, 2048\n"
"buffer_load_dwordx4 v[112:115], v1, s[36:39], s44 offen offset:0 sc1\n"
"v_mfma_f32_16x16x32_bf16 v[28:31], v[124:127], a[208:211], v[28:31]\n"
"v_add_f32 v84, v84, v106\n"
"v_add_f32 v85, v85, v107\n"
"v_mfma_f32_16x16x32_bf16 v[32:35], v[120:123], a[196:199], v[32:35]\n"
"v_mfma_f32_16x16x32_bf16 v[32:35], v[124:127], a[212:215], v[32:35]\n"
"v_mfma_f32_16x16x32_bf16 v[36:39], v[120:123], a[200:203], v[36:39]\n"
"buffer_load_dwordx4 v[116:119], v1, s[36:39], s44 offen offset:1024 sc1\n"
"v_mfma_f32_16x16x32_bf16 v[36:39], v[124:127], a[216:219], v[36:39]\n"
"v_mfma_f32_16x16x32_bf16 v[40:43], v[120:123], a[204:207], v[40:43]\n"
"v_mfma_f32_16x16x32_bf16 v[40:43], v[124:127], a[220:223], v[40:43]\n"
"s_waitcnt vmcnt(30)\n"
"v_mfma_f32_16x16x32_bf16 v[28:31], v[128:131], a[224:227], v[28:31]\n"
"s_add_u32 s44, s91, 4096\n"
"buffer_load_dwordx4 v[120:123], v1, s[36:39], s44 offen offset:0 sc1\n"
"v_mfma_f32_16x16x32_bf16 v[28:31], v[132:135], a[240:243], v[28:31]\n"
"v_mfma_f32_16x16x32_bf16 v[32:35], v[128:131], a[228:231], v[32:35]\n"
"v_mfma_f32_16x16x32_bf16 v[32:35], v[132:135], a[244:247], v[32:35]\n"
"v_mfma_f32_16x16x32_bf16 v[36:39], v[128:131], a[232:235], v[36:39]\n"
"buffer_load_dwordx4 v[124:127], v1, s[36:39], s44 offen offset:1024 sc1\n"
"v_mfma_f32_16x16x32_bf16 v[36:39], v[132:135], a[248:251], v[36:39]\n"
"v_mfma_f32_16x16x32_bf16 v[40:43], v[128:131], a[236:239], v[40:43]\n"
"v_mfma_f32_16x16x32_bf16 v[40:43], v[132:135], a[252:255], v[40:43]\n"
"s_waitcnt vmcnt(30)\n"
"v_mfma_f32_16x16x32_bf16 v[28:31], v[136:139], v[224:227], v[28:31]\n"
"s_add_u32 s44, s91, 6144\n"
"buffer_load_dwordx4 v[128:131], v1, s[36:39], s44 offen offset:0 sc1\n"
"v_mfma_f32_16x16x32_bf16 v[44:47], v[140:143], v[240:243], v[28:31]\n"
"v_mfma_f32_16x16x32_bf16 v[32:35], v[136:139], v[228:231], v[32:35]\n"
"v_mfma_f32_16x16x32_bf16 v[48:51], v[140:143], v[244:247], v[32:35]\n"
"v_mfma_f32_16x16x32_bf16 v[36:39], v[136:139], v[232:235], v[36:39]\n"
"buffer_load_dwordx4 v[132:135], v1, s[36:39], s44 offen offset:1024 sc1\n"
"v_mfma_f32_16x16x32_bf16 v[52:55], v[140:143], v[248:251], v[36:39]\n"
"v_mfma_f32_16x16x32_bf16 v[40:43], v[136:139], v[236:239], v[40:43]\n"
"v_mfma_f32_16x16x32_bf16 v[56:59], v[140:143], v[252:255], v[40:43]\n"
"s_waitcnt vmcnt(30)\n"
"v_mfma_f32_16x16x32_bf16 v[28:31], v[144:147], a[0:3], 0\n"
"s_add_u32 s44, s91, 8192\n"
"buffer_load_dwordx4 v[136:139], v1, s[36:39], s44 offen offset:0 sc1\n"
"v_mfma_f32_16x16x32_bf16 v[28:31], v[148:151], a[16:19], v[28:31]\n"
"v_mfma_f32_16x16x32_bf16 v[32:35], v[144:147], a[4:7], 0\n"
"v_cvt_pk_bf16_f32 v60, v44, v45\n"
"v_cvt_pk_bf16_f32 v61, v46, v47\n"
"v_mfma_f32_16x16x32_bf16 v[32:35], v[148:151], a[20:23], v[32:35]\n"
"v_cvt_pk_bf16_f32 v62, v48, v49\n"
"v_cvt_pk_bf16_f32 v63, v50, v51\n"
"v_mfma_f32_16x16x32_bf16 v[36:39], v[144:147], a[8:11], 0\n"
"buffer_load_dwordx4 v[140:143], v1, s[36:39], s44 offen offset:1024 sc1\n"
"v_mfma_f32_16x16x32_bf16 v[36:39], v[148:151], a[24:27], v[36:39]\n"
"v_cvt_pk_bf16_f32 v64, v52, v53\n"
"v_cvt_pk_bf16_f32 v65, v54, v55\n"
"v_mfma_f32_16x16x32_bf16 v[40:43], v[144:147], a[12:15], 0\n"
"v_cvt_pk_bf16_f32 v66, v56, v57\n"
"v_cvt_pk_bf16_f32 v67, v58, v59\n"
"v_mfma_f32_16x16x32_bf16 v[40:43], v[148:151], a[28:31], v[40:43]\n"
"s_waitcnt vmcnt(30)\n"
"v_add3_u32 v68, v6, v60, v61\n"
"v_add3_u32 v68, v68, v62, v63\n"
"v_mfma_f32_16x16x32_bf16 v[28:31], v[152:155], a[32:35], v[28:31]\n"
"s_add_u32 s44, s91, 10240\n"
"buffer_load_dwordx4 v[144:147], v1, s[36:39], s44 offen offset:0 sc1\n"
"v_mfma_f32_16x16x32_bf16 v[28:31], v[156:159], a[48:51], v[28:31]\n"
"v_add3_u32 v68, v68, v64, v65\n"
"v_add3_u32 v68, v68, v66, v67\n"
"v_mfma_f32_16x16x32_bf16 v[32:35], v[152:155], a[36:39], v[32:35]\n"
"buffer_store_dwordx4 v[60:63], v1, s[36:39], s30 offen offset:0 sc1\n"
"v_mfma_f32_16x16x32_bf16 v[32:35], v[156:159], a[52:55], v[32:35]\n"
"buffer_store_dwordx4 v[64:67], v1, s[36:39], s30 offen offset:1024 sc1\n"
"v_mfma_f32_16x16x32_bf16 v[36:39], v[152:155], a[40:43], v[36:39]\n"
"buffer_load_dwordx4 v[148:151], v1, s[36:39], s44 offen offset:1024 sc1\n"
"v_mfma_f32_16x16x32_bf16 v[36:39], v[156:159], a[56:59], v[36:39]\n"
"buffer_store_dword v68, v8, s[36:39], s30 offen offset:2048 sc1\n"
"v_mfma_f32_16x16x32_bf16 v[40:43], v[152:155], a[44:47], v[40:43]\n"
"buffer_load_dwordx4 v[86:89], v1, s[36:39], s59 offen offset:0 sc1\n"
"v_mfma_f32_16x16x32_bf16 v[40:43], v[156:159], a[60:63], v[40:43]\n"
"s_waitcnt vmcnt(34)\n"
"buffer_load_dwordx4 v[90:93], v1, s[36:39], s59 offen offset:1024 sc1\n"
"v_mfma_f32_16x16x32_bf16 v[28:31], v[160:163], a[64:67], v[28:31]\n"
"s_add_u32 s44, s91, 12288\n"
"buffer_load_dwordx4 v[152:155], v1, s[36:39], s44 offen offset:0 sc1\n"
"v_mfma_f32_16x16x32_bf16 v[28:31], v[164:167], a[80:83], v[28:31]\n"
"buffer_load_dword v94, v8, s[36:39], s59 offen offset:2048 sc1\n"
"v_mfma_f32_16x16x32_bf16 v[32:35], v[160:163], a[68:71], v[32:35]\n"
"v_mfma_f32_16x16x32_bf16 v[32:35], v[164:167], a[84:87], v[32:35]\n"
"v_mfma_f32_16x16x32_bf16 v[36:39], v[160:163], a[72:75], v[36:39]\n"
"buffer_load_dwordx4 v[156:159], v1, s[36:39], s44 offen offset:1024 sc1\n"
"v_mfma_f32_16x16x32_bf16 v[36:39], v[164:167], a[88:91], v[36:39]\n"
"v_mfma_f32_16x16x32_bf16 v[40:43], v[160:163], a[76:79], v[40:43]\n"
"v_mfma_f32_16x16x32_bf16 v[40:43], v[164:167], a[92:95], v[40:43]\n"
"s_waitcnt vmcnt(36)\n"
"v_mfma_f32_16x16x32_bf16 v[28:31], v[168:171], a[96:99], v[28:31]\n"
"s_add_u32 s44, s91, 14336\n"
"buffer_load_dwordx4 v[160:163], v1, s[36:39], s44 offen offset:0 sc1\n"
"v_mfma_f32_16x16x32_bf16 v[28:31], v[172:175], a[112:115], v[28:31]\n"
"s_waitcnt vmcnt(26)\n"
"v_add3_u32 v4, v6, v96, v97\n"
"v_add3_u32 v4, v4, v98, v99\n"
"v_mfma_f32_16x16x32_bf16 v[32:35], v[168:171], a[100:103], v[32:35]\n"
"v_add3_u32 v4, v4, v100, v101\n"
"v_add3_u32 v4, v4, v102, v103\n"
"v_mfma_f32_16x16x32_bf16 v[32:35], v[172:175], a[116:119], v[32:35]\n"
"v_cmp_ne_u32 vcc, v4, v104\n"
"v_mfma_f32_16x16x32_bf16 v[36:39], v[168:171], a[104:107], v[36:39]\n"
"buffer_load_dwordx4 v[164:167], v1, s[36:39], s44 offen offset:1024 sc1\n"
"v_mfma_f32_16x16x32_bf16 v[36:39], v[172:175], a[120:123], v[36:39]\n"
"s_cbranch_vccnz LPX3_block_kernel_slow\n"
"LPX3_block_kernel_back:\n"
"v_lshlrev_b32 v106, 16, v96\n"
"v_and_b32 v107, s45, v96\n"
"v_mfma_f32_16x16x32_bf16 v[40:43], v[168:171], a[108:111], v[40:43]\n"
"v_add_f32 v70, v70, v106\n"
"v_add_f32 v71, v71, v107\n"
"v_mfma_f32_16x16x32_bf16 v[40:43], v[172:175], a[124:127], v[40:43]\n"
"s_waitcnt vmcnt(36)\n"
"v_lshlrev_b32 v106, 16, v97\n"
"v_and_b32 v107, s45, v97\n"
"v_mfma_f32_16x16x32_bf16 v[28:31], v[176:179], a[128:131], v[28:31]\n"
"s_add_u32 s44, s91, 16384\n"
"buffer_load_dwordx4 v[168:171], v1, s[36:39], s44 offen offset:0 sc1\n"
"v_mfma_f32_16x16x32_bf16 v[28:31], v[180:183], a[144:147], v[28:31]\n"
"v_add_f32 v72, v72, v106\n"
"v_add_f32 v73, v73, v107\n"
"v_mfma_f32_16x16x32_bf16 v[32:35], v[176:179], a[132:135], v[32:35]\n"
"v_lshlrev_b32 v106, 16, v98\n"
"v_and_b32 v107, s45, v98\n"
"v_mfma_f32_16x16x32_bf16 v[32:35], v[180:183], a[148:151], v[32:35]\n"
"v_add_f32 v74, v74, v106\n"
"v_add_f32 v75, v75, v107\n"
"v_mfma_f32_16x16x32_bf16 v[36:39], v[176:179], a[136:139], v[36:39]\n"
"buffer_load_dwordx4 v[172:175], v1, s[36:39], s44 offen offset:1024 sc1\n"
"v_mfma_f32_16x16x32_bf16 v[36:39], v[180:183], a[152:155], v[36:39]\n"
"v_lshlrev_b32 v106, 16, v99\n"
"v_and_b32 v107, s45, v99\n"
"v_mfma_f32_16x16x32_bf16 v[40:43], v[176:179], a[140:143], v[40:43]\n"
"v_add_f32 v76, v76, v106\n"
"v_add_f32 v77, v77, v107\n"
"v_mfma_f32_16x16x32_bf16 v[40:43], v[180:183], a[156:159], v[40:43]\n"
"s_waitcnt vmcnt(34)\n"
"v_lshlrev_b32 v106, 16, v100\n"
"v_and_b32 v107, s45, v100\n"
"v_mfma_f32_16x16x32_bf16 v[28:31], v[184:187], a[160:163], v[28:31]\n"
"v_add_f32 v78, v78, v106\n"
"v_add_f32 v79, v79, v107\n"
"v_mfma_f32_16x16x32_bf16 v[28:31], v[188:191], a[176:179], v[28:31]\n"
"v_lshlrev_b32 v106, 16, v101\n"
"v_and_b32 v107, s45, v101\n"
"v_mfma_f32_16x16x32_bf16 v[32:35], v[184:187], a[164:167], v[32:35]\n"
"v_add_f32 v80, v80, v106\n"
"v_add_f32 v81, v81, v107\n"
"v_mfma_f32_16x16x32_bf16 v[32:35], v[188:191], a[180:183], v[32:35]\n"
"v_lshlrev_b32 v106, 16, v102\n"
"v_and_b32 v107, s45, v102\n"
"v_mfma_f32_16x16x32_bf16 v[36:39], v[184:187], a[168:171], v[36:39]\n"
"v_add_f32 v82, v82, v106\n"
"v_add_f32 v83, v83, v107\n"
"v_mfma_f32_16x16x32_bf16 v[36:39], v[188:191], a[184:187], v[36:39]\n"
"v_lshlrev_b32 v106, 16, v103\n"
"v_and_b32 v107, s45, v103\n"
"v_mfma_f32_16x16x32_bf16 v[40:43], v[184:187], a[172:175], v[40:43]\n"
"v_add_f32 v84, v84, v106\n"
"v_add_f32 v85, v85, v107\n"
"v_mfma_f32_16x16x32_bf16 v[40:43], v[188:191], a[188:191], v[40:43]\n"
"s_waitcnt vmcnt(28)\n"
"v_mfma_f32_16x16x32_bf16 v[28:31], v[192:195], a[192:195], v[28:31]\n"
"v_mfma_f32_16x16x32_bf16 v[28:31], v[196:199], a[208:211], v[28:31]\n"
"v_mfma_f32_16x16x32_bf16 v[32:35], v[192:195], a[196:199], v[32:35]\n"
"v_mfma_f32_16x16x32_bf16 v[32:35], v[196:199], a[212:215], v[32:35]\n"
"v_mfma_f32_16x16x32_bf16 v[36:39], v[192:195], a[200:203], v[36:39]\n"
"v_mfma_f32_16x16x32_bf16 v[36:39], v[196:199], a[216:219], v[36:39]\n"
"v_mfma_f32_16x16x32_bf16 v[40:43], v[192:195], a[204:207], v[40:43]\n"
"v_mfma_f32_16x16x32_bf16 v[40:43], v[196:199], a[220:223], v[40:43]\n"
"s_waitcnt vmcnt(26)\n"
"v_mfma_f32_16x16x32_bf16 v[28:31], v[200:203], a[224:227], v[28:31]\n"
"v_mfma_f32_16x16x32_bf16 v[28:31], v[204:207], a[240:243], v[28:31]\n"
"v_mfma_f32_16x16x32_bf16 v[32:35], v[200:203], a[228:231], v[32:35]\n"
"v_mfma_f32_16x16x32_bf16 v[32:35], v[204:207], a[244:247], v[32:35]\n"
"v_mfma_f32_16x16x32_bf16 v[36:39], v[200:203], a[232:235], v[36:39]\n"
"v_mfma_f32_16x16x32_bf16 v[36:39], v[204:207], a[248:251], v[36:39]\n"
"v_mfma_f32_16x16x32_bf16 v[40:43], v[200:203], a[236:239], v[40:43]\n"
"v_mfma_f32_16x16x32_bf16 v[40:43], v[204:207], a[252:255], v[40:43]\n"
"s_waitcnt vmcnt(24)\n"
"v_mfma_f32_16x16x32_bf16 v[28:31], v[208:211], v[224:227], v[28:31]\n"
"v_mfma_f32_16x16x32_bf16 v[44:47], v[212:215], v[240:243], v[28:31]\n"
"v_mfma_f32_16x16x32_bf16 v[32:35], v[208:211], v[228:231], v[32:35]\n"
"v_mfma_f32_16x16x32_bf16 v[48:51], v[212:215], v[244:247], v[32:35]\n"
"v_mfma_f32_16x16x32_bf16 v[36:39], v[208:211], v[232:235], v[36:39]\n"
"v_mfma_f32_16x16x32_bf16 v[52:55], v[212:215], v[248:251], v[36:39]\n"
"v_mfma_f32_16x16x32_bf16 v[40:43], v[208:211], v[236:239], v[40:43]\n"
"v_mfma_f32_16x16x32_bf16 v[56:59], v[212:215], v[252:255], v[40:43]\n"
"s_waitcnt vmcnt(22)\n"
"v_mfma_f32_16x16x32_bf16 v[28:31], v[216:219], a[0:3], 0\n"
"s_lshl_b32 s8, s6, 11\n"
"v_mfma_f32_16x16x32_bf16 v[28:31], v[220:223], a[16:19], v[28:31]\n"
"s_add_u32 s12, s8, 77824\n"
"v_mfma_f32_16x16x32_bf16 v[32:35], v[216:219], a[4:7], 0\n"
"s_lshl_b32 s8, s5, 7\n"
"v_cvt_pk_bf16_f32 v60, v44, v45\n"
"v_cvt_pk_bf16_f32 v61, v46, v47\n"
"v_mfma_f32_16x16x32_bf16 v[32:35], v[220:223], a[20:23], v[32:35]\n"
"s_add_u32 s8, s8, s63\n"
"v_cvt_pk_bf16_f32 v62, v48, v49\n"
"v_cvt_pk_bf16_f32 v63, v50, v51\n"
"v_mfma_f32_16x16x32_bf16 v[36:39], v[216:219], a[8:11], 0\n"
"s_add_u32 s9, s8, 9216\n"
"v_cvt_pk_bf16_f32 v64, v52, v53\n"
"v_cvt_pk_bf16_f32 v65, v54, v55\n"
"v_mfma_f32_16x16x32_bf16 v[36:39], v[220:223], a[24:27], v[36:39]\n"
"s_mul_i32 s10, s4, 73728\n"
"v_cvt_pk_bf16_f32 v66, v56, v57\n"
"v_cvt_pk_bf16_f32 v67, v58, v59\n"
"v_mfma_f32_16x16x32_bf16 v[40:43], v[216:219], a[12:15], 0\n"
"s_mul_i32 s11, s7, 36864\n"
"v_add3_u32 v68, v6, v60, v61\n"
"v_add3_u32 v68, v68, v62, v63\n"
"v_mfma_f32_16x16x32_bf16 v[40:43], v[220:223], a[28:31], v[40:43]\n"
"s_waitcnt vmcnt(20)\n"
"v_add3_u32 v68, v68, v64, v65\n"
"v_add3_u32 v68, v68, v66, v67\n"
"v_mfma_f32_16x16x32_bf16 v[28:31], v[112:115], a[32:35], v[28:31]\n"
"s_add_u32 s10, s10, s11\n"
"buffer_store_dwordx4 v[60:63], v1, s[36:39], s31 offen offset:0 sc1\n"
"v_mfma_f32_16x16x32_bf16 v[28:31], v[116:119], a[48:51], v[28:31]\n"
"s_lshr_b32 s11, s5, 1\n"
"buffer_store_dwordx4 v[64:67], v1, s[36:39], s31 offen offset:1024 sc1\n"
"v_mfma_f32_16x16x32_bf16 v[32:35], v[112:115], a[36:39], v[32:35]\n"
"s_lshl_b32 s11, s11, 5\n"
"buffer_store_dword v68, v8, s[36:39], s31 offen offset:2048 sc1\n"
"v_mfma_f32_16x16x32_bf16 v[32:35], v[116:119], a[52:55], v[32:35]\n"
"s_add_u32 s10, s10, s11\n"
"buffer_load_dwordx4 v[196:199], v1, s[36:39], s60 offen offset:0 sc1\n"
"v_mfma_f32_16x16x32_bf16 v[36:39], v[112:115], a[40:43], v[36:39]\n"
"buffer_load_dwordx4 v[200:203], v1, s[36:39], s60 offen offset:1024 sc1\n"
"v_mfma_f32_16x16x32_bf16 v[36:39], v[116:119], a[56:59], v[36:39]\n"
"buffer_load_dword v204, v8, s[36:39], s60 offen offset:2048 sc1\n"
"v_mfma_f32_16x16x32_bf16 v[40:43], v[112:115], a[44:47], v[40:43]\n"
"v_mfma_f32_16x16x32_bf16 v[40:43], v[116:119], a[60:63], v[40:43]\n"
"s_waitcnt vmcnt(24)\n"
"v_mfma_f32_16x16x32_bf16 v[28:31], v[120:123], a[64:67], v[28:31]\n"
"v_add_u32 v23, s12, v1\n"
"v_mfma_f32_16x16x32_bf16 v[28:31], v[124:127], a[80:83], v[28:31]\n"
"ds_read_b64 v[112:113], v25 offset:0\n"
"v_mfma_f32_16x16x32_bf16 v[32:35], v[120:123], a[68:71], v[32:35]\n"
"ds_read_b64 v[114:115], v25 offset:3072\n"
"v_mfma_f32_16x16x32_bf16 v[32:35], v[124:127], a[84:87], v[32:35]\n"
"ds_read_b64 v[116:117], v25 offset:6144\n"
"v_mfma_f32_16x16x32_bf16 v[36:39], v[120:123], a[72:75], v[36:39]\n"
"ds_read_b64 v[118:119], v25 offset:9216\n"
"v_mfma_f32_16x16x32_bf16 v[36:39], v[124:127], a[88:91], v[36:39]\n"
"v_mfma_f32_16x16x32_bf16 v[40:43], v[120:123], a[76:79], v[40:43]\n"
"v_mfma_f32_16x16x32_bf16 v[40:43], v[124:127], a[92:95], v[40:43]\n"
"s_waitcnt vmcnt(22)\n"
"v_mfma_f32_16x16x32_bf16 v[28:31], v[128:131], a[96:99], v[28:31]\n"
"s_waitcnt vmcnt(11)\n"
"v_add3_u32 v4, v6, v86, v87\n"
"v_add3_u32 v4, v4, v88, v89\n"
"v_mfma_f32_16x16x32_bf16 v[28:31], v[132:135], a[112:115], v[28:31]\n"
"v_add3_u32 v4, v4, v90, v91\n"
"v_add3_u32 v4, v4, v92, v93\n"
"v_mfma_f32_16x16x32_bf16 v[32:35], v[128:131], a[100:103], v[32:35]\n"
"v_cmp_ne_u32 vcc, v4, v94\n"
"v_mfma_f32_16x16x32_bf16 v[32:35], v[132:135], a[116:119], v[32:35]\n"
"s_cbranch_vccnz LPX4_block_kernel_slow\n"
"LPX4_block_kernel_back:\n"
"v_lshlrev_b32 v106, 16, v86\n"
"v_and_b32 v107, s45, v86\n"
"v_mfma_f32_16x16x32_bf16 v[36:39], v[128:131], a[104:107], v[36:39]\n"
"v_add_f32 v70, v70, v106\n"
"v_add_f32 v71, v71, v107\n"
"v_mfma_f32_16x16x32_bf16 v[36:39], v[132:135], a[120:123], v[36:39]\n"
"v_lshlrev_b32 v106, 16, v87\n"
"v_and_b32 v107, s45, v87\n"
"v_mfma_f32_16x16x32_bf16 v[40:43], v[128:131], a[108:111], v[40:43]\n"
"v_add_f32 v72, v72, v106\n"
"v_add_f32 v73, v73, v107\n"
"v_mfma_f32_16x16x32_bf16 v[40:43], v[132:135], a[124:127], v[40:43]\n"
"s_waitcnt vmcnt(20)\n"
"v_lshlrev_b32 v106, 16, v88\n"
"v_and_b32 v107, s45, v88\n"
"v_mfma_f32_16x16x32_bf16 v[28:31], v[136:139], a[128:131], v[28:31]\n"
"v_add_f32 v74, v74, v106\n"
"v_add_f32 v75, v75, v107\n"
"v_mfma_f32_16x16x32_bf16 v[28:31], v[140:143], a[144:147], v[28:31]\n"
"v_lshlrev_b32 v106, 16, v89\n"
"v_and_b32 v107, s45, v89\n"
"v_mfma_f32_16x16x32_bf16 v[32:35], v[136:139], a[132:135], v[32:35]\n"
"v_add_f32 v76, v76, v106\n"
"v_add_f32 v77, v77, v107\n"
"v_mfma_f32_16x16x32_bf16 v[32:35], v[140:143], a[148:151], v[32:35]\n"
"v_lshlrev_b32 v106, 16, v90\n"
"v_and_b32 v107, s45, v90\n"
"v_mfma_f32_16x16x32_bf16 v[36:39], v[136:139], a[136:139], v[36:39]\n"
"v_add_f32 v78, v78, v106\n"
"v_add_f32 v79, v79, v107\n"
"v_mfma_f32_16x16x32_bf16 v[36:39], v[140:143], a[152:155], v[36:39]\n"
"v_lshlrev_b32 v106, 16, v91\n"
"v_and_b32 v107, s45, v91\n"
"v_mfma_f32_16x16x32_bf16 v[40:43], v[136:139], a[140:143], v[40:43]\n"
"v_add_f32 v80, v80, v106\n"
"v_add_f32 v81, v81, v107\n"
"v_mfma_f32_16x16x32_bf16 v[40:43], v[140:143], a[156:159], v[40:43]\n"
"s_waitcnt vmcnt(16)\n"
"v_lshlrev_b32 v106, 16, v92\n"
"v_and_b32 v107, s45, v92\n"
"v_mfma_f32_16x16x32_bf16 v[28:31], v[144:147], a[160:163], v[28:31]\n"
"v_add_f32 v82, v82, v106\n"
"v_add_f32 v83, v83, v107\n"
"v_mfma_f32_16x16x32_bf16 v[28:31], v[148:151], a[176:179], v[28:31]\n"
"v_lshlrev_b32 v106, 16, v93\n"
"v_and_b32 v107, s45, v93\n"
"v_mfma_f32_16x16x32_bf16 v[32:35], v[144:147], a[164:167], v[32:35]\n"
"v_add_f32 v84, v84, v106\n"
"v_add_f32 v85, v85, v107\n"
"v_mfma_f32_16x16x32_bf16 v[32:35], v[148:151], a[180:183], v[32:35]\n"
"v_mfma_f32_16x16x32_bf16 v[36:39], v[144:147], a[168:171], v[36:39]\n"
"v_mfma_f32_16x16x32_bf16 v[36:39], v[148:151], a[184:187], v[36:39]\n"
"v_mfma_f32_16x16x32_bf16 v[40:43], v[144:147], a[172:175], v[40:43]\n"
"v_mfma_f32_16x16x32_bf16 v[40:43], v[148:151], a[188:191], v[40:43]\n"
"s_waitcnt vmcnt(10)\n"
"v_mfma_f32_16x16x32_bf16 v[28:31], v[152:155], a[192:195], v[28:31]\n"
"v_mfma_f32_16x16x32_bf16 v[28:31], v[156:159], a[208:211], v[28:31]\n"
"v_mfma_f32_16x16x32_bf16 v[32:35], v[152:155], a[196:199], v[32:35]\n"
"s_waitcnt vmcnt(0)\n"
"v_add3_u32 v4, v6, v196, v197\n"
"v_add3_u32 v4, v4, v198, v199\n"
"v_mfma_f32_16x16x32_bf16 v[32:35], v[156:159], a[212:215], v[32:35]\n"
"v_add3_u32 v4, v4, v200, v201\n"
"v_add3_u32 v4, v4, v202, v203\n"
"v_mfma_f32_16x16x32_bf16 v[36:39], v[152:155], a[200:203], v[36:39]\n"
"v_cmp_ne_u32 vcc, v4, v204\n"
"v_mfma_f32_16x16x32_bf16 v[36:39], v[156:159], a[216:219], v[36:39]\n"
"s_cbranch_vccnz LPX5_block_kernel_slow\n"
"LPX5_block_kernel_back:\n"
"v_lshlrev_b32 v106, 16, v196\n"
"v_and_b32 v107, s45, v196\n"
"v_mfma_f32_16x16x32_bf16 v[40:43], v[152:155], a[204:207], v[40:43]\n"
"v_add_f32 v70, v70, v106\n"
"v_add_f32 v71, v71, v107\n"
"v_mfma_f32_16x16x32_bf16 v[40:43], v[156:159], a[220:223], v[40:43]\n"
"s_waitcnt vmcnt(8)\n"
"v_lshlrev_b32 v106, 16, v197\n"
"v_and_b32 v107, s45, v197\n"
"v_mfma_f32_16x16x32_bf16 v[28:31], v[160:163], a[224:227], v[28:31]\n"
"v_add_f32 v72, v72, v106\n"
"v_add_f32 v73, v73, v107\n"
"v_mfma_f32_16x16x32_bf16 v[28:31], v[164:167], a[240:243], v[28:31]\n"
"v_lshlrev_b32 v106, 16, v198\n"
"v_and_b32 v107, s45, v198\n"
"v_mfma_f32_16x16x32_bf16 v[32:35], v[160:163], a[228:231], v[32:35]\n"
"v_add_f32 v74, v74, v106\n"
"v_add_f32 v75, v75, v107\n"
"v_mfma_f32_16x16x32_bf16 v[32:35], v[164:167], a[244:247], v[32:35]\n"
"v_lshlrev_b32 v106, 16, v199\n"
"v_and_b32 v107, s45, v199\n"
"v_mfma_f32_16x16x32_bf16 v[36:39], v[160:163], a[232:235], v[36:39]\n"
"buffer_load_dwordx4 v[186:189], v1, s[36:39], s61 offen offset:0 sc1\n"
"v_mfma_f32_16x16x32_bf16 v[36:39], v[164:167], a[248:251], v[36:39]\n"
"buffer_load_dwordx4 v[190:193], v1, s[36:39], s61 offen offset:1024 sc1\n"
"v_mfma_f32_16x16x32_bf16 v[40:43], v[160:163], a[236:239], v[40:43]\n"
"buffer_load_dword v194, v8, s[36:39], s61 offen offset:2048 sc1\n"
"v_mfma_f32_16x16x32_bf16 v[40:43], v[164:167], a[252:255], v[40:43]\n"
"s_waitcnt vmcnt(9)\n"
"v_add_f32 v76, v76, v106\n"
"v_add_f32 v77, v77, v107\n"
"v_mfma_f32_16x16x32_bf16 v[28:31], v[168:171], v[224:227], v[28:31]\n"
"v_lshlrev_b32 v106, 16, v200\n"
"v_and_b32 v107, s45, v200\n"
"v_mfma_f32_16x16x32_bf16 v[44:47], v[172:175], v[240:243], v[28:31]\n"
"v_add_f32 v78, v78, v106\n"
"v_add_f32 v79, v79, v107\n"
"v_mfma_f32_16x16x32_bf16 v[32:35], v[168:171], v[228:231], v[32:35]\n"
"v_lshlrev_b32 v106, 16, v201\n"
"v_and_b32 v107, s45, v201\n"
"v_mfma_f32_16x16x32_bf16 v[48:51], v[172:175], v[244:247], v[32:35]\n"
"v_add_f32 v80, v80, v106\n"
"v_add_f32 v81, v81, v107\n"
"v_mfma_f32_16x16x32_bf16 v[36:39], v[168:171], v[232:235], v[36:39]\n"
"v_lshlrev_b32 v106, 16, v202\n"
"v_and_b32 v107, s45, v202\n"
"v_mfma_f32_16x16x32_bf16 v[52:55], v[172:175], v[248:251], v[36:39]\n"
"v_add_f32 v82, v82, v106\n"
"v_add_f32 v83, v83, v107\n"
"v_mfma_f32_16x16x32_bf16 v[40:43], v[168:171], v[236:239], v[40:43]\n"
"v_lshlrev_b32 v106, 16, v203\n"
"v_and_b32 v107, s45, v203\n"
"v_mfma_f32_16x16x32_bf16 v[56:59], v[172:175], v[252:255], v[40:43]\n"
"v_add_f32 v84, v84, v106\n"
"v_add_f32 v85, v85, v107\n"
"buffer_load_dwordx4 v[176:179], v1, s[36:39], s62 offen offset:0 sc1\n"
"buffer_load_dwordx4 v[180:183], v1, s[36:39], s62 offen offset:1024 sc1\n"
"buffer_load_dword v184, v8, s[36:39], s62 offen offset:2048 sc1\n"
"s_waitcnt vmcnt(3)\n"
"v_add3_u32 v4, v6, v186, v187\n"
"v_add3_u32 v4, v4, v188, v189\n"
"v_add3_u32 v4, v4, v190, v191\n"
"v_add3_u32 v4, v4, v192, v193\n"
"v_cmp_ne_u32 vcc, v4, v194\n"
"s_nop 0\n"
"s_cbranch_vccnz LPX6_block_kernel_slow\n"
"LPX6_block_kernel_back:\n"
"v_lshlrev_b32 v106, 16, v186\n"
"v_and_b32 v107, s45, v186\n"
"v_pk_add_f32 v[70:71], v[70:71], v[106:107]\n"
"v_lshlrev_b32 v106, 16, v187\n"
"v_and_b32 v107, s45, v187\n"
"v_pk_add_f32 v[72:73], v[72:73], v[106:107]\n"
"v_lshlrev_b32 v106, 16, v188\n"
"v_and_b32 v107, s45, v188\n"
"v_pk_add_f32 v[74:75], v[74:75], v[106:107]\n"
"v_lshlrev_b32 v106, 16, v189\n"
"v_and_b32 v107, s45, v189\n"
"v_pk_add_f32 v[76:77], v[76:77], v[106:107]\n"
"v_lshlrev_b32 v106, 16, v190\n"
"v_and_b32 v107, s45, v190\n"
"v_pk_add_f32 v[78:79], v[78:79], v[106:107]\n"
"v_lshlrev_b32 v106, 16, v191\n"
"v_and_b32 v107, s45, v191\n"
"v_pk_add_f32 v[80:81], v[80:81], v[106:107]\n"
"v_lshlrev_b32 v106, 16, v192\n"
"v_and_b32 v107, s45, v192\n"
"v_pk_add_f32 v[82:83], v[82:83], v[106:107]\n"
"v_lshlrev_b32 v106, 16, v193\n"
"v_and_b32 v107, s45, v193\n"
"v_pk_add_f32 v[84:85], v[84:85], v[106:107]\n"
"v_mov_b32 v68, s47\n"
"s_waitcnt lgkmcnt(0)\n"
"v_pk_add_f32 v[70:71], v[70:71], v[44:45]\n"
"v_lshlrev_b32 v28, 16, v112\n"
"v_and_b32 v29, s45, v112\n"
"v_pk_fma_f32 v[28:29], v[70:71], v[68:69], v[28:29] op_sel_hi:[1,0,1]\n"
"v_pk_add_f32 v[72:73], v[72:73], v[46:47]\n"
"v_lshlrev_b32 v30, 16, v113\n"
"v_and_b32 v31, s45, v113\n"
"v_pk_fma_f32 v[30:31], v[72:73], v[68:69], v[30:31] op_sel_hi:[1,0,1]\n"
"v_pk_add_f32 v[74:75], v[74:75], v[48:49]\n"
"v_lshlrev_b32 v32, 16, v114\n"
"v_and_b32 v33, s45, v114\n"
"v_pk_fma_f32 v[32:33], v[74:75], v[68:69], v[32:33] op_sel_hi:[1,0,1]\n"
"v_pk_add_f32 v[76:77], v[76:77], v[50:51]\n"
"v_lshlrev_b32 v34, 16, v115\n"
"v_and_b32 v35, s45, v115\n"
"v_pk_fma_f32 v[34:35], v[76:77], v[68:69], v[34:35] op_sel_hi:[1,0,1]\n"
"v_pk_add_f32 v[78:79], v[78:79], v[52:53]\n"
"v_lshlrev_b32 v36, 16, v116\n"
"v_and_b32 v37, s45, v116\n"
"v_pk_fma_f32 v[36:37], v[78:79], v[68:69], v[36:37] op_sel_hi:[1,0,1]\n"
"v_pk_add_f32 v[80:81], v[80:81], v[54:55]\n"
"v_lshlrev_b32 v38, 16, v117\n"
"v_and_b32 v39, s45, v117\n"
"v_pk_fma_f32 v[38:39], v[80:81], v[68:69], v[38:39] op_sel_hi:[1,0,1]\n"
"v_pk_add_f32 v[82:83], v[82:83], v[56:57]\n"
"v_lshlrev_b32 v40, 16, v118\n"
"v_and_b32 v41, s45, v118\n"
"v_pk_fma_f32 v[40:41], v[82:83], v[68:69], v[40:41] op_sel_hi:[1,0,1]\n"
"v_pk_add_f32 v[84:85], v[84:85], v[58:59]\n"
"v_lshlrev_b32 v42, 16, v119\n"
"v_and_b32 v43, s45, v119\n"
"v_pk_fma_f32 v[42:43], v[84:85], v[68:69], v[42:43] op_sel_hi:[1,0,1]\n"
"s_waitcnt vmcnt(0)\n"
"v_add3_u32 v4, v6, v176, v177\n"
"v_add3_u32 v4, v4, v178, v179\n"
"v_add3_u32 v4, v4, v180, v181\n"
"v_add3_u32 v4, v4, v182, v183\n"
"v_cmp_ne_u32 vcc, v4, v184\n"
"s_nop 0\n"
"s_cbranch_vccnz LPX7_block_kernel_slow\n"
"LPX7_block_kernel_back:\n"
"v_lshlrev_b32 v106, 16, v176\n"
"v_and_b32 v107, s45, v176\n"
"v_pk_fma_f32 v[28:29], v[106:107], v[68:69], v[28:29] op_sel_hi:[1,0,1]\n"
"v_cvt_pk_bf16_f32 v60, v28, v29\n"
"v_lshlrev_b32 v106, 16, v177\n"
"v_and_b32 v107, s45, v177\n"
"v_pk_fma_f32 v[30:31], v[106:107], v[68:69], v[30:31] op_sel_hi:[1,0,1]\n"
"v_cvt_pk_bf16_f32 v61, v30, v31\n"
"ds_write_b64 v19, v[60:61] offset:0\n"
"v_lshlrev_b32 v106, 16, v178\n"
"v_and_b32 v107, s45, v178\n"
"v_pk_fma_f32 v[32:33], v[106:107], v[68:69], v[32:33] op_sel_hi:[1,0,1]\n"
"v_cvt_pk_bf16_f32 v62, v32, v33\n"
"v_lshlrev_b32 v106, 16, v179\n"
"v_and_b32 v107, s45, v179\n"
"v_pk_fma_f32 v[34:35], v[106:107], v[68:69], v[34:35] op_sel_hi:[1,0,1]\n"
"v_cvt_pk_bf16_f32 v63, v34, v35\n"
"ds_write_b64 v19, v[62:63] offset:2048\n"
"v_lshlrev_b32 v106, 16, v180\n"
"v_and_b32 v107, s45, v180\n"
"v_pk_fma_f32 v[36:37], v[106:107], v[68:69], v[36:37] op_sel_hi:[1,0,1]\n"
"v_cvt_pk_bf16_f32 v64, v36, v37\n"
"v_lshlrev_b32 v106, 16, v181\n"
"v_and_b32 v107, s45, v181\n"
"v_pk_fma_f32 v[38:39], v[106:107], v[68:69], v[38:39] op_sel_hi:[1,0,1]\n"
"v_cvt_pk_bf16_f32 v65, v38, v39\n"
"ds_write_b64 v19, v[64:65] offset:4096\n"
"v_lshlrev_b32 v106, 16, v182\n"
"v_and_b32 v107, s45, v182\n"
"v_pk_fma_f32 v[40:41], v[106:107], v[68:69], v[40:41] op_sel_hi:[1,0,1]\n"
"v_cvt_pk_bf16_f32 v66, v40, v41\n"
"v_lshlrev_b32 v106, 16, v183\n"
"v_and_b32 v107, s45, v183\n"
"v_pk_fma_f32 v[42:43], v[106:107], v[68:69], v[42:43] op_sel_hi:[1,0,1]\n"
"v_cvt_pk_bf16_f32 v67, v42, v43\n"
"ds_write_b64 v19, v[66:67] offset:6144\n"
"s_waitcnt lgkmcnt(0)\n"
"s_barrier\n"
"ds_read_b128 v[28:31], v23 offset:0\n"
"ds_read_b128 v[32:35], v23 offset:1024\n"
"ds_read_b128 v[36:39], v23 offset:2048\n"
"s_waitcnt lgkmcnt(2)\n"
"buffer_store_dwordx4 v[28:31], v3, s[40:43], s8 offen sc1\n"
"s_waitcnt lgkmcnt(1)\n"
"buffer_store_dwordx4 v[32:35], v3, s[40:43], s9 offen sc1\n"
"s_cmp_lg_u32 s6, 3\n"
"s_cbranch_scc1 LOX_block_kernel\n"
"s_waitcnt lgkmcnt(0)\n"
"buffer_store_dwordx4 v[36:39], v27, s[40:43], s10 offen offset:1024 sc1\n"
"LOX_block_kernel:\n"
"s_branch LEND_block_kernel\n"
"LFA_block_kernel_slow:\n"
"s_mov_b32 s46, 262144\n"
"LFA_block_kernel_again:\n"
"buffer_load_dwordx2 v[10:11], v44, s[36:39], 0 offen sc1\n"
"s_waitcnt vmcnt(0)\n"
"v_cmp_ne_u64 vcc, v[10:11], v[6:7]\n"
"s_sub_u32 s46, s46, 1\n"
"s_cbranch_scc1 LFA_block_kernel_back\n"
"s_cbranch_vccnz LFA_block_kernel_again\n"
"s_branch LFA_block_kernel_back\n"
"LFB_block_kernel_slow:\n"
"s_mov_b32 s46, 262144\n"
"LFB_block_kernel_again:\n"
"buffer_load_dwordx2 v[10:11], v9, s[36:39], 0 offen sc1\n"
"s_waitcnt vmcnt(0)\n"
"v_cmp_ne_u64 vcc, v[10:11], v[6:7]\n"
"s_sub_u32 s46, s46, 1\n"
"s_cbranch_scc1 LFB_block_kernel_back\n"
"s_cbranch_vccnz LFB_block_kernel_again\n"
"s_branch LFB_block_kernel_back\n"
"LOG0_block_kernel_slow:\n"
"s_mov_b32 s46, 262144\n"
"LOG0_block_kernel_again:\n"
"buffer_load_dwordx4 v[40:43], v1, s[36:39], s58 offen offset:0 sc1\n"
"buffer_load_dwordx4 v[44:47], v1, s[36:39], s58 offen offset:1024 sc1\n"
"s_waitcnt vmcnt(0)\n"
"v_max3_u32 v4, v40, v41, v42\n"
"v_max3_u32 v4, v4, v43, v44\n"
"v_max3_u32 v4, v4, v45, v46\n"
"v_max_u32 v4, v4, v47\n"
"v_cmp_eq_u32 vcc, -1, v4\n"
"s_sub_u32 s46, s46, 1\n"
"s_cbranch_scc1 LOG0_block_kernel_back\n"
"s_cbranch_vccnz LOG0_block_kernel_again\n"
"s_branch LOG0_block_kernel_back\n"
"LOG1_block_kernel_slow:\n"
"s_mov_b32 s46, 262144\n"
"LOG1_block_kernel_again:\n"
"buffer_load_dwordx4 v[48:51], v1, s[36:39], s58 offen offset:2048 sc1\n"
"buffer_load_dwordx4 v[52:55], v1, s[36:39], s58 offen offset:3072 sc1\n"
"s_waitcnt vmcnt(0)\n"
"v_max3_u32 v4, v48, v49, v50\n"
"v_max3_u32 v4, v4, v51, v52\n"
"v_max3_u32 v4, v4, v53, v54\n"
"v_max_u32 v4, v4, v55\n"
"v_cmp_eq_u32 vcc, -1, v4\n"
"s_sub_u32 s46, s46, 1\n"
"s_cbranch_scc1 LOG1_block_kernel_back\n"
"s_cbranch_vccnz LOG1_block_kernel_again\n"
"s_branch LOG1_block_kernel_back\n"
"LOG2_block_kernel_slow:\n"
"s_mov_b32 s46, 262144\n"
"LOG2_block_kernel_again:\n"
"buffer_load_dwordx4 v[56:59], v1, s[36:39], s59 offen offset:0 sc1\n"
"buffer_load_dwordx4 v[60:63], v1, s[36:39], s59 offen offset:1024 sc1\n"
"s_waitcnt vmcnt(0)\n"
"v_max3_u32 v4, v56, v57, v58\n"
"v_max3_u32 v4, v4, v59, v60\n"
"v_max3_u32 v4, v4, v61, v62\n"
"v_max_u32 v4, v4, v63\n"
"v_cmp_eq_u32 vcc, -1, v4\n"
"s_sub_u32 s46, s46, 1\n"
"s_cbranch_scc1 LOG2_block_kernel_back\n"
"s_cbranch_vccnz LOG2_block_kernel_again\n"
"s_branch LOG2_block_kernel_back\n"
"LOG3_block_kernel_slow:\n"
"s_mov_b32 s46, 262144\n"
"LOG3_block_kernel_again:\n"
"buffer_load_dwordx4 v[64:67], v1, s[36:39], s59 offen offset:2048 sc1\n"
"buffer_load_dwordx4 v[68:71], v1, s[36:39], s59 offen offset:3072 sc1\n"
"s_waitcnt vmcnt(0)\n"
"v_max3_u32 v4, v64, v65, v66\n"
"v_max3_u32 v4, v4, v67, v68\n"
"v_max3_u32 v4, v4, v69, v70\n"
"v_max_u32 v4, v4, v71\n"
"v_cmp_eq_u32 vcc, -1, v4\n"
"s_sub_u32 s46, s46, 1\n"
"s_cbranch_scc1 LOG3_block_kernel_back\n"
"s_cbranch_vccnz LOG3_block_kernel_again\n"
"s_branch LOG3_block_kernel_back\n"
"LOG4_block_kernel_slow:\n"
"s_mov_b32 s46, 262144\n"
"LOG4_block_kernel_again:\n"
"buffer_load_dwordx4 v[72:75], v1, s[36:39], s60 offen offset:0 sc1\n"
"buffer_load_dwordx4 v[76:79], v1, s[36:39], s60 offen offset:1024 sc1\n"
"s_waitcnt vmcnt(0)\n"
"v_max3_u32 v4, v72, v73, v74\n"
"v_max3_u32 v4, v4, v75, v76\n"
"v_max3_u32 v4, v4, v77, v78\n"
"v_max_u32 v4, v4, v79\n"
"v_cmp_eq_u32 vcc, -1, v4\n"
"s_sub_u32 s46, s46, 1\n"
"s_cbranch_scc1 LOG4_block_kernel_back\n"
"s_cbranch_vccnz LOG4_block_kernel_again\n"
"s_branch LOG4_block_kernel_back\n"
"LOG5_block_kernel_slow:\n"
"s_mov_b32 s46, 262144\n"
"LOG5_block_kernel_again:\n"
"buffer_load_dwordx4 v[80:83], v1, s[36:39], s60 offen offset:2048 sc1\n"
"buffer_load_dwordx4 v[84:87], v1, s[36:39], s60 offen offset:3072 sc1\n"
"s_waitcnt vmcnt(0)\n"
"v_max3_u32 v4, v80, v81, v82\n"
"v_max3_u32 v4, v4, v83, v84\n"
"v_max3_u32 v4, v4, v85, v86\n"
"v_max_u32 v4, v4, v87\n"
"v_cmp_eq_u32 vcc, -1, v4\n"
"s_sub_u32 s46, s46, 1\n"
"s_cbranch_scc1 LOG5_block_kernel_back\n"
"s_cbranch_vccnz LOG5_block_kernel_again\n"
"s_branch LOG5_block_kernel_back\n"
"LOG6_block_kernel_slow:\n"
"s_mov_b32 s46, 262144\n"
"LOG6_block_kernel_again:\n"
"buffer_load_dwordx4 v[88:91], v1, s[36:39], s61 offen offset:0 sc1\n"
"buffer_load_dwordx4 v[92:95], v1, s[36:39], s61 offen offset:1024 sc1\n"
"s_waitcnt vmcnt(0)\n"
"v_max3_u32 v4, v88, v89, v90\n"
"v_max3_u32 v4, v4, v91, v92\n"
"v_max3_u32 v4, v4, v93, v94\n"
"v_max_u32 v4, v4, v95\n"
"v_cmp_eq_u32 vcc, -1, v4\n"
"s_sub_u32 s46, s46, 1\n"
"s_cbranch_scc1 LOG6_block_kernel_back\n"
"s_cbranch_vccnz LOG6_block_kernel_again\n"
"s_branch LOG6_block_kernel_back\n"
"LOG7_block_kernel_slow:\n"
"s_mov_b32 s46, 262144\n"
"LOG7_block_kernel_again:\n"
"buffer_load_dwordx4 v[96:99], v1, s[36:39], s61 offen offset:2048 sc1\n"
"buffer_load_dwordx4 v[100:103], v1, s[36:39], s61 offen offset:3072 sc1\n"
"s_waitcnt vmcnt(0)\n"
"v_max3_u32 v4, v96, v97, v98\n"
"v_max3_u32 v4, v4, v99, v100\n"
"v_max3_u32 v4, v4, v101, v102\n"
"v_max_u32 v4, v4, v103\n"
"v_cmp_eq_u32 vcc, -1, v4\n"
"s_sub_u32 s46, s46, 1\n"
"s_cbranch_scc1 LOG7_block_kernel_back\n"
"s_cbranch_vccnz LOG7_block_kernel_again\n"
"s_branch LOG7_block_kernel_back\n"
"LOP8_block_kernel_slow:\n"
"s_mov_b32 s46, 262144\n"
"LOP8_block_kernel_again:\n"
"buffer_load_dword v104, v26, s[36:39], s62 offen offset:0 sc1\n"
"buffer_load_dword v105, v26, s[36:39], s62 offen offset:256 sc1\n"
"buffer_load_dword v106, v26, s[36:39], s62 offen offset:512 sc1\n"
"buffer_load_dword v107, v26, s[36:39], s62 offen offset:768 sc1\n"
"buffer_load_dword v108, v27, s[36:39], s62 offen offset:0 sc1\n"
"buffer_load_dword v109, v27, s[36:39], s62 offen offset:256 sc1\n"
"buffer_load_dword v110, v27, s[36:39], s62 offen offset:512 sc1\n"
"buffer_load_dword v111, v27, s[36:39], s62 offen offset:768 sc1\n"
"s_waitcnt vmcnt(0)\n"
"v_max3_u32 v4, v104, v105, v106\n"
"v_max3_u32 v4, v4, v107, v108\n"
"v_max3_u32 v4, v4, v109, v110\n"
"v_max_u32 v4, v4, v111\n"
"v_cmp_eq_u32 vcc, -1, v4\n"
"s_sub_u32 s46, s46, 1\n"
"s_cbranch_scc1 LOP8_block_kernel_back\n"
"s_cbranch_vccnz LOP8_block_kernel_again\n"
"s_branch LOP8_block_kernel_back\n"
"LHS_block_kernel_slow:\n"
"s_mov_b32 s46, 262144\n"
"LHS_block_kernel_again:\n"
"buffer_load_dword v36, v24, s[36:39], s69 offen offset:2048 sc1\n"
"buffer_load_dword v37, v24, s[36:39], s69 offen offset:2304 sc1\n"
"buffer_load_dword v38, v24, s[36:39], s69 offen offset:2560 sc1\n"
"buffer_load_dword v39, v24, s[36:39], s69 offen offset:2816 sc1\n"
"s_waitcnt vmcnt(0)\n"
"v_max3_u32 v4, v36, v37, v38\n"
"v_max_u32 v4, v4, v39\n"
"v_cmp_eq_u32 vcc, -1, v4\n"
"s_sub_u32 s46, s46, 1\n"
"s_cbranch_scc1 LHS_block_kernel_back\n"
"s_cbranch_vccnz LHS_block_kernel_again\n"
"s_branch LHS_block_kernel_back\n"
"LHG0_block_kernel_slow:\n"
"s_mov_b32 s46, 262144\n"
"LHG0_block_kernel_again:\n"
"buffer_load_dwordx4 v[40:43], v1, s[36:39], s65 offen offset:0 sc1\n"
"buffer_load_dwordx4 v[44:47], v1, s[36:39], s65 offen offset:1024 sc1\n"
"s_waitcnt vmcnt(0)\n"
"v_max3_u32 v4, v40, v41, v42\n"
"v_max3_u32 v4, v4, v43, v44\n"
"v_max3_u32 v4, v4, v45, v46\n"
"v_max_u32 v4, v4, v47\n"
"v_cmp_eq_u32 vcc, -1, v4\n"
"s_sub_u32 s46, s46, 1\n"
"s_cbranch_scc1 LHG0_block_kernel_back\n"
"s_cbranch_vccnz LHG0_block_kernel_again\n"
"s_branch LHG0_block_kernel_back\n"
"LHG1_block_kernel_slow:\n"
"s_mov_b32 s46, 262144\n"
"LHG1_block_kernel_again:\n"
"buffer_load_dwordx4 v[48:51], v1, s[36:39], s65 offen offset:2048 sc1\n"
"buffer_load_dwordx4 v[52:55], v1, s[36:39], s65 offen offset:3072 sc1\n"
"s_waitcnt vmcnt(0)\n"
"v_max3_u32 v4, v48, v49, v50\n"
"v_max3_u32 v4, v4, v51, v52\n"
"v_max3_u32 v4, v4, v53, v54\n"
"v_max_u32 v4, v4, v55\n"
"v_cmp_eq_u32 vcc, -1, v4\n"
"s_sub_u32 s46, s46, 1\n"
"s_cbranch_scc1 LHG1_block_kernel_back\n"
"s_cbranch_vccnz LHG1_block_kernel_again\n"
"s_branch LHG1_block_kernel_back\n"
"LHG2_block_kernel_slow:\n"
"s_mov_b32 s46, 262144\n"
"LHG2_block_kernel_again:\n"
"buffer_load_dwordx4 v[56:59], v1, s[36:39], s66 offen offset:0 sc1\n"
"buffer_load_dwordx4 v[60:63], v1, s[36:39], s66 offen offset:1024 sc1\n"
"s_waitcnt vmcnt(0)\n"
"v_max3_u32 v4, v56, v57, v58\n"
"v_max3_u32 v4, v4, v59, v60\n"
"v_max3_u32 v4, v4, v61, v62\n"
"v_max_u32 v4, v4, v63\n"
"v_cmp_eq_u32 vcc, -1, v4\n"
"s_sub_u32 s46, s46, 1\n"
"s_cbranch_scc1 LHG2_block_kernel_back\n"
"s_cbranch_vccnz LHG2_block_kernel_again\n"
"s_branch LHG2_block_kernel_back\n"
"LHG3_block_kernel_slow:\n"
"s_mov_b32 s46, 262144\n"
"LHG3_block_kernel_again:\n"
"buffer_load_dwordx4 v[64:67], v1, s[36:39], s66 offen offset:2048 sc1\n"
"buffer_load_dwordx4 v[68:71], v1, s[36:39], s66 offen offset:3072 sc1\n"
"s_waitcnt vmcnt(0)\n"
"v_max3_u32 v4, v64, v65, v66\n"
"v_max3_u32 v4, v4, v67, v68\n"
"v_max3_u32 v4, v4, v69, v70\n"
"v_max_u32 v4, v4, v71\n"
"v_cmp_eq_u32 vcc, -1, v4\n"
"s_sub_u32 s46, s46, 1\n"
"s_cbranch_scc1 LHG3_block_kernel_back\n"
"s_cbranch_vccnz LHG3_block_kernel_again\n"
"s_branch LHG3_block_kernel_back\n"
"LHG4_block_kernel_slow:\n"
"s_mov_b32 s46, 262144\n"
"LHG4_block_kernel_again:\n"
"buffer_load_dwordx4 v[72:75], v1, s[36:39], s67 offen offset:0 sc1\n"
"buffer_load_dwordx4 v[76:79], v1, s[36:39], s67 offen offset:1024 sc1\n"
"s_waitcnt vmcnt(0)\n"
"v_max3_u32 v4, v72, v73, v74\n"
"v_max3_u32 v4, v4, v75, v76\n"
"v_max3_u32 v4, v4, v77, v78\n"
"v_max_u32 v4, v4, v79\n"
"v_cmp_eq_u32 vcc, -1, v4\n"
"s_sub_u32 s46, s46, 1\n"
"s_cbranch_scc1 LHG4_block_kernel_back\n"
"s_cbranch_vccnz LHG4_block_kernel_again\n"
"s_branch LHG4_block_kernel_back\n"
"LHG5_block_kernel_slow:\n"
"s_mov_b32 s46, 262144\n"
"LHG5_block_kernel_again:\n"
"buffer_load_dwordx4 v[80:83], v1, s[36:39], s67 offen offset:2048 sc1\n"
"buffer_load_dwordx4 v[84:87], v1, s[36:39], s67 offen offset:3072 sc1\n"
"s_waitcnt vmcnt(0)\n"
"v_max3_u32 v4, v80, v81, v82\n"
"v_max3_u32 v4, v4, v83, v84\n"
"v_max3_u32 v4, v4, v85, v86\n"
"v_max_u32 v4, v4, v87\n"
"v_cmp_eq_u32 vcc, -1, v4\n"
"s_sub_u32 s46, s46, 1\n"
"s_cbranch_scc1 LHG5_block_kernel_back\n"
"s_cbranch_vccnz LHG5_block_kernel_again\n"
"s_branch LHG5_block_kernel_back\n"
"LHG6_block_kernel_slow:\n"
"s_mov_b32 s46, 262144\n"
"LHG6_block_kernel_again:\n"
"buffer_load_dwordx4 v[88:91], v1, s[36:39], s68 offen offset:0 sc1\n"
"buffer_load_dwordx4 v[92:95], v1, s[36:39], s68 offen offset:1024 sc1\n"
"s_waitcnt vmcnt(0)\n"
"v_max3_u32 v4, v88, v89, v90\n"
"v_max3_u32 v4, v4, v91, v92\n"
"v_max3_u32 v4, v4, v93, v94\n"
"v_max_u32 v4, v4, v95\n"
"v_cmp_eq_u32 vcc, -1, v4\n"
"s_sub_u32 s46, s46, 1\n"
"s_cbranch_scc1 LHG6_block_kernel_back\n"
"s_cbranch_vccnz LHG6_block_kernel_again\n"
"s_branch LHG6_block_kernel_back\n"
"LHG7_block_kernel_slow:\n"
"s_mov_b32 s46, 262144\n"
"LHG7_block_kernel_again:\n"
"buffer_load_dwordx4 v[96:99], v1, s[36:39], s68 offen offset:2048 sc1\n"
"buffer_load_dwordx4 v[100:103], v1, s[36:39], s68 offen offset:3072 sc1\n"
"s_waitcnt vmcnt(0)\n"
"v_max3_u32 v4, v96, v97, v98\n"
"v_max3_u32 v4, v4, v99, v100\n"
"v_max3_u32 v4, v4, v101, v102\n"
"v_max_u32 v4, v4, v103\n"
"v_cmp_eq_u32 vcc, -1, v4\n"
"s_sub_u32 s46, s46, 1\n"
"s_cbranch_scc1 LHG7_block_kernel_back\n"
"s_cbranch_vccnz LHG7_block_kernel_again\n"
"s_branch LHG7_block_kernel_back\n"
"LHG8_block_kernel_slow:\n"
"s_mov_b32 s46, 262144\n"
"LHG8_block_kernel_again:\n"
"buffer_load_dwordx2 v[104:105], v2, s[36:39], s69 offen offset:0 sc1\n"
"buffer_load_dwordx2 v[106:107], v2, s[36:39], s69 offen offset:512 sc1\n"
"buffer_load_dwordx2 v[108:109], v2, s[36:39], s69 offen offset:1024 sc1\n"
"buffer_load_dwordx2 v[110:111], v2, s[36:39], s69 offen offset:1536 sc1\n"
"s_waitcnt vmcnt(0)\n"
"v_max3_u32 v4, v104, v105, v106\n"
"v_max3_u32 v4, v4, v107, v108\n"
"v_max3_u32 v4, v4, v109, v110\n"
"v_max_u32 v4, v4, v111\n"
"v_cmp_eq_u32 vcc, -1, v4\n"
"s_sub_u32 s46, s46, 1\n"
"s_cbranch_scc1 LHG8_block_kernel_back\n"
"s_cbranch_vccnz LHG8_block_kernel_again\n"
"s_branch LHG8_block_kernel_back\n"
"LF2_block_kernel_slow:\n"
"s_mov_b32 s46, 262144\n"
"LF2_block_kernel_again:\n"
"buffer_load_dwordx2 v[10:11], v9, s[36:39], 0 offen sc1\n"
"s_waitcnt vmcnt(0)\n"
"v_cmp_ne_u64 vcc, v[10:11], v[6:7]\n"
"s_sub_u32 s46, s46, 1\n"
"s_cbranch_scc1 LF2_block_kernel_back\n"
"s_cbranch_vccnz LF2_block_kernel_again\n"
"s_branch LF2_block_kernel_back\n"
"LFC_block_kernel_slow:\n"
"s_mov_b32 s46, 262144\n"
"LFC_block_kernel_again:\n"
"buffer_load_dwordx2 v[10:11], v9, s[36:39], 0 offen sc1\n"
"s_waitcnt vmcnt(0)\n"
"v_cmp_ne_u64 vcc, v[10:11], v[6:7]\n"
"s_sub_u32 s46, s46, 1\n"
"s_cbranch_scc1 LFC_block_kernel_back\n"
"s_cbranch_vccnz LFC_block_kernel_again\n"
"s_branch LFC_block_kernel_back\n"
"LPX0_block_kernel_slow:\n"
"s_mov_b32 s46, 262144\n"
"LPX0_block_kernel_again:\n"
"buffer_load_dwordx4 v[86:89], v21, s[36:39], s66 offen sc1\n"
"buffer_load_dwordx4 v[90:93], v21, s[36:39], s67 offen sc1\n"
"buffer_load_dword v94, v22, s[36:39], s66 offen offset:1024 sc1\n"
"buffer_load_dword v95, v22, s[36:39], s67 offen offset:1024 sc1\n"
"s_waitcnt vmcnt(0)\n"
"v_add3_u32 v106, v6, v86, v87\n"
"v_add3_u32 v106, v106, v88, v89\n"
"v_add3_u32 v107, v6, v90, v91\n"
"v_add3_u32 v107, v107, v92, v93\n"
"v_cmp_ne_u64 vcc, v[106:107], v[94:95]\n"
"s_sub_u32 s46, s46, 1\n"
"s_cbranch_scc1 LPX0_block_kernel_back\n"
"s_cbranch_vccnz LPX0_block_kernel_again\n"
"s_branch LPX0_block_kernel_back\n"
"LPX1_block_kernel_slow:\n"
"s_mov_b32 s46, 262144\n"
"LPX1_block_kernel_again:\n"
"buffer_load_dwordx4 v[96:99], v1, s[36:39], s56 offen offset:0 sc1\n"
"buffer_load_dwordx4 v[100:103], v1, s[36:39], s56 offen offset:1024 sc1\n"
"buffer_load_dword v104, v8, s[36:39], s56 offen offset:2048 sc1\n"
"s_waitcnt vmcnt(0)\n"
"v_add3_u32 v4, v6, v96, v97\n"
"v_add3_u32 v4, v4, v98, v99\n"
"v_add3_u32 v4, v4, v100, v101\n"
"v_add3_u32 v4, v4, v102, v103\n"
"v_cmp_ne_u32 vcc, v4, v104\n"
"s_sub_u32 s46, s46, 1\n"
"s_cbranch_scc1 LPX1_block_kernel_back\n"
"s_cbranch_vccnz LPX1_block_kernel_again\n"
"s_branch LPX1_block_kernel_back\n"
"LPX2_block_kernel_slow:\n"
"s_mov_b32 s46, 262144\n"
"LPX2_block_kernel_again:\n"
"buffer_load_dwordx4 v[86:89], v1, s[36:39], s57 offen offset:0 sc1\n"
"buffer_load_dwordx4 v[90:93], v1, s[36:39], s57 offen offset:1024 sc1\n"
"buffer_load_dword v94, v8, s[36:39], s57 offen offset:2048 sc1\n"
"s_waitcnt vmcnt(0)\n"
"v_add3_u32 v4, v6, v86, v87\n"
"v_add3_u32 v4, v4, v88, v89\n"
"v_add3_u32 v4, v4, v90, v91\n"
"v_add3_u32 v4, v4, v92, v93\n"
"v_cmp_ne_u32 vcc, v4, v94\n"
"s_sub_u32 s46, s46, 1\n"
"s_cbranch_scc1 LPX2_block_kernel_back\n"
"s_cbranch_vccnz LPX2_block_kernel_again\n"
"s_branch LPX2_block_kernel_back\n"
"LPX3_block_kernel_slow:\n"
"s_mov_b32 s46, 262144\n"
"LPX3_block_kernel_again:\n"
"buffer_load_dwordx4 v[96:99], v1, s[36:39], s58 offen offset:0 sc1\n"
"buffer_load_dwordx4 v[100:103], v1, s[36:39], s58 offen offset:1024 sc1\n"
"buffer_load_dword v104, v8, s[36:39], s58 offen offset:2048 sc1\n"
"s_waitcnt vmcnt(0)\n"
"v_add3_u32 v4, v6, v96, v97\n"
"v_add3_u32 v4, v4, v98, v99\n"
"v_add3_u32 v4, v4, v100, v101\n"
"v_add3_u32 v4, v4, v102, v103\n"
"v_cmp_ne_u32 vcc, v4, v104\n"
"s_sub_u32 s46, s46, 1\n"
"s_cbranch_scc1 LPX3_block_kernel_back\n"
"s_cbranch_vccnz LPX3_block_kernel_again\n"
"s_branch LPX3_block_kernel_back\n"
"LPX4_block_kernel_slow:\n"
"s_mov_b32 s46, 262144\n"
"LPX4_block_kernel_again:\n"
"buffer_load_dwordx4 v[86:89], v1, s[36:39], s59 offen offset:0 sc1\n"
"buffer_load_dwordx4 v[90:93], v1, s[36:39], s59 offen offset:1024 sc1\n"
"buffer_load_dword v94, v8, s[36:39], s59 offen offset:2048 sc1\n"
"s_waitcnt vmcnt(0)\n"
"v_add3_u32 v4, v6, v86, v87\n"
"v_add3_u32 v4, v4, v88, v89\n"
"v_add3_u32 v4, v4, v90, v91\n"
"v_add3_u32 v4, v4, v92, v93\n"
"v_cmp_ne_u32 vcc, v4, v94\n"
"s_sub_u32 s46, s46, 1\n"
"s_cbranch_scc1 LPX4_block_kernel_back\n"
"s_cbranch_vccnz LPX4_block_kernel_again\n"
"s_branch LPX4_block_kernel_back\n"
"LPX5_block_kernel_slow:\n"
"s_mov_b32 s46, 262144\n"
"LPX5_block_kernel_again:\n"
"buffer_load_dwordx4 v[196:199], v1, s[36:39], s60 offen offset:0 sc1\n"
"buffer_load_dwordx4 v[200:203], v1, s[36:39], s60 offen offset:1024 sc1\n"
"buffer_load_dword v204, v8, s[36:39], s60 offen offset:2048 sc1\n"
"s_waitcnt vmcnt(0)\n"
"v_add3_u32 v4, v6, v196, v197\n"
"v_add3_u32 v4, v4, v198, v199\n"
"v_add3_u32 v4, v4, v200, v201\n"
"v_add3_u32 v4, v4, v202, v203\n"
"v_cmp_ne_u32 vcc, v4, v204\n"
"s_sub_u32 s46, s46, 1\n"
"s_cbranch_scc1 LPX5_block_kernel_back\n"
"s_cbranch_vccnz LPX5_block_kernel_again\n"
"s_branch LPX5_block_kernel_back\n"
"LPX6_block_kernel_slow:\n"
"s_mov_b32 s46, 262144\n"
"LPX6_block_kernel_again:\n"
"buffer_load_dwordx4 v[186:189], v1, s[36:39], s61 offen offset:0 sc1\n"
"buffer_load_dwordx4 v[190:193], v1, s[36:39], s61 offen offset:1024 sc1\n"
"buffer_load_dword v194, v8, s[36:39], s61 offen offset:2048 sc1\n"
"s_waitcnt vmcnt(0)\n"
"v_add3_u32 v4, v6, v186, v187\n"
"v_add3_u32 v4, v4, v188, v189\n"
"v_add3_u32 v4, v4, v190, v191\n"
"v_add3_u32 v4, v4, v192, v193\n"
"v_cmp_ne_u32 vcc, v4, v194\n"
"s_sub_u32 s46, s46, 1\n"
"s_cbranch_scc1 LPX6_block_kernel_back\n"
"s_cbranch_vccnz LPX6_block_kernel_again\n"
"s_branch LPX6_block_kernel_back\n"
"LPX7_block_kernel_slow:\n"
"s_mov_b32 s46, 262144\n"
"LPX7_block_kernel_again:\n"
"buffer_load_dwordx4 v[176:179], v1, s[36:39], s62 offen offset:0 sc1\n"
"buffer_load_dwordx4 v[180:183], v1, s[36:39], s62 offen offset:1024 sc1\n"
"buffer_load_dword v184, v8, s[36:39], s62 offen offset:2048 sc1\n"
"s_waitcnt vmcnt(0)\n"
"v_add3_u32 v4, v6, v176, v177\n"
"v_add3_u32 v4, v4, v178, v179\n"
"v_add3_u32 v4, v4, v180, v181\n"
"v_add3_u32 v4, v4, v182, v183\n"
"v_cmp_ne_u32 vcc, v4, v184\n"
"s_sub_u32 s46, s46, 1\n"
"s_cbranch_scc1 LPX7_block_kernel_back\n"
"s_cbranch_vccnz LPX7_block_kernel_again\n"
"s_branch LPX7_block_kernel_back\n"
"LEND_block_kernel:\n"
  : "+v"(tid) : "s"(__builtin_amdgcn_kernarg_segment_ptr()), "s"(blockIdx.x) : "v1", "v2", "v3", "v4", "v5", "v6", "v7", "v8", "v9", "v10", "v11", "v12", "v13", "v14", "v15", "v16", "v17", "v18", "v19", "v20", "v21", "v22", "v23", "v24", "v25", "v26", "v27", "v28", "v29", "v30", "v31", "v32", "v33", "v34", "v35", "v36", "v37", "v38", "v39", "v40", "v41", "v42", "v43", "v44", "v45", "v46", "v47", "v48", "v49", "v50", "v51", "v52", "v53", "v54", "v55", "v56", "v57", "v58", "v59", "v60", "v61", "v62", "v63", "v64", "v65", "v66", "v67", "v68", "v69", "v70", "v71", "v72", "v73", "v74", "v75", "v76", "v77", "v78", "v79", "v80", "v81", "v82", "v83", "v84", "v85", "v86", "v87", "v88", "v89", "v90", "v91", "v92", "v93", "v94", "v95", "v96", "v97", "v98", "v99", "v100", "v101", "v102", "v103", "v104", "v105", "v106", "v107", "v108", "v109", "v110", "v111", "v112", "v113", "v114", "v115", "v116", "v117", "v118", "v119", "v120", "v121", "v122", "v123", "v124", "v125", "v126", "v127", "v128", "v129", "v130", "v131", "v132", "v133", "v134", "v135", "v136", "v137", "v138", "v139", "v140", "v141", "v142", "v143", "v144", "v145", "v146", "v147", "v148", "v149", "v150", "v151", "v152", "v153", "v154", "v155", "v156", "v157", "v158", "v159", "v160", "v161", "v162", "v163", "v164", "v165", "v166", "v167", "v168", "v169", "v170", "v171", "v172", "v173", "v174", "v175", "v176", "v177", "v178", "v179", "v180", "v181", "v182", "v183", "v184", "v185", "v186", "v187", "v188", "v189", "v190", "v191", "v192", "v193", "v194", "v195", "v196", "v197", "v198", "v199", "v200", "v201", "v202", "v203", "v204", "v205", "v206", "v207", "v208", "v209", "v210", "v211", "v212", "v213", "v214", "v215", "v216", "v217", "v218", "v219", "v220", "v221", "v222", "v223", "v224", "v225", "v226", "v227", "v228", "v229", "v230", "v231", "v232", "v233", "v234", "v235", "v236", "v237", "v238", "v239", "v240", "v241", "v242", "v243", "v244", "v245", "v246", "v247", "v248", "v249", "v250", "v251", "v252", "v253", "v254", "v255", "a0", "a1", "a2", "a3", "a4", "a5", "a6", "a7", "a8", "a9", "a10", "a11", "a12", "a13", "a14", "a15", "a16", "a17", "a18", "a19", "a20", "a21", "a22", "a23", "a24", "a25", "a26", "a27", "a28", "a29", "a30", "a31", "a32", "a33", "a34", "a35", "a36", "a37", "a38", "a39", "a40", "a41", "a42", "a43", "a44", "a45", "a46", "a47", "a48", "a49", "a50", "a51", "a52", "a53", "a54", "a55", "a56", "a57", "a58", "a59", "a60", "a61", "a62", "a63", "a64", "a65", "a66", "a67", "a68", "a69", "a70", "a71", "a72", "a73", "a74", "a75", "a76", "a77", "a78", "a79", "a80", "a81", "a82", "a83", "a84", "a85", "a86", "a87", "a88", "a89", "a90", "a91", "a92", "a93", "a94", "a95", "a96", "a97", "a98", "a99", "a100", "a101", "a102", "a103", "a104", "a105", "a106", "a107", "a108", "a109", "a110", "a111", "a112", "a113", "a114", "a115", "a116", "a117", "a118", "a119", "a120", "a121", "a122", "a123", "a124", "a125", "a126", "a127", "a128", "a129", "a130", "a131", "a132", "a133", "a134", "a135", "a136", "a137", "a138", "a139", "a140", "a141", "a142", "a143", "a144", "a145", "a146", "a147", "a148", "a149", "a150", "a151", "a152", "a153", "a154", "a155", "a156", "a157", "a158", "a159", "a160", "a161", "a162", "a163", "a164", "a165", "a166", "a167", "a168", "a169", "a170", "a171", "a172", "a173", "a174", "a175", "a176", "a177", "a178", "a179", "a180", "a181", "a182", "a183", "a184", "a185", "a186", "a187", "a188", "a189", "a190", "a191", "a192", "a193", "a194", "a195", "a196", "a197", "a198", "a199", "a200", "a201", "a202", "a203", "a204", "a205", "a206", "a207", "a208", "a209", "a210", "a211", "a212", "a213", "a214", "a215", "a216", "a217", "a218", "a219", "a220", "a221", "a222", "a223", "a224", "a225", "a226", "a227", "a228", "a229", "a230", "a231", "a232", "a233", "a234", "a235", "a236", "a237", "a238", "a239", "a240", "a241", "a242", "a243", "a244", "a245", "a246", "a247", "a248", "a249", "a250", "a251", "a252", "a253", "a254", "a255", "s3", "s4", "s5", "s6", "s7", "s8", "s9", "s10", "s11", "s12", "s13", "s14", "s15", "s16", "s17", "s18", "s19", "s20", "s21", "s22", "s23", "s24", "s25", "s26", "s27", "s28", "s29", "s30", "s31", "s32", "s33", "s34", "s35", "s36", "s37", "s38", "s39", "s40", "s41", "s42", "s43", "s44", "s45", "s46", "s47", "s48", "s49", "s50", "s51", "s52", "s53", "s54", "s55", "s56", "s57", "s58", "s59", "s60", "s61", "s62", "s63", "s64", "s65", "s66", "s67", "s68", "s69", "s70", "s71", "s72", "s73", "s74", "s75", "s76", "s77", "s78", "s79", "s80", "s81", "s82", "s83", "s84", "s85", "s86", "s87", "s88", "s89", "s90", "s91", "s92", "s93", "s94", "s95", "s96", "s97", "s98", "s99", "vcc", "scc", "memory");
#endif
}

extern "C" void kernel_launch(void* const* d_in, const int* in_sizes, int n_in, void* d_out, int out_size, void* d_ws, size_t ws_size, hipStream_t stream) {
  (void)in_sizes; (void)n_in; (void)out_size; (void)ws_size;
  Args a;
  a.x = d_in[0]; a.wqkv = d_in[1]; a.wo = d_in[2]; a.w1 = d_in[4]; a.w2 = d_in[5]; a.out = d_out; a.ws = d_ws;
  (void)hipFuncSetAttribute((const void*)block_kernel, hipFuncAttributeMaxDynamicSharedMemorySize, 163840);
  hipLaunchKernelGGL(block_kernel, dim3(256), dim3(256), 163840, stream, a);
}
```
